```python
import torch

def _softmax(x, axis=-1):
    return torch.softmax(x, dim=axis)
def _gelu(x):
    return torch.nn.functional.gelu(x, approximate="tanh")
def _silu(x):
    return torch.nn.functional.silu(x)
def _sigmoid(x):
    return torch.sigmoid(x)
def _layernorm(x, axis=-1, eps=1e-5):
    m = x.mean(dim=axis, keepdim=True)
    v = x.var(dim=axis, keepdim=True, unbiased=False)
    return (x - m) / torch.sqrt(v + eps)
def _scatter_add(tgt, idx, src, axis=0):
    return tgt.clone().index_add_(axis, idx, src)
def _segment_sum(src, seg, n):
    out = torch.zeros((n,) + src.shape[1:], dtype=src.dtype)
    return out.index_add_(0, seg, src)
def _pad(x, pw):
    flat = tuple(v for pair in reversed(pw) for v in pair)
    return torch.nn.functional.pad(x, flat)

kernel_name = 'gather_scatter__edge_featured_mpnn_egc__graph'

def setup_inputs():

    _w = (lambda a: (lambda t: t.to(torch.bfloat16) if t.is_floating_point() else t)(torch.as_tensor(a)))
    N, deg, D, De = 25000, 32, 64, max(0, 1)
    E = N * deg
    return {
        "x": _w((lambda *s: torch.randn(*s, dtype=torch.bfloat16) * 0.2)(N, D)),
        "e_attr": _w((lambda *s: torch.randn(*s, dtype=torch.bfloat16) * 0.2)(E, De)),
        "src": _w((lambda lo, hi, *s: torch.randint(lo, hi, s))(0, N, E).to(torch.int64)),
        "dst": _w((lambda lo, hi, *s: torch.randint(lo, hi, s))(0, N, E).to(torch.int64)),
        "u_w": _w((lambda *s: torch.randn(*s, dtype=torch.bfloat16) * 0.05)(D, D)),
        "v_w": _w((lambda *s: torch.randn(*s, dtype=torch.bfloat16) * 0.05)(D, D)),
        "g_w": _w((lambda *s: torch.randn(*s, dtype=torch.bfloat16) * 0.2)(2 * D + De, D)),
    }

def reference(x, e_attr, src, dst, u_w, v_w, g_w):
    N, D = x.shape
    g_in = torch.concatenate([x[dst], x[src], e_attr], axis=-1)
    eta = _sigmoid(g_in @ g_w)
    hv = x @ v_w
    msg = eta * hv[src]
    agg = _segment_sum(msg, dst, N)
    return _silu(x @ u_w + agg)
```

```cpp
#include <hip/hip_runtime.h>
#include <hip/hip_bf16.h>
#include <stdint.h>

static __device__ __forceinline__ float bf2f(uint16_t v) { return __uint_as_float(((uint32_t)v) << 16); }
static __device__ __forceinline__ uint16_t f2bf(float f) { __hip_bfloat16 h = __float2bfloat16(f); return *(uint16_t*)&h; }

__global__ void node_k(const uint16_t* x, const uint16_t* uw, const uint16_t* vw, const uint16_t* gw,
                       float* xa, float* xb, float* hv, float* agg, int N) {
  int n = blockIdx.x * 4 + (threadIdx.x >> 6); int f = threadIdx.x & 63;
  if (n >= N) return;
  float a = 0, b = 0, h = 0, u = 0;
  for (int k = 0; k < 64; k++) {
    float xv = bf2f(x[n * 64 + k]);
    a += xv * bf2f(gw[k * 64 + f]); b += xv * bf2f(gw[(64 + k) * 64 + f]);
    h += xv * bf2f(vw[k * 64 + f]); u += xv * bf2f(uw[k * 64 + f]);
  }
  xa[n * 64 + f] = a; xb[n * 64 + f] = b; hv[n * 64 + f] = h; agg[n * 64 + f] = u;
}
__global__ void edge_k(const uint16_t* e, const int* src, const int* dst, const uint16_t* gw,
                       const float* xa, const float* xb, const float* hv, float* agg, int E) {
  int i = blockIdx.x * 4 + (threadIdx.x >> 6); int f = threadIdx.x & 63;
  if (i >= E) return;
  int s = src[i], d = dst[i];
  float z = xa[d * 64 + f] + xb[s * 64 + f] + bf2f(e[i]) * bf2f(gw[128 * 64 + f]);
  float eta = 1.f / (1.f + __expf(-z));
  atomicAdd(&agg[d * 64 + f], eta * hv[s * 64 + f]);
}
__global__ void out_k(const float* agg, uint16_t* out, int n) {
  int i = blockIdx.x * 256 + threadIdx.x; if (i >= n) return;
  float v = agg[i]; out[i] = f2bf(v / (1.f + __expf(-v)));
}

extern "C" void kernel_launch(void* const* d_in, const int* in_sizes, int n_in,
                              void* d_out, int out_size, void* d_ws, size_t ws_size,
                              hipStream_t stream) {
  const uint16_t* x = (const uint16_t*)d_in[0]; const uint16_t* e = (const uint16_t*)d_in[1];
  const int* src = (const int*)d_in[2]; const int* dst = (const int*)d_in[3];
  const uint16_t* uw = (const uint16_t*)d_in[4]; const uint16_t* vw = (const uint16_t*)d_in[5]; const uint16_t* gw = (const uint16_t*)d_in[6];
  int N = in_sizes[0] / 64, E = in_sizes[2];
  float* xa = (float*)d_ws; float* xb = xa + N * 64; float* hv = xb + N * 64; float* agg = hv + N * 64;
  node_k<<<(N + 3) / 4, 256, 0, stream>>>(x, uw, vw, gw, xa, xb, hv, agg, N);
  edge_k<<<(E + 3) / 4, 256, 0, stream>>>(e, src, dst, gw, xa, xb, hv, agg, E);
  out_k<<<(N * 64 + 255) / 256, 256, 0, stream>>>(agg, (uint16_t*)d_out, N * 64);
}
```

```cpp
#include <hip/hip_runtime.h>
#include <hip/hip_bf16.h>
#include <stdint.h>

#pragma clang diagnostic ignored "-Wint-to-pointer-cast"
#pragma clang diagnostic ignored "-Wpointer-to-int-cast"
namespace {

typedef float    f32x2  __attribute__((ext_vector_type(2)));
typedef float    f32x4  __attribute__((ext_vector_type(4)));
typedef float    f32x16 __attribute__((ext_vector_type(16)));
typedef short    bf16x8 __attribute__((ext_vector_type(8)));
typedef short    s16x4  __attribute__((ext_vector_type(4)));
typedef uint32_t u32x2  __attribute__((ext_vector_type(2)));
typedef uint32_t u32x4  __attribute__((ext_vector_type(4)));
typedef unsigned long long u64;
typedef __attribute__((address_space(1))) unsigned long long gu64;
#define LDSP(T) __attribute__((address_space(3))) T*
#define DI __device__ __forceinline__
#define RLX_AGENT __ATOMIC_RELAXED, __HIP_MEMORY_SCOPE_AGENT
#define RLX_WG    __ATOMIC_RELAXED, __HIP_MEMORY_SCOPE_WORKGROUP

constexpr int   NCH = 256;
constexpr int   NB  = 128;
constexpr int   NPB = 196;
constexpr int   CH  = 3128;
constexpr int   CHB = 880;
constexpr int   NMAX = NB * NPB;
constexpr int   NTILE = NMAX / 32;
constexpr float NLOG2E = -1.4426950408889634f;
constexpr int   AUX_SC1 = 16;
constexpr unsigned SPIN_CAP = 1u << 18;
static_assert(4 * CHB >= CH + 3 * NB && (CHB * 20) % 16 == 0, "blocks");

constexpr size_t WS_NONCE = 0;
constexpr size_t WS_SEG = 128;
constexpr size_t WS_TBG = WS_SEG + (size_t)NCH * NB * 8;
constexpr size_t WS_TRG = WS_TBG + (size_t)NCH * 16;
constexpr size_t WS_REC = WS_TRG + (size_t)NCH * 8;
constexpr size_t WS_TAB = (WS_REC + (size_t)NCH * CHB * 20 + 1024 + 127) & ~(size_t)127;
constexpr size_t WS_DBG = WS_TAB + (size_t)2 * NMAX * 128;
static_assert(WS_REC % 128 == 0 && WS_TAB % 128 == 0, "ws align");

constexpr int AGG_ROW  = 160;
constexpr int SORT_CAP = 8128;
constexpr int TAKE_MAX = SORT_CAP - 3 * NPB - 48;
constexpr int L_CNT    = 0;
constexpr int L_OFF    = L_CNT + 1024;
constexpr int L_NQ     = L_OFF + 1024;
constexpr int L_MISC   = L_NQ + 1024;
constexpr int L_QN     = L_MISC + 512;
constexpr int L_EA     = L_QN + SORT_CAP;
constexpr int L_XU     = L_EA + 197 * 128;
constexpr int L_AGG    = (L_XU + 196 * 128 + 127) & ~127;
constexpr int L_SORT   = (L_AGG + 197 * AGG_ROW + 127) & ~127;
constexpr int L_TOTAL  = L_SORT + SORT_CAP * 8;
static_assert(L_TOTAL <= 163840, "LDS");
static_assert((L_QN & 15) == 0 && (L_EA & 15) == 0 && (L_XU & 15) == 0 && TAKE_MAX >= 4096, "align");
constexpr int A_HIST = L_SORT;
constexpr int A_BASE = A_HIST + 512;
constexpr int A_ST   = A_BASE + 512;
constexpr int A_W    = (A_ST + CHB * 20 + 127) & ~127;
constexpr int A_IMG  = A_W + 16384;
constexpr int P_W    = A_IMG + 16384;
constexpr int P_X    = L_AGG;
static_assert(P_W + 8192 <= L_TOTAL && P_X + 224 * 128 <= L_SORT, "phase A images");
constexpr uint32_t QN_DUMMY = (196u * 128u) | ((196u * AGG_ROW) << 16);
constexpr uint32_t OFF_DUMMY = 0x7fffff00u;
constexpr int M_MORE = 0, M_NSTEP = 1, M_T4 = 2;
constexpr int M_CW = 3, M_CB = 4, M_CT = 5, M_PUB = 6;
constexpr int M_MX = 8;
constexpr int M_TOT = 16, M_ALL = 32, M_XAM = 48;
constexpr int M_BND = 56;
constexpr int M_CMX = 60;

DI float    bf2f(uint32_t v16) { return __uint_as_float(v16 << 16); }
DI uint32_t cvtpk(float lo, float hi) {
  typedef __bf16 bf16x2 __attribute__((ext_vector_type(2)));
  return __builtin_bit_cast(uint32_t, __builtin_convertvector(f32x2{lo, hi}, bf16x2));
}
DI f32x16   mfma(bf16x8 a, bf16x8 b, f32x16 c) { return __builtin_amdgcn_mfma_f32_32x32x16_bf16(a, b, c, 0, 0, 0); }
DI int      crow(int r, int h) { return (r & 3) + 8 * (r >> 2) + 4 * h; }
DI uint32_t absbits(float v) { return __float_as_uint(v) & 0x7fffffffu; }

template <int CTRL, int RM> DI uint32_t dpp0(uint32_t v) { return (uint32_t)__builtin_amdgcn_update_dpp(0, (int)v, CTRL, RM, 0xf, false); }
DI uint32_t wave_scan_add(uint32_t v) {
  v += dpp0<0x111, 0xf>(v); v += dpp0<0x112, 0xf>(v); v += dpp0<0x114, 0xf>(v); v += dpp0<0x118, 0xf>(v);
  v += dpp0<0x142, 0xa>(v); v += dpp0<0x143, 0xc>(v);
  return v;
}
DI uint32_t wave_max_u32(uint32_t v) {
  v = max(v, dpp0<0x111, 0xf>(v)); v = max(v, dpp0<0x112, 0xf>(v)); v = max(v, dpp0<0x114, 0xf>(v)); v = max(v, dpp0<0x118, 0xf>(v));
  v = max(v, dpp0<0x142, 0xa>(v)); v = max(v, dpp0<0x143, 0xc>(v));
  return v;
}
DI float wave_sum_f32(float s) {
  s += __uint_as_float(dpp0<0x111, 0xf>(__float_as_uint(s))); s += __uint_as_float(dpp0<0x112, 0xf>(__float_as_uint(s)));
  s += __uint_as_float(dpp0<0x114, 0xf>(__float_as_uint(s))); s += __uint_as_float(dpp0<0x118, 0xf>(__float_as_uint(s)));
  s += __uint_as_float(dpp0<0x142, 0xa>(__float_as_uint(s))); s += __uint_as_float(dpp0<0x143, 0xc>(__float_as_uint(s)));
  return s;
}
#define LDS_BARRIER() asm volatile("s_waitcnt lgkmcnt(0)\n\ts_barrier" ::: "memory")
#define WAIT_VM(N) asm volatile("s_waitcnt vmcnt(" #N ")" ::: "memory")

DI uint32_t arrive(uint32_t* word, int lane) {
  asm volatile("s_waitcnt lgkmcnt(0)" ::: "memory");
  uint32_t v = 0;
  if (lane == 0) v = __hip_atomic_fetch_add(word, 1u, RLX_WG);
  return __builtin_amdgcn_readfirstlane(v);
}
DI void await_lds(uint32_t* word, uint32_t want) {
  for (unsigned spins = 0; spins < SPIN_CAP; spins++) {
    if (__builtin_amdgcn_readfirstlane(__hip_atomic_load(word, RLX_WG)) >= want) break;
    __builtin_amdgcn_s_sleep(1);
  }
  asm volatile("" ::: "memory");
}

DI void dma16(uint32_t ldsaddr, uint32_t voff, __amdgpu_buffer_rsrc_t srd) {
  asm volatile("s_mov_b32 m0, %0\n\ts_nop 0\n\tbuffer_load_dwordx4 %1, %2, 0 offen lds" :: "s"(ldsaddr), "v"(voff), "s"(srd) : "memory");
}
DI bf16x8 tr_frag(uint32_t addr, int four_rows) {
  const s16x4 lo = __builtin_amdgcn_ds_read_tr16_b64_v4i16((LDSP(s16x4))addr);
  const s16x4 hi = __builtin_amdgcn_ds_read_tr16_b64_v4i16((LDSP(s16x4))(addr + four_rows));
  return (bf16x8)__builtin_shufflevector(lo, hi, 0, 1, 2, 3, 4, 5, 6, 7);
}

#define TS(i) do { } while (0)
#define TS_DECL do { } while (0)
#define TS_OUT(slot) do { } while (0)
#define TS_PUB(i) do { } while (0)

DI uint32_t shape(float v, uint32_t low) {
  const uint32_t b = __float_as_uint(v);
  const uint32_t t = min(__builtin_elementwise_sub_sat((b & 0x7fffffffu) + 0x8000u, low), 0x7fffffffu);
  return (b & 0x80000000u) | (t & 0xffff0000u) | low;
}
DI void table_half(u32x4* o, const bf16x8* xf, uint32_t troff, int c, float& mb, float& mv, float& s2) {
  f32x16 zb, hv;
#pragma unroll
  for (int i = 0; i < 16; i++) { zb[i] = 0.f; hv[i] = 0.f; }
#pragma unroll
  for (int s = 0; s < 4; s++) {
    zb = mfma(tr_frag(troff + s * 2048 + c * 64, 512), xf[s], zb);
    hv = mfma(tr_frag(troff + 8192 + s * 2048 + c * 64, 512), xf[s], hv);
  }
#pragma unroll
  for (int q = 0; q < 4; q++) {
#pragma unroll
    for (int p = 0; p < 2; p++) {
      const int i = 4 * q + 2 * p;
      const float b0 = zb[i] * NLOG2E, b1 = zb[i + 1] * NLOG2E, v0 = hv[i], v1 = hv[i + 1];
      mb = fmaxf(mb, fmaxf(fabsf(b0), fabsf(b1))); mv = fmaxf(mv, fmaxf(fabsf(v0), fabsf(v1)));
      s2 = fmaf(v0, v0, s2); s2 = fmaf(v1, v1, s2);
      const uint32_t d0 = shape(b0, cvtpk(v0, 0.f));
      const uint32_t w1 = shape(v1, d0 >> 16);
      o[q][2 * p] = d0; o[q][2 * p + 1] = shape(b1, w1 >> 16);
    }
  }
}
DI void table_image(const u32x4* o, uint32_t img, int lane) {
  const int n31 = lane & 31, h = lane >> 5;
#pragma unroll
  for (int q = 0; q < 4; q++) *(LDSP(u32x4))(img + n31 * 128 + (((2 * q + h) ^ (n31 & 7)) * 16)) = o[q];
  asm volatile("s_waitcnt lgkmcnt(0)" ::: "memory");
}
DI void table_store(uint32_t img, int tile, int c, __amdgpu_buffer_rsrc_t tsrd, int lane) {
  u32x4 r[4];
#pragma unroll
  for (int i = 0; i < 4; i++) { const int row = 8 * i + (lane >> 3); r[i] = *(LDSP(const u32x4))(img + row * 128 + (((lane & 7) ^ (row & 7)) * 16)); }
  asm volatile("s_waitcnt lgkmcnt(0)" : "+v"(r[0]), "+v"(r[1]), "+v"(r[2]), "+v"(r[3]) :: "memory");
#pragma unroll
  for (int i = 0; i < 4; i++)
    __builtin_amdgcn_raw_buffer_store_b128(r[i], tsrd, (c * NMAX + tile * 32 + 8 * i + (lane >> 3)) * 128 + (lane & 7) * 16, 0, AUX_SC1);
}

DI f32x2 exp2x2(f32x2 v) { f32x2 r = {__builtin_amdgcn_exp2f(v[0]), __builtin_amdgcn_exp2f(v[1])}; return r; }
DI f32x2 rcpx2(f32x2 v) { f32x2 r = {__builtin_amdgcn_rcpf(v[0]), __builtin_amdgcn_rcpf(v[1])}; return r; }
DI f32x2 fma2(f32x2 a, f32x2 b, f32x2 c) { return __builtin_elementwise_fma(a, b, c); }
template <int MUL> DI uint32_t madlo_i(uint32_t v, uint32_t add) {
  uint32_t r; asm("v_mad_u32_u16 %0, %1, %3, %2" : "=v"(r) : "v"(v), "v"(add), "i"(MUL)); return r;
}
template <int MUL> DI uint32_t madhi_i(uint32_t v, uint32_t add) {
  uint32_t r; asm("v_mad_u32_u16 %0, %1, %3, %2 op_sel:[1,0,0,0]" : "=v"(r) : "v"(v), "v"(add), "i"(MUL)); return r;
}
DI void lds_add_u32(uint32_t addr, float v) { __hip_atomic_fetch_add((LDSP(uint32_t))addr, __float_as_uint(v), RLX_WG); }
DI void lds_add_f32(uint32_t addr, float v) { __hip_atomic_fetch_add((LDSP(float))addr, v, RLX_WG); }

struct Lane {
  uint32_t rec;
  uint32_t qn;
  uint32_t c16;
  uint32_t ea, agg;
  f32x2 c01, c23;
  f32x2 magic;
};
struct Quad {
  u32x4 d[4];
  u32x4 r0, r1;
  uint32_t q;
};
template <int S> DI void fetch(Quad& Q, const Lane& L, __amdgpu_buffer_rsrc_t srd) {
  Q.r0 = *(LDSP(const u32x4))(L.rec + S * 32);
  Q.r1 = *(LDSP(const u32x4))(L.rec + S * 32 + 16);
  Q.q  = *(LDSP(const uint32_t))(L.qn + S * 4);
  Q.d[0] = __builtin_amdgcn_raw_buffer_load_b128(srd, Q.r0[0] + L.c16, 0, AUX_SC1);
  Q.d[1] = __builtin_amdgcn_raw_buffer_load_b128(srd, Q.r0[2] + L.c16, 0, AUX_SC1);
  Q.d[2] = __builtin_amdgcn_raw_buffer_load_b128(srd, Q.r1[0] + L.c16, 0, AUX_SC1);
  Q.d[3] = __builtin_amdgcn_raw_buffer_load_b128(srd, Q.r1[2] + L.c16, 0, AUX_SC1);
  asm volatile("" ::: "memory");
}
DI f32x2 fma_e(u32x2 rec, f32x2 c, u32x2 xb) {
  f32x2 r; asm("v_pk_fma_f32 %0, %1, %2, %3 op_sel:[1,0,0] op_sel_hi:[1,1,1]" : "=v"(r) : "v"(rec), "v"(c), "v"(xb)); return r;
}
DI f32x2 vpair(u32x2 d) {
  f32x2 r; asm("v_lshlrev_b64 %0, 16, %1" : "=v"(r) : "v"(d)); return r;
}
template <int MODE> DI void gate(const Quad& Q, const Lane& L) {
  const f32x4 ea = *(LDSP(const f32x4))(madlo_i<1>(Q.q, L.ea));
  const uint32_t ga = madhi_i<1>(Q.q, L.agg);
  const u32x2 rec[4] = {u32x2{Q.r0[0], Q.r0[1]}, u32x2{Q.r0[2], Q.r0[3]}, u32x2{Q.r1[0], Q.r1[1]}, u32x2{Q.r1[2], Q.r1[3]}};
  const f32x2 one = {1.f, 1.f};
#pragma unroll
  for (int p = 0; p < 2; p++) {
    const f32x2 c = p ? L.c23 : L.c01, eap = {ea[2 * p], ea[2 * p + 1]};
    f32x2 a[4], hv[4];
#pragma unroll
    for (int k = 0; k < 4; k++) {
      const u32x2 dd = {Q.d[k][2 * p], Q.d[k][2 * p + 1]};
      hv[k] = vpair(dd);
      const f32x2 y = fma_e(rec[k], c, dd);
      a[k] = MODE == 2 ? one + exp2x2(y + eap) : fma2(eap, exp2x2(y), one);
    }
    if (MODE == 0) {
      const f32x2 d01 = a[0] * a[1], n01 = fma2(hv[1], a[0], hv[0] * a[1]);
      const f32x2 d23 = a[2] * a[3], n23 = fma2(hv[3], a[2], hv[2] * a[3]);
      const f32x2 m = fma2(fma2(n23, d01, n01 * d23), rcpx2(d01 * d23), L.magic);
      lds_add_u32(ga + 64 * p, m[0]); lds_add_u32(ga + 64 * p + 32, m[1]);
    } else {
      const f32x2 m = (hv[0] * rcpx2(a[0]) + hv[1] * rcpx2(a[1])) + (hv[2] * rcpx2(a[2]) + hv[3] * rcpx2(a[3]));
      lds_add_f32(ga + 64 * p, m[0]); lds_add_f32(ga + 64 * p + 32, m[1]);
    }
  }
}
template <int MODE> DI void edge_loop(Lane& L, int n, __amdgpu_buffer_rsrc_t srd) {
  if (n <= 0) return;
  Quad A, B;
  fetch<0>(A, L, srd);
  while (true) {
    fetch<1>(B, L, srd);
    gate<MODE>(A, L);
    if (--n == 0) break;
    fetch<2>(A, L, srd);
    gate<MODE>(B, L);
    if (--n == 0) break;
    L.rec += 64; L.qn += 8;
  }
}

struct Recs { u32x4 rc[6]; uint32_t lc[6]; };
DI void load_recs(Recs& R, const uint32_t* sg, const char* ws, int g, int j, int done) {
  const __amdgpu_buffer_rsrc_t rsrd = __builtin_amdgcn_make_buffer_rsrc((void*)(ws + WS_REC), (short)0, NCH * CHB * 20, 0x00020000);
#pragma unroll
  for (int p = 0; p < 2; p++) {
#pragma unroll
    for (int r = 0; r < 3; r++) {
      uint32_t off = (uint32_t)((g + 96 * r) * (CHB * 20) + ((int)(sg[r] & 0xffff) + (done >> 2) + 8 * p + j) * 20);
      if ((int)(sg[r] >> 16) - done <= 32 * p + 4 * j) off = 0x40000000u;
      asm volatile("buffer_load_dwordx4 %0, %1, %2, 0 offen sc1" : "=&v"(R.rc[2 * r + p]) : "v"(off), "s"(rsrd) : "memory");
      asm volatile("buffer_load_dword %0, %1, %2, 0 offen offset:16 sc1" : "=&v"(R.lc[2 * r + p]) : "v"(off), "s"(rsrd) : "memory");
    }
  }
}
#define WAIT_RECS(R) asm volatile("s_waitcnt vmcnt(0)" : "+v"(R.rc[0]), "+v"(R.rc[1]), "+v"(R.rc[2]), "+v"(R.rc[3]), "+v"(R.rc[4]), "+v"(R.rc[5]), \
                                  "+v"(R.lc[0]), "+v"(R.lc[1]), "+v"(R.lc[2]), "+v"(R.lc[3]), "+v"(R.lc[4]), "+v"(R.lc[5]) :: "memory")

__global__ __launch_bounds__(1024) void k_mpnn(const int* __restrict__ src, const int* __restrict__ dst,
                                               const uint16_t* __restrict__ ea, const uint16_t* __restrict__ x,
                                               const uint16_t* __restrict__ uw, const uint16_t* __restrict__ vw,
                                               const uint16_t* __restrict__ gw, char* __restrict__ ws,
                                               uint16_t* __restrict__ out, int N, int E) {
  extern __shared__ __attribute__((aligned(128))) char smem[];
  const uint32_t lb = (uint32_t)(uintptr_t)(LDSP(char))smem;
  uint32_t* misc = (uint32_t*)(smem + L_MISC);
  const int t = threadIdx.x, lane = t & 63;
  const int w = blockIdx.x;
  const int half = (blockIdx.x & 7) >> 2, b = (blockIdx.x & 3) * 32 + (blockIdx.x >> 3);
  const int wv = __builtin_amdgcn_readfirstlane(t >> 6);
  const int r31 = lane & 31, h = lane >> 5;
  TS_DECL; TS(0);

  const int e0 = w * CH;
  const int n  = min(CH, E - e0);
  const int cnt = min(max(n - 4 * t, 0), 4);
  u32x2 xn; u32x4 d4, s4; u32x2 e2;
  {
    const __amdgpu_buffer_rsrc_t dsrd = __builtin_amdgcn_make_buffer_rsrc((void*)dst, (short)0, E * 4, 0x00020000);
    const __amdgpu_buffer_rsrc_t ssrd = __builtin_amdgcn_make_buffer_rsrc((void*)src, (short)0, E * 4, 0x00020000);
    const __amdgpu_buffer_rsrc_t esrd = __builtin_amdgcn_make_buffer_rsrc((void*)ea, (short)0, (E * 2 + 3) & ~3, 0x00020000);
    const uint32_t eo = cnt > 0 ? (uint32_t)(e0 + 4 * t) : 0x20000000u;
    asm volatile("buffer_load_dwordx4 %0, %1, %2, 0 offen" : "=&v"(d4) : "v"(eo * 4), "s"(dsrd) : "memory");
    asm volatile("buffer_load_dwordx4 %0, %1, %2, 0 offen" : "=&v"(s4) : "v"(eo * 4), "s"(ssrd) : "memory");
    asm volatile("buffer_load_dwordx2 %0, %1, %2, 0 offen" : "=&v"(e2) : "v"(eo * 2), "s"(esrd) : "memory");
    asm volatile("global_load_dwordx2 %0, %1, %2 sc1" : "=&v"(xn) : "v"(0), "s"(ws + WS_NONCE) : "memory");
  }
  uint32_t* hist = (uint32_t*)(smem + A_HIST);
  uint32_t* base = (uint32_t*)(smem + A_BASE);
  if (t < 128) hist[t] = 0;
  if (t < 512) *(uint32_t*)(smem + (t < 256 ? L_CNT + t * 4 : L_NQ + (t - 256) * 4)) = 0;
  if (t >= 768 && t < 896) misc[t - 768] = 0;
  if (t >= 896 && t < 928) *(float*)(smem + L_EA + 196 * 128 + (t - 896) * 4) = 0.f;
  LDS_BARRIER();
  asm volatile("s_waitcnt vmcnt(0)" : "+v"(xn), "+v"(d4), "+v"(s4), "+v"(e2) :: "memory");
  const uint32_t tag32 = xn[0] + 1u;
  const u64 tag40 = ((((u64)xn[1] << 32) | xn[0]) + 1ull) & 0xffffffffffull;
  const uint32_t ev[4] = {e2[0] & 0xffffu, e2[0] >> 16, e2[1] & 0xffffu, e2[1] >> 16};
  uint32_t bk[4], rk0[4];
#pragma unroll
  for (int k = 0; k < 4; k++) {
    bk[k] = (d4[k] * 21400u) >> 22;
    rk0[k] = 0;
    if (k < cnt) rk0[k] = __hip_atomic_fetch_add(&hist[bk[k]], 1u, RLX_WG);
  }
  u32x4 wc; u32x2 cw;
  const __amdgpu_buffer_rsrc_t xsrd = __builtin_amdgcn_make_buffer_rsrc((void*)x, (short)0, N * 128, 0x00020000);
  const int tile = wv >= 13 ? w + 256 * (wv - 13) : (wv == 12 ? w + 768 : NTILE);
  const uint32_t img = lb + A_IMG + (wv & 3) * 4096;
  {
    const uint16_t* wp = t < 512 ? gw + 64 * 64 + t * 8 : vw + (t - 512) * 8;
    asm volatile("global_load_dwordx4 %0, %1, off" : "=&v"(wc) : "v"(wp) : "memory");
    asm volatile("global_load_dwordx2 %0, %1, %2" : "=&v"(cw) : "v"((128 * 64 + 32 * half + 4 * (t & 7)) * 2), "s"(gw) : "memory");
    const uint32_t cA = (uint32_t)(((lane & 7) ^ (lane >> 4)) * 16);
    if (wv >= 12) {
      if (tile < NTILE) {
#pragma unroll
        for (int i = 0; i < 4; i++) dma16(img + i * 1024, (tile * 32 + 8 * i + (lane >> 3)) * 128 + ((i & 1) ? cA ^ 64u : cA), xsrd);
      }
    } else {
      const __amdgpu_buffer_rsrc_t asrd = __builtin_amdgcn_make_buffer_rsrc((void*)gw, (short)0, 64 * 128, 0x00020000);
      const __amdgpu_buffer_rsrc_t usrd = __builtin_amdgcn_make_buffer_rsrc((void*)uw, (short)0, 64 * 128, 0x00020000);
      const uint32_t woff = (lane >> 2) * 128 + half * 64 + (lane & 3) * 16;
#pragma unroll
      for (int i = 0; i < 3; i++) {
        const int p = wv + 12 * i;
        if (p < 4) dma16(lb + P_W + p * 1024, p * 2048 + woff, asrd);
        else if (p < 8) dma16(lb + P_W + p * 1024, (p - 4) * 2048 + woff, usrd);
        else if (p < 33) { const int q = p - 8; dma16(lb + P_X + q * 1024, (b * NPB + 8 * q + (lane >> 3)) * 128 + ((q & 1) ? cA ^ 64u : cA), xsrd); }
      }
    }
  }
  TS(1);
  LDS_BARRIER();
  if (t < 64) {
    const u32x2 c = *(const u32x2*)(hist + 2 * t);
    const uint32_t p0 = (c[0] + 3) >> 2, tot = p0 + ((c[1] + 3) >> 2);
    const uint32_t ex = wave_scan_add(tot) - tot;
    *(u32x2*)(base + 2 * t) = u32x2{ex, ex + p0};
  }
  if (wv < 12) WAIT_VM(2); else if (tile < NTILE) WAIT_VM(4); else WAIT_VM(0);
  asm volatile("" : "+v"(wc), "+v"(cw) :: "memory");
  *(u32x4*)(smem + A_W + t * 16) = wc;
  LDS_BARRIER();
#pragma unroll
  for (int k = 0; k < 4; k++) {
    if (k < cnt) {
      const uint32_t p = base[bk[k]] * 4 + rk0[k];
      const uint32_t o = (p >> 2) * 20 + (p & 3);
      *(uint32_t*)(smem + A_ST + o + (p & 3) * 3) = (s4[k] & 0xffffu) | (ev[k] << 16);
      *(uint8_t*)(smem + A_ST + o + 16) = (uint8_t)(d4[k] - bk[k] * NPB);
    }
  }
  if (wv <= 12) {
    const uint32_t m2 = wave_max_u32(max(max(ev[0] & 0x7fffu, ev[1] & 0x7fffu), max(ev[2] & 0x7fffu, ev[3] & 0x7fffu)) << 16);
    if (lane == 63) __hip_atomic_fetch_max(&misc[M_MX + 2], m2, RLX_WG);
  }
  LDS_BARRIER();
  TS(2);
  if (wv <= 12) {
    const __amdgpu_buffer_rsrc_t rsrd = __builtin_amdgcn_make_buffer_rsrc((void*)(ws + WS_REC + (size_t)w * CHB * 20), (short)0, CHB * 20, 0x00020000);
    const uint32_t nb = base[127] + ((hist[127] + 3) >> 2);
#pragma unroll
    for (int i = 0; i < 2; i++) {
      const int c = t + 832 * i;
      if (c * 16 < (int)nb * 20) __builtin_amdgcn_raw_buffer_store_b128(*(const u32x4*)(smem + A_ST + c * 16), rsrd, c * 16, 0, AUX_SC1);
    }
    WAIT_VM(0);
    if (arrive(&misc[M_CW], lane) == 12) {
      const u32x2 c = *(const u32x2*)(hist + 2 * lane), o = *(const u32x2*)(base + 2 * lane);
      gu64* g = (gu64*)(ws + WS_SEG) + w * NB + 2 * lane;
      __hip_atomic_store(g,     (u64)(o[0] | (c[0] << 12)) | (tag40 << 24), RLX_AGENT);
      __hip_atomic_store(g + 1, (u64)(o[1] | (c[1] << 12)) | (tag40 << 24), RLX_AGENT);
      if (lane == 0) { __hip_atomic_store(&misc[M_PUB], 1u, RLX_WG); TS_PUB(0); }
    }
  }
  Lane L;
  L.c01 = f32x2{bf2f(cw[0] & 0xffffu) * NLOG2E, __uint_as_float(cw[0] & 0xffff0000u) * NLOG2E};
  L.c23 = f32x2{bf2f(cw[1] & 0xffffu) * NLOG2E, __uint_as_float(cw[1] & 0xffff0000u) * NLOG2E};
  const uint32_t ntw = w < NTILE - 768 ? 4u : 3u;
  if (wv >= 5 && wv < 12) {
    await_lds(&misc[M_CW], 13u);
    const int rt = wv - 5, row = 32 * rt + r31;
    const uint32_t xa = lb + P_X + row * 128 + ((h ^ ((row >> 1) & 7)) * 16);
    const uint32_t troff = lb + P_W + (8 * h + ((lane & 15) >> 2)) * 64 + (16 * ((lane >> 4) & 1) + 4 * (lane & 3)) * 2;
    bf16x8 a[4];
#pragma unroll
    for (int s = 0; s < 4; s++) a[s] = *(LDSP(const bf16x8))(xa ^ (32u * s));
    uint32_t xam = 0;
#pragma unroll
    for (int part = 0; part < 2; part++) {
      f32x16 acc;
#pragma unroll
      for (int i = 0; i < 16; i++) acc[i] = 0.f;
#pragma unroll
      for (int s = 0; s < 4; s++) acc = mfma(a[s], tr_frag(troff + part * 4096 + s * 1024, 256), acc);
      const uint32_t o = lb + (part ? L_XU : L_EA) + (32 * rt + 4 * h) * 128 + r31 * 4;
#pragma unroll
      for (int i = 0; i < 16; i++) {
        const float v = part ? acc[i] : acc[i] * NLOG2E;
        if (rt < 6 || crow(i, 0) + 4 * h < 4) { *(LDSP(float))(o + crow(i, 0) * 128) = v; if (!part) xam = max(xam, absbits(v)); }
      }
    }
    xam = wave_max_u32(xam);
    if (lane == 63) misc[M_XAM + rt] = xam;
  } else if (wv == 4) {
    const uint32_t cm = wave_max_u32(max(max(absbits(L.c01[0]), absbits(L.c01[1])), max(absbits(L.c23[0]), absbits(L.c23[1]))));
    if (lane == 63) misc[M_CMX] = cm;
  } else if (wv >= 12 && tile < NTILE) {
    WAIT_VM(0);
    bf16x8 xf[4];
    const uint32_t xa = img + r31 * 128 + ((h ^ ((r31 >> 1) & 7)) * 16);
#pragma unroll
    for (int s = 0; s < 4; s++) xf[s] = *(LDSP(const bf16x8))(xa ^ (32u * s));
    const uint32_t troff = lb + A_W + (8 * h + ((lane & 15) >> 2)) * 128 + (16 * ((lane >> 4) & 1) + 4 * (lane & 3)) * 2;
    float mb = 0.f, mv = 0.f, s2 = 0.f;
    u32x4 o1[4];
    table_half(o1, xf, troff, 0, mb, mv, s2);
    table_image(o1, img, lane);
    table_half(o1, xf, troff, 1, mb, mv, s2);
    const uint32_t m0 = wave_max_u32(__float_as_uint(mb)), m1 = wave_max_u32(__float_as_uint(mv));
    s2 = wave_sum_f32(s2);
    if (lane == 63) {
      __hip_atomic_fetch_max(&misc[M_MX], m0, RLX_WG); __hip_atomic_fetch_max(&misc[M_MX + 1], m1, RLX_WG);
      misc[M_MX + 4 + (wv & 3)] = __float_as_uint(s2);
    }
    if (arrive(&misc[M_CB], lane) == ntw - 1 && lane == 0) {
      const u32x4 m = *(const u32x4*)(misc + M_MX), q = *(const u32x4*)(misc + M_MX + 4);
      const float sq = (__uint_as_float(q[0]) + __uint_as_float(q[1])) + (__uint_as_float(q[2]) + __uint_as_float(q[3]));
      const uint32_t g0 = min((m[0] + 0xffffu) >> 16, 0x7fffu) | (min((m[1] + 0xffffu) >> 16, 0x7fffu) << 16);
      const uint32_t g1 = (m[2] >> 16) | (__float_as_uint(sq) & 0xffff0000u);
      gu64* g = (gu64*)(ws + WS_TBG) + 2 * w;
      __hip_atomic_store(g,     (u64)g0 | ((u64)tag32 << 32), RLX_AGENT);
      __hip_atomic_store(g + 1, (u64)g1 | ((u64)tag32 << 32), RLX_AGENT);
    }
    await_lds(&misc[M_PUB], 1u);
    const __amdgpu_buffer_rsrc_t wsrd = __builtin_amdgcn_make_buffer_rsrc((void*)(ws + WS_TAB), (short)0, 2 * NMAX * 128, 0x00020000);
    table_store(img, tile, 0, wsrd, lane);
    table_image(o1, img, lane);
    table_store(img, tile, 1, wsrd, lane);
  }
  TS(3);

  auto thread_id = [&]() {
    int v = wv * 64 + (int)__builtin_amdgcn_mbcnt_hi(~0u, __builtin_amdgcn_mbcnt_lo(~0u, 0u));
    asm volatile("" : "+v"(v));
    return v;
  };
  const int tb = thread_id(), lb_ = tb & 63;
  const __amdgpu_buffer_rsrc_t tsrd = __builtin_amdgcn_make_buffer_rsrc((void*)(ws + WS_TAB + (size_t)half * NMAX * 128), (short)0, N * 128, 0x00020000);
  uint32_t sg[3] = {0, 0, 0};
  auto poll_segments = [&](int g) {
    gu64* gs = (gu64*)(ws + WS_SEG) + g * NB + b;
    const bool third = g + 192 < NCH;
    for (unsigned spins = 0;; spins++) {
      const u64 v0 = __hip_atomic_load(gs, RLX_AGENT), v1 = __hip_atomic_load(gs + 96 * NB, RLX_AGENT);
      const u64 v2 = __hip_atomic_load(gs + (third ? 192 * NB : 0), RLX_AGENT);
      const bool ok = (v0 >> 24) == tag40 && (v1 >> 24) == tag40 && (v2 >> 24) == tag40;
      sg[0] = ((uint32_t)v0 & 0xfffu) | ((((uint32_t)v0 >> 12) & 0xfffu) << 16);
      sg[1] = ((uint32_t)v1 & 0xfffu) | ((((uint32_t)v1 >> 12) & 0xfffu) << 16);
      sg[2] = third ? ((uint32_t)v2 & 0xfffu) | ((((uint32_t)v2 >> 12) & 0xfffu) << 16) : 0u;
      if (__all(ok)) break;
      if (spins > SPIN_CAP) { sg[0] = sg[1] = sg[2] = 0; break; }
      __builtin_amdgcn_s_sleep(8);
    }
  };
  Recs R;
  int done = 0;
  uint32_t rk[6][2]; bool part1 = false;
  auto post_total = [&](int dn, int j, int l) {
    uint32_t v = 0;
    if (j == 0) v = min(max((int)(sg[0] >> 16) - dn, 0), 64) + min(max((int)(sg[1] >> 16) - dn, 0), 64) + min(max((int)(sg[2] >> 16) - dn, 0), 64);
    v = wave_scan_add(v);
    if (l == 63) misc[M_TOT + wv] = v;
  };
  auto rank = [&](int tt, int dn, int take) {
    const int j = tt & 7;
    const uint32_t spare = (196u + (uint32_t)(tt & 63) % 60u) * 0x01010101u;
    bool more = false, p1 = false;
#pragma unroll
    for (int i = 0; i < 6; i++) {
      const int len = (int)(sg[i >> 1] >> 16) - dn;
      more |= len > take;
      const int v = min(max(min(len, take) - 32 * (i & 1) - 4 * j, 0), 4);
      const uint32_t keep = v == 4 ? 0xffffffffu : (1u << (8 * v)) - 1u;
      R.lc[i] = (R.lc[i] & keep) | (spare & ~keep);
      if (i & 1) p1 |= v > 0;
    }
    part1 = __any(p1);
#pragma unroll
    for (int pt = 0; pt < 2; pt++) {
      if (pt == 0 || part1) {
        uint32_t r[3][4];
#pragma unroll
        for (int st = 0; st < 3; st++)
#pragma unroll
          for (int k = 0; k < 4; k++) r[st][k] = __hip_atomic_fetch_add((uint32_t*)(smem + L_CNT) + ((R.lc[2 * st + pt] >> (8 * k)) & 0xffu), 1u, RLX_WG);
#pragma unroll
        for (int st = 0; st < 3; st++) {
          rk[2 * st + pt][0] = r[st][0] | (r[st][1] << 16); rk[2 * st + pt][1] = r[st][2] | (r[st][3] << 16);
          asm volatile("" : "+v"(rk[2 * st + pt][0]), "+v"(rk[2 * st + pt][1]));
        }
      } else {
#pragma unroll
        for (int st = 0; st < 3; st++) rk[2 * st + 1][0] = rk[2 * st + 1][1] = 0;
      }
    }
    if (__any(more) && (tt & 63) == 0) misc[M_MORE] = 1;
  };
  auto total = [&]() {
    const u32x4 s = *(const u32x4*)(misc + M_TOT) + *(const u32x4*)(misc + M_TOT + 4) + *(const u32x4*)(misc + M_TOT + 8);
    return (int)__builtin_amdgcn_readfirstlane((s[0] + s[1]) + (s[2] + s[3]));
  };
  auto scan = [&](int ln_) {
    u32x4 c = *(const u32x4*)(smem + L_CNT + ln_ * 16);
    if (ln_ >= NPB / 4) c = u32x4{0, 0, 0, 0};
    const u32x4 p = (c + 3) & ~3u;
    const uint32_t s1 = p[0] + p[1], s2 = s1 + p[2], tot = s2 + p[3];
    const uint32_t inc = wave_scan_add(tot), ex = inc - tot;
    *(u32x4*)(smem + L_OFF + ln_ * 16) = ln_ < NPB / 4 ? u32x4{ex, ex + p[0], ex + s1, ex + s2} : u32x4{SORT_CAP, SORT_CAP, SORT_CAP, SORT_CAP};
    const uint32_t T4 = (uint32_t)__builtin_amdgcn_readlane((int)inc, 63);
    const uint32_t T32 = (T4 + 31) & ~31u;
    if ((T4 >> 2) + ln_ < (T32 >> 2) + 2) *(uint32_t*)(smem + L_QN + ((T4 >> 2) + ln_) * 4) = QN_DUMMY;
    if (ln_ == 0) { misc[M_NSTEP] = T32 >> 5; misc[M_T4] = T4; }
  };
  auto scatter = [&](int tt) {
    if (wv < 12) {
#pragma unroll
      for (int i = 0; i < 6; i++) {
        if ((i & 1) == 0 || part1) {
#pragma unroll
          for (int k = 0; k < 4; k++) {
            const uint32_t pos = min(*(const uint32_t*)(smem + L_OFF + ((R.lc[i] >> (8 * k)) & 0xffu) * 4) + ((rk[i][k >> 1] >> (16 * (k & 1))) & 0xffffu), (uint32_t)(SORT_CAP - 1));
            const uint32_t rec = R.rc[i][k];
            *(u32x2*)(smem + L_SORT + pos * 8) = u32x2{(rec & 0xffffu) << 7, rec & 0xffff0000u};
          }
        }
      }
    }
    u32x2 dummy = {(uint32_t)tt, 0u};
    asm volatile("" : "+v"(dummy));
    dummy = u32x2{dummy[0] * 0u + OFF_DUMMY, 0u};
    const int u = 1023 - tt;
    if (u < NPB * 4) {
      const uint32_t nd = u >> 2, k = u & 3;
      const uint32_t c = *(const uint32_t*)(smem + L_CNT + nd * 4), o = *(const uint32_t*)(smem + L_OFF + nd * 4);
      const uint32_t val = nd * (128u + (AGG_ROW << 16));
      for (uint32_t q = k; 4 * q < c; q += 4) *(uint32_t*)(smem + L_QN + o + q * 4) = val;
      if (k == 3) *(uint32_t*)(smem + L_NQ + nd * 4) += (c + 3) >> 2;
      if (k < 3 && c + k < ((c + 3) & ~3u)) *(u32x2*)(smem + L_SORT + (o + c + k) * 8) = dummy;
    } else if (u < NPB * 4 + 64) {
      const uint32_t T4 = misc[M_T4], T32 = (T4 + 31) & ~31u, i = T4 + (u - NPB * 4);
      if (i < T32 + 8) *(u32x2*)(smem + L_SORT + i * 8) = dummy;
    }
  };
  auto lane_consts = [&](int tt, int nstep) {
    const int n_ = (nstep + 15 - wv) >> 4, first = 8 * (wv * (nstep >> 4) + min(wv, nstep & 15));
    const int g = (tt & 63) >> 3, j = tt & 7;
    L.rec = lb + L_SORT + (first + g * n_) * 32;
    L.qn  = lb + L_QN + (first + g * n_) * 4;
    L.c16 = j * 16;
    L.ea  = lb + L_EA + j * 16;
    L.agg = lb + L_AGG + j * 4;
    asm volatile("" : "+v"(L.rec), "+v"(L.qn), "+v"(L.ea), "+v"(L.agg));
    return n_;
  };

  int take = 64;
  if (wv < 12) {
    poll_segments(tb >> 3);
    TS(4);
    load_recs(R, sg, ws, tb >> 3, tb & 7, 0);
    post_total(0, tb & 7, lb_);
    uint32_t v = (tb & 7) == 0 ? (sg[0] >> 16) + (sg[1] >> 16) + (sg[2] >> 16) : 0u;
    v = wave_scan_add(v);
    if (lb_ == 63) misc[M_ALL + wv] = v;
    WAIT_RECS(R);
    TS(5);
    rank(tb, 0, take);
  }
  LDS_BARRIER();
  if (w == 0 && tb == 0) __hip_atomic_store((gu64*)(ws + WS_NONCE), (((u64)xn[1] << 32) | xn[0]) + 1ull, RLX_AGENT);
  if (total() > TAKE_MAX) {
    LDS_BARRIER();
    if (tb < 256) *(uint32_t*)(smem + L_CNT + tb * 4) = 0;
    LDS_BARRIER();
    take = 16;
    if (wv < 12) rank(tb, 0, take);
    LDS_BARRIER();
  }
  if (wv == 0) scan(lb_);
  else if (wv == 11) {
    gu64* gt = (gu64*)(ws + WS_TBG) + 2 * lb_;
    uint32_t mb, mv, me; float sq;
    for (unsigned spins = 0;; spins++) {
      bool ok = true; mb = mv = me = 0; sq = 0.f;
#pragma unroll
      for (int i = 0; i < 4; i++) {
        const u64 v0 = __hip_atomic_load(gt + 128 * i, RLX_AGENT), v1 = __hip_atomic_load(gt + 128 * i + 1, RLX_AGENT);
        ok &= (uint32_t)(v0 >> 32) == tag32 && (uint32_t)(v1 >> 32) == tag32;
        mb = max(mb, (uint32_t)v0 << 16); mv = max(mv, (uint32_t)v0 & 0xffff0000u);
        me = max(me, (uint32_t)v1 << 16); sq += __uint_as_float((uint32_t)v1 & 0xffff0000u);
      }
      if (__all(ok)) break;
      if (spins > SPIN_CAP) break;
      __builtin_amdgcn_s_sleep(8);
    }
    mb = wave_max_u32(mb); mv = wave_max_u32(mv); me = wave_max_u32(me); sq = wave_sum_f32(sq);
    if (lb_ == 63) *(u32x4*)(misc + M_BND) = u32x4{mb, mv, me, __float_as_uint(sq)};
  }
  LDS_BARRIER();
  TS(6);
  bool fast; float delta;
  {
    const u32x4 bd = *(const u32x4*)(misc + M_BND);
    const u32x4 x0 = *(const u32x4*)(misc + M_XAM), x1 = *(const u32x4*)(misc + M_XAM + 4);
    const uint32_t ma = max(max(max(x0[0], x0[1]), max(x0[2], x0[3])), max(max(x1[0], x1[1]), x1[2]));
    const float mv = __uint_as_float(bd[1]);
    const float yb = (__uint_as_float(ma) + __uint_as_float(bd[0]) + __uint_as_float(bd[2]) * __uint_as_float(misc[M_CMX])) * 1.01f;
    const u32x4 s = *(const u32x4*)(misc + M_ALL) + *(const u32x4*)(misc + M_ALL + 4) + *(const u32x4*)(misc + M_ALL + 8);
    const float all = (float)((s[0] + s[1]) + (s[2] + s[3]) + 4u);
    const int p1 = (int)(__float_as_uint(4.1f * mv) >> 23) - 127 + 2, p2 = (int)(__float_as_uint(all * mv * 1.01f) >> 23) - 127 + 1 - 8;
    const int p = max(max(p1, p2), -80);
    delta = __uint_as_float((uint32_t)(p - 23 + 127) << 23);
    const float mg = __uint_as_float(((uint32_t)(p + 127) << 23) | 0x400000u);
    const float mgs = __uint_as_float(__builtin_amdgcn_readfirstlane(__float_as_uint(mg)));
    L.magic = f32x2{mgs, mgs};
    delta = __uint_as_float(__builtin_amdgcn_readfirstlane(__float_as_uint(delta)));
    const bool fine = delta * 4096.f <= __builtin_sqrtf(__uint_as_float(bd[3]) / (64.f * (float)N)) || mv == 0.f;
    fast = __builtin_amdgcn_readfirstlane((yb <= 28.f) && (mv <= 1.0e12f) && fine);
  }
  if (wv >= 12 && tile < NTILE) {
    WAIT_VM(0);
    if (arrive(&misc[M_CT], lb_) == ntw - 1 && lb_ == 0) { __hip_atomic_store((gu64*)(ws + WS_TRG) + w, (u64)tag32 << 32, RLX_AGENT); TS_PUB(1); }
  }
  scatter(tb);
  for (int i = tb; i < 197 * AGG_ROW / 16; i += 1024) *(u32x4*)(smem + L_AGG + i * 16) = u32x4{0, 0, 0, 0};
  if (fast) {
    for (int i = tb; i < NPB * 8; i += 1024) {
      f32x4 v = *(const f32x4*)(smem + L_EA + i * 16);
      v[0] = __builtin_amdgcn_exp2f(v[0]); v[1] = __builtin_amdgcn_exp2f(v[1]); v[2] = __builtin_amdgcn_exp2f(v[2]); v[3] = __builtin_amdgcn_exp2f(v[3]);
      *(f32x4*)(smem + L_EA + i * 16) = v;
    }
  }
  if (wv == 11) {
    gu64* gt = (gu64*)(ws + WS_TRG) + lb_;
    for (unsigned spins = 0; spins < SPIN_CAP; spins++) {
      bool ok = true;
#pragma unroll
      for (int i = 0; i < 4; i++) ok &= (uint32_t)(__hip_atomic_load(gt + 64 * i, RLX_AGENT) >> 32) == tag32;
      if (__all(ok)) break;
      __builtin_amdgcn_s_sleep(4);
    }
  }
  int again = __builtin_amdgcn_readfirstlane(misc[M_MORE]);
  LDS_BARRIER();
  int nstep = __builtin_amdgcn_readfirstlane(misc[M_NSTEP]);
  TS(7);
  {
    const int n_ = lane_consts(tb, nstep);
    if (fast) edge_loop<0>(L, n_, tsrd);
    else      edge_loop<2>(L, n_, tsrd);
  }
  while (again) {
    done += take;
    const int t2 = thread_id();
    if (wv < 12) { poll_segments(t2 >> 3); load_recs(R, sg, ws, t2 >> 3, t2 & 7, done); WAIT_RECS(R); }
    LDS_BARRIER();
    if (t2 < 256) *(uint32_t*)(smem + L_CNT + t2 * 4) = 0;
    if (t2 == 0) misc[M_MORE] = 0;
    if (wv < 12) post_total(done, t2 & 7, t2 & 63);
    LDS_BARRIER();
    take = total() <= TAKE_MAX ? 64 : 16;
    if (wv < 12) rank(t2, done, take);
    LDS_BARRIER();
    if (wv == 0) scan(t2 & 63);
    LDS_BARRIER();
    scatter(t2);
    again = __builtin_amdgcn_readfirstlane(misc[M_MORE]);
    LDS_BARRIER();
    nstep = __builtin_amdgcn_readfirstlane(misc[M_NSTEP]);
    const int n_ = lane_consts(t2, nstep);
    if (fast) edge_loop<0>(L, n_, tsrd);
    else      edge_loop<2>(L, n_, tsrd);
  }
  LDS_BARRIER();

  const int te = thread_id();
  if (te < NPB * 4) {
    const int nl = te >> 2, u = te & 3, node = b * NPB + nl;
    if (node < N) {
      float y[8];
      const uint32_t bias = *(const uint32_t*)(smem + L_NQ + nl * 4) * __float_as_uint(L.magic[0]);
      const f32x4 u0 = *(const f32x4*)(smem + L_XU + nl * 128 + u * 32), u1 = *(const f32x4*)(smem + L_XU + nl * 128 + u * 32 + 16);
#pragma unroll
      for (int i = 0; i < 4; i++) {
        const u32x2 v2 = *(const u32x2*)(smem + L_AGG + nl * AGG_ROW + (8 * i + 2 * u) * 4);
#pragma unroll
        for (int j = 0; j < 2; j++) {
          const float agg = fast ? (float)(int)(v2[j] - bias) * delta : __uint_as_float(v2[j]);
          const float v = (j ? u1[i] : u0[i]) + agg;
          y[4 * j + i] = v * __builtin_amdgcn_rcpf(1.f + __builtin_amdgcn_exp2f(v * NLOG2E));
        }
      }
      const u32x4 o = {cvtpk(y[0], y[1]), cvtpk(y[2], y[3]), cvtpk(y[4], y[5]), cvtpk(y[6], y[7])};
      *(u32x4*)(out + (size_t)node * 64 + 32 * half + 8 * u) = o;
    }
  }
  TS_OUT(256 + blockIdx.x);
}

}

extern "C" void kernel_launch(void* const* d_in, const int* in_sizes, int n_in,
                              void* d_out, int out_size, void* d_ws, size_t ws_size,
                              hipStream_t stream) {
  const uint16_t* x  = (const uint16_t*)d_in[0];
  const uint16_t* ea = (const uint16_t*)d_in[1];
  const int*      src = (const int*)d_in[2];
  const int*      dst = (const int*)d_in[3];
  const uint16_t* uw = (const uint16_t*)d_in[4];
  const uint16_t* vw = (const uint16_t*)d_in[5];
  const uint16_t* gw = (const uint16_t*)d_in[6];
  const int N = in_sizes[0] / 64, E = in_sizes[2];
  char* ws = (char*)d_ws;
  (void)hipFuncSetAttribute((const void*)k_mpnn, hipFuncAttributeMaxDynamicSharedMemorySize, L_TOTAL);
  k_mpnn<<<NCH, 1024, L_TOTAL, stream>>>(src, dst, ea, x, uw, vw, gw, ws, (uint16_t*)d_out, N, E);
}
```

```cpp
#include <hip/hip_runtime.h>
#include <hip/hip_bf16.h>
#include <stdint.h>

#pragma clang diagnostic ignored "-Wint-to-pointer-cast"
#pragma clang diagnostic ignored "-Wpointer-to-int-cast"
namespace {

typedef float    f32x2  __attribute__((ext_vector_type(2)));
typedef float    f32x4  __attribute__((ext_vector_type(4)));
typedef float    f32x16 __attribute__((ext_vector_type(16)));
typedef short    bf16x8 __attribute__((ext_vector_type(8)));
typedef short    s16x4  __attribute__((ext_vector_type(4)));
typedef uint32_t u32x2  __attribute__((ext_vector_type(2)));
typedef uint32_t u32x4  __attribute__((ext_vector_type(4)));
typedef unsigned long long u64;
typedef __attribute__((address_space(1))) unsigned long long gu64;
#define LDSP(T) __attribute__((address_space(3))) T*
#define DI __device__ __forceinline__
#define RLX_AGENT __ATOMIC_RELAXED, __HIP_MEMORY_SCOPE_AGENT
#define RLX_WG    __ATOMIC_RELAXED, __HIP_MEMORY_SCOPE_WORKGROUP

constexpr int   NWG = 256;
constexpr int   NPB = 98;
constexpr int   CH  = 3128;
constexpr int   CHB = 976;
#define XPW 6
constexpr float NLOG2E = -1.4426950408889634f;
constexpr int   AUX_SC1 = 16;
constexpr unsigned SPIN_CAP = 1u << 18;
static_assert(4 * CHB >= CH + 3 * NWG && (CHB * 20) % 16 == 0, "blocks");

constexpr size_t WS_NONCE = 0;
constexpr size_t WS_SEG = 128;
constexpr size_t WS_BND = WS_SEG + (size_t)NWG * NWG * 8;
constexpr size_t WS_REC = WS_BND + (size_t)NWG * 32;
constexpr size_t WS_DBG = (WS_REC + (size_t)NWG * CHB * 20 + 1024 + 127) & ~(size_t)127;
static_assert(WS_REC % 128 == 0, "ws align");

constexpr int SORT_CAP = 4528;
constexpr int TAKE32_MAX = SORT_CAP - 3 * NPB - 64 - 8;
constexpr int L_CNT    = 0;
constexpr int L_OFF    = L_CNT + 512;
constexpr int L_NQ     = L_OFF + 512;
constexpr int L_MISC   = L_NQ + 512;
constexpr int L_QN     = L_MISC + 512;
constexpr int L_EA     = (L_QN + SORT_CAP / 2 + 15) & ~15;
constexpr int L_XU     = L_EA + 99 * 256;
constexpr int L_AGG    = (L_XU + 98 * 256 + 127) & ~127;
constexpr int L_STAGE  = L_AGG + 99 * 256;
constexpr int L_SORT   = L_STAGE + 8 * 8192;
constexpr int L_TOTAL  = L_SORT + SORT_CAP * 4;
static_assert(L_TOTAL <= 163840, "LDS");
static_assert((L_STAGE & 127) == 0 && (L_SORT & 15) == 0 && TAKE32_MAX >= 4096, "align");
constexpr int A_HIST = L_STAGE;
constexpr int A_BASE = A_HIST + 1024;
constexpr int A_ST   = A_BASE + 1024;
constexpr int A_W    = (A_ST + CHB * 20 + 127) & ~127;
constexpr int P_X    = L_AGG;
static_assert(A_W + 4 * 8192 <= L_SORT && P_X + 128 * 128 <= L_STAGE, "phase A images");
constexpr uint32_t DUMMY = 0x0000FFFFu;
constexpr uint32_t QN_DUMMY = 98u * 256u;
constexpr int M_MORE = 0, M_NTILE = 1, M_T4 = 2;
constexpr int M_CW = 3, M_CB = 4;
constexpr int M_TOT = 10, M_ALL = 11, M_XAM = 12;
constexpr int M_FAST = 13, M_EXP = 14;
constexpr int M_MX = 16;
constexpr int M_SQ = 24;

DI float    bf2f(uint32_t v16) { return __uint_as_float(v16 << 16); }
DI uint32_t cvtpk(float lo, float hi) {
  typedef __bf16 bf16x2 __attribute__((ext_vector_type(2)));
  return __builtin_bit_cast(uint32_t, __builtin_convertvector(f32x2{lo, hi}, bf16x2));
}
DI f32x16   mfma(bf16x8 a, bf16x8 b, f32x16 c) { return __builtin_amdgcn_mfma_f32_32x32x16_bf16(a, b, c, 0, 0, 0); }
DI int      crow(int r, int h) { return (r & 3) + 8 * (r >> 2) + 4 * h; }
DI uint32_t absbits(float v) { return __float_as_uint(v) & 0x7fffffffu; }

template <int CTRL, int RM> DI uint32_t dpp0(uint32_t v) { return (uint32_t)__builtin_amdgcn_update_dpp(0, (int)v, CTRL, RM, 0xf, false); }
DI uint32_t wave_scan_add(uint32_t v) {
  v += dpp0<0x111, 0xf>(v); v += dpp0<0x112, 0xf>(v); v += dpp0<0x114, 0xf>(v); v += dpp0<0x118, 0xf>(v);
  v += dpp0<0x142, 0xa>(v); v += dpp0<0x143, 0xc>(v);
  return v;
}
DI uint32_t wave_max_u32(uint32_t v) {
  v = max(v, dpp0<0x111, 0xf>(v)); v = max(v, dpp0<0x112, 0xf>(v)); v = max(v, dpp0<0x114, 0xf>(v)); v = max(v, dpp0<0x118, 0xf>(v));
  v = max(v, dpp0<0x142, 0xa>(v)); v = max(v, dpp0<0x143, 0xc>(v));
  return v;
}
DI float wave_sum_f32(float s) {
  s += __uint_as_float(dpp0<0x111, 0xf>(__float_as_uint(s))); s += __uint_as_float(dpp0<0x112, 0xf>(__float_as_uint(s)));
  s += __uint_as_float(dpp0<0x114, 0xf>(__float_as_uint(s))); s += __uint_as_float(dpp0<0x118, 0xf>(__float_as_uint(s)));
  s += __uint_as_float(dpp0<0x142, 0xa>(__float_as_uint(s))); s += __uint_as_float(dpp0<0x143, 0xc>(__float_as_uint(s)));
  return s;
}
#define SB() __builtin_amdgcn_sched_barrier(0)
#define LDS_BARRIER() asm volatile("s_waitcnt lgkmcnt(0)\n\ts_barrier" ::: "memory")
#define WAIT_VM(N) asm volatile("s_waitcnt vmcnt(" #N ")" ::: "memory")

DI uint32_t arrive(uint32_t* word, int lane) {
  asm volatile("s_waitcnt lgkmcnt(0)" ::: "memory");
  uint32_t v = 0;
  if (lane == 0) v = __hip_atomic_fetch_add(word, 1u, RLX_WG);
  return __builtin_amdgcn_readfirstlane(v);
}
DI void await_lds(uint32_t* word, uint32_t want) {
  for (unsigned spins = 0; spins < SPIN_CAP; spins++) {
    if (__builtin_amdgcn_readfirstlane(__hip_atomic_load(word, RLX_WG)) >= want) break;
    __builtin_amdgcn_s_sleep(1);
  }
  asm volatile("" ::: "memory");
}

DI void dma16(uint32_t ldsaddr, uint32_t voff, __amdgpu_buffer_rsrc_t srd) {
  asm volatile("s_mov_b32 m0, %0\n\ts_nop 0\n\tbuffer_load_dwordx4 %1, %2, 0 offen lds" :: "s"(ldsaddr), "v"(voff), "s"(srd) : "memory");
}
DI bf16x8 tr_frag(uint32_t addr, int four_rows) {
  const s16x4 lo = __builtin_amdgcn_ds_read_tr16_b64_v4i16((LDSP(s16x4))addr);
  const s16x4 hi = __builtin_amdgcn_ds_read_tr16_b64_v4i16((LDSP(s16x4))(addr + four_rows));
  return (bf16x8)__builtin_shufflevector(lo, hi, 0, 1, 2, 3, 4, 5, 6, 7);
}

#define TS(i) do { } while (0)
#define TS_DECL do { } while (0)
#define TS_OUT(slot) do { } while (0)
#define TS_PUB(i) do { } while (0)

DI f32x2 pair(const f32x16& v, int i) { f32x2 r = {v[i], v[i + 1]}; return r; }
DI f32x2 exp2x2(f32x2 v) { f32x2 r = {__builtin_amdgcn_exp2f(v[0]), __builtin_amdgcn_exp2f(v[1])}; return r; }
DI f32x2 rcpx2(f32x2 v) { f32x2 r = {__builtin_amdgcn_rcpf(v[0]), __builtin_amdgcn_rcpf(v[1])}; return r; }
DI f32x2 fma2(f32x2 a, f32x2 b, f32x2 c) { return __builtin_elementwise_fma(a, b, c); }

struct Wts { bf16x8 b[2][4], v[2][4], c[2]; };
template <int V> struct IC { static constexpr int value = V; };
struct Acc { f32x16 z, hv; };

struct Frac { f32x2 n[2], d[2]; };
DI void exp_all(f32x16& z) {
#pragma unroll
  for (int i = 0; i < 16; i++) z[i] = __builtin_amdgcn_exp2f(z[i]);
}
DI void combine(Frac& F, const f32x16& u, const f32x16& hv, int o, f32x2 ea) {
  const f32x2 one = {1.f, 1.f};
  const f32x2 h0 = pair(hv, 8 * o), h1 = pair(hv, 8 * o + 2), h2 = pair(hv, 8 * o + 4), h3 = pair(hv, 8 * o + 6);
  const f32x2 a0 = fma2(ea, pair(u, 8 * o), one),     a1 = fma2(ea, pair(u, 8 * o + 2), one);
  const f32x2 a2 = fma2(ea, pair(u, 8 * o + 4), one), a3 = fma2(ea, pair(u, 8 * o + 6), one);
  const f32x2 d01 = a0 * a1, n01 = fma2(h1, a0, h0 * a1);
  const f32x2 d23 = a2 * a3, n23 = fma2(h3, a2, h2 * a3);
  F.d[o] = d01 * d23; F.n[o] = fma2(n23, d01, n01 * d23);
}
template <bool EXPEA> DI f32x2 octet_plain(const Acc& A, int o, f32x2 ea) {
  const f32x2 one = {1.f, 1.f};
  const f32x16& z = A.z; const f32x16& hv = A.hv;
  const f32x2 h0 = pair(hv, 8 * o), h1 = pair(hv, 8 * o + 2), h2 = pair(hv, 8 * o + 4), h3 = pair(hv, 8 * o + 6);
  f32x2 r[4];
#pragma unroll
  for (int j = 0; j < 4; j++)
    r[j] = EXPEA ? rcpx2(fma2(ea, exp2x2(pair(z, 8 * o + 2 * j)), one)) : rcpx2(one + exp2x2(pair(z, 8 * o + 2 * j) + ea));
  const f32x2 r0 = r[0], r1 = r[1], r2 = r[2], r3 = r[3];
  return (h0 * r0 + h1 * r1) + (h2 * r2 + h3 * r3);
}

DI uint32_t madlo(uint32_t v, uint32_t mul, uint32_t add) {
  uint32_t r; asm("v_mad_u32_u16 %0, %1, %2, %3" : "=v"(r) : "v"(v), "s"(mul), "v"(add)); return r;
}
template <int MUL> DI uint32_t madlo_i(uint32_t v, uint32_t add) {
  uint32_t r; asm("v_mad_u32_u16 %0, %1, %3, %2" : "=v"(r) : "v"(v), "v"(add), "i"(MUL)); return r;
}
template <int MUL> DI uint32_t madhi_i(uint32_t v, uint32_t add) {
  uint32_t r; asm("v_mad_u32_u16 %0, %1, %3, %2 op_sel:[1,0,0,0]" : "=v"(r) : "v"(v), "v"(add), "i"(MUL)); return r;
}
template <int K> DI uint32_t byte_x4(uint32_t v) {
  uint32_t r;
  if (K == 0) asm("v_lshlrev_b32_sdwa %0, 2, %1 dst_sel:DWORD dst_unused:UNUSED_PAD src0_sel:DWORD src1_sel:BYTE_0" : "=v"(r) : "v"(v));
  if (K == 1) asm("v_lshlrev_b32_sdwa %0, 2, %1 dst_sel:DWORD dst_unused:UNUSED_PAD src0_sel:DWORD src1_sel:BYTE_1" : "=v"(r) : "v"(v));
  if (K == 2) asm("v_lshlrev_b32_sdwa %0, 2, %1 dst_sel:DWORD dst_unused:UNUSED_PAD src0_sel:DWORD src1_sel:BYTE_2" : "=v"(r) : "v"(v));
  if (K == 3) asm("v_lshlrev_b32_sdwa %0, 2, %1 dst_sel:DWORD dst_unused:UNUSED_PAD src0_sel:DWORD src1_sel:BYTE_3" : "=v"(r) : "v"(v));
  return r;
}
DI uint32_t byte_x4(uint32_t v, int k) { return k == 0 ? byte_x4<0>(v) : (k == 1 ? byte_x4<1>(v) : (k == 2 ? byte_x4<2>(v) : byte_x4<3>(v))); }
DI void lds_add_u32(uint32_t addr, float v) { __hip_atomic_fetch_add((LDSP(uint32_t))addr, __float_as_uint(v), RLX_WG); }
DI void lds_add_f32(uint32_t addr, float v) { __hip_atomic_fetch_add((LDSP(float))addr, v, RLX_WG); }

struct Lane {
  uint32_t rec_a;
  uint32_t cA, cB;
  uint32_t afrag[4];
  uint32_t erec;
  uint32_t esel;
  uint32_t qn_a;
  uint32_t col4;
  uint32_t stage;
};
struct Tile { bf16x8 a[4], ef; };
struct Quads { uint32_t q0, q1; };

DI void issue_tile(const Lane& L, int tile, int buf, __amdgpu_buffer_rsrc_t srd) {
  const u32x2 r0 = *(LDSP(const u32x2))(L.rec_a + tile * 128);
  const u32x2 r1 = *(LDSP(const u32x2))(L.rec_a + tile * 128 + 64);
  const uint32_t a0 = madlo(r0[0], 128u, L.cA), a1 = madlo(r0[1], 128u, L.cB);
  const uint32_t a2 = madlo(r1[0], 128u, L.cA), a3 = madlo(r1[1], 128u, L.cB);
  const uint32_t sb = L.stage + buf * 4096;
  dma16(sb, a0, srd); dma16(sb + 1024, a1, srd); dma16(sb + 2048, a2, srd); dma16(sb + 3072, a3, srd);
}
template <int BUF> DI void load_tile(Tile& T, Quads& Q, const Lane& L, int tile) {
  const uint32_t erec = *(LDSP(const uint32_t))(L.erec + tile * 128);
  Q.q0 = *(LDSP(const uint32_t))(L.qn_a + tile * 16);
  Q.q1 = *(LDSP(const uint32_t))(L.qn_a + tile * 16 + 8);
#pragma unroll
  for (int s = 0; s < 4; s++) T.a[s] = *(LDSP(const bf16x8))(L.afrag[s] + BUF * 4096);
  const u32x4 eu = {__builtin_amdgcn_perm(erec, erec, L.esel), 0u, 0u, 0u};
  T.ef = __builtin_bit_cast(bf16x8, eu);
}
DI void mma(Acc& A, const Tile& T, const Wts& W, int c) {
  f32x16 zero;
#pragma unroll
  for (int i = 0; i < 16; i++) zero[i] = 0.f;
  A.z  = mfma(T.a[0], W.b[c][0], zero);
  A.hv = mfma(T.a[0], W.v[c][0], zero);
#pragma unroll
  for (int s = 1; s < 4; s++) { A.z = mfma(T.a[s], W.b[c][s], A.z); A.hv = mfma(T.a[s], W.v[c][s], A.hv); }
  A.z  = mfma(T.ef, W.c[c], A.z);
}
struct Gate { f32x2 e0, e1; };
struct Rows { uint32_t a[4]; };
DI void rows_of(Rows& R, const Lane& L, const Quads& Q) {
  R.a[0] = madlo_i<1>(Q.q0, L.col4); R.a[1] = madhi_i<1>(Q.q0, L.col4); R.a[2] = madlo_i<1>(Q.q1, L.col4); R.a[3] = madhi_i<1>(Q.q1, L.col4);
}
DI void load_gate(Gate& G, const Rows& R, int c) {
  G.e0 = f32x2{*(LDSP(const float))(R.a[0] + 128 * c), *(LDSP(const float))(R.a[1] + 128 * c)};
  G.e1 = f32x2{*(LDSP(const float))(R.a[2] + 128 * c), *(LDSP(const float))(R.a[3] + 128 * c)};
}
constexpr int AGG_REL = L_AGG - L_EA;
#define PY 2
#define PYY PY
#define PXO 0
#define PXY 1
#define PRIO_HI() do { if (YOUNG) __builtin_amdgcn_s_setprio(PYY); else __builtin_amdgcn_s_setprio(PY); SB(); } while (0)
#define PRIO_LO() do { if (YOUNG) __builtin_amdgcn_s_setprio(PXY); else __builtin_amdgcn_s_setprio(PXO); SB(); } while (0)
#define PIN(x) asm volatile("" : "+v"(x))
#define EX(z, i) z[i] = __builtin_amdgcn_exp2f(z[i])
template <class F0, class F1>
DI void section_x(Acc& An, const Tile& T, const Wts& W, int cn, f32x16& z, Frac& F, f32x2& r0, f32x2& r1,
                  Gate& G, const Rows& Rg, int cg, const Rows& Ra, int ca, f32x2 m0, f32x2 m1, F0 extra0, F1 extra1) {
  f32x16 zero;
#pragma unroll
  for (int i = 0; i < 16; i++) zero[i] = 0.f;
  An.z = mfma(T.a[0], W.b[cn][0], zero); PIN(An.z);
  load_gate(G, Rg, cg);
  EX(z, 0); EX(z, 1); EX(z, 2);
  SB();
  An.hv = mfma(T.a[0], W.v[cn][0], zero); PIN(An.hv);
  lds_add_u32(Ra.a[0] + AGG_REL + 128 * ca, m0[0]); lds_add_u32(Ra.a[1] + AGG_REL + 128 * ca, m0[1]);
  EX(z, 3); EX(z, 4);
  SB();
  An.z = mfma(T.a[1], W.b[cn][1], An.z); PIN(An.z);
  lds_add_u32(Ra.a[2] + AGG_REL + 128 * ca, m1[0]); lds_add_u32(Ra.a[3] + AGG_REL + 128 * ca, m1[1]);
  EX(z, 5); EX(z, 6);
  SB();
  An.hv = mfma(T.a[1], W.v[cn][1], An.hv); PIN(An.hv);
  EX(z, 7); EX(z, 8);
  extra0();
  SB();
  An.z = mfma(T.a[2], W.b[cn][2], An.z); PIN(An.z);
  EX(z, 9); EX(z, 10);
  extra1();
  SB();
  An.hv = mfma(T.a[2], W.v[cn][2], An.hv); PIN(An.hv);
  EX(z, 11); EX(z, 12); EX(z, 13);
  SB();
  An.z = mfma(T.a[3], W.b[cn][3], An.z); PIN(An.z);
  EX(z, 14); EX(z, 15);
  SB();
  An.hv = mfma(T.a[3], W.v[cn][3], An.hv); PIN(An.hv);
  r0[0] = __builtin_amdgcn_rcpf(F.d[0][0]); r0[1] = __builtin_amdgcn_rcpf(F.d[0][1]); PIN(r0);
  SB();
  An.z = mfma(T.ef, W.c[cn], An.z); PIN(An.z);
  r1[0] = __builtin_amdgcn_rcpf(F.d[1][0]); r1[1] = __builtin_amdgcn_rcpf(F.d[1][1]); PIN(r1);
  PIN(G.e0); PIN(G.e1);
  SB();
}
DI void section_y(Frac& F, f32x2& m0, f32x2& m1, f32x2 r0, f32x2 r1, const f32x16& u, const f32x16& hv, const Gate& G, f32x2 magic) {
  const f32x2 one = {1.f, 1.f};
  f32x2 a[2][4], d01[2], d23[2], n01[2], n23[2];
  a[0][1] = fma2(G.e0, pair(u, 2), one);  a[1][1] = fma2(G.e1, pair(u, 10), one); SB();
  a[0][3] = fma2(G.e0, pair(u, 6), one);  a[1][3] = fma2(G.e1, pair(u, 14), one); SB();
  a[0][0] = fma2(G.e0, pair(u, 0), one);  a[1][0] = fma2(G.e1, pair(u, 8), one); SB();
  a[0][2] = fma2(G.e0, pair(u, 4), one);  a[1][2] = fma2(G.e1, pair(u, 12), one); SB();
  n01[0] = pair(hv, 0) * a[0][1]; n01[1] = pair(hv, 8) * a[1][1]; SB();
  n23[0] = pair(hv, 4) * a[0][3]; n23[1] = pair(hv, 12) * a[1][3]; SB();
  m0 = fma2(F.n[0], r0, magic); m1 = fma2(F.n[1], r1, magic); SB();
  d01[0] = a[0][0] * a[0][1]; d01[1] = a[1][0] * a[1][1]; SB();
  d23[0] = a[0][2] * a[0][3]; d23[1] = a[1][2] * a[1][3]; SB();
  n01[0] = fma2(pair(hv, 2), a[0][0], n01[0]); n01[1] = fma2(pair(hv, 10), a[1][0], n01[1]); SB();
  n23[0] = fma2(pair(hv, 6), a[0][2], n23[0]); n23[1] = fma2(pair(hv, 14), a[1][2], n23[1]); SB();
  F.d[0] = d01[0] * d23[0]; F.d[1] = d01[1] * d23[1]; SB();
  n01[0] = n01[0] * d23[0]; n01[1] = n01[1] * d23[1]; SB();
  n23[0] = n23[0] * d01[0]; n23[1] = n23[1] * d01[1]; SB();
  F.n[0] = n01[0] + n23[0]; F.n[1] = n01[1] + n23[1];
}
struct Pipe {
  Tile T; Acc A0; Rows R, RP;
  Frac F;
  f32x2 m0, m1;
  u32x2 ra, rb;
};
template <int BUF, bool YOUNG>
DI void step_fast(const Lane& L, const Wts& W, Pipe& P, int t1, int t3, f32x2 magic, __amdgpu_buffer_rsrc_t srd) {
  Acc A1; Gate G; f32x2 r0, r1; Quads QN; Rows RN; uint32_t erec, da[4];
  const uint32_t sb = L.stage + BUF * 4096;
  section_x(A1, P.T, W, 1, P.A0.z, P.F, r0, r1, G, P.R, 0, P.RP, 0, P.m0, P.m1,
            [&]() { da[0] = madlo(P.ra[0], 128u, L.cA); da[1] = madlo(P.ra[1], 128u, L.cB); },
            [&]() { da[2] = madlo(P.rb[0], 128u, L.cA); da[3] = madlo(P.rb[1], 128u, L.cB); });
  asm volatile("s_waitcnt vmcnt(0)" ::: "memory");
#pragma unroll
  for (int s = 0; s < 4; s++) P.T.a[s] = *(LDSP(const bf16x8))(L.afrag[s] + (BUF ^ 1) * 4096);
  erec  = *(LDSP(const uint32_t))(L.erec + t1 * 128);
  QN.q0 = *(LDSP(const uint32_t))(L.qn_a + t1 * 16);
  QN.q1 = *(LDSP(const uint32_t))(L.qn_a + t1 * 16 + 8);
  dma16(sb, da[0], srd);
  dma16(sb + 1024, da[1], srd);
  SB();
  PRIO_HI();
  section_y(P.F, P.m0, P.m1, r0, r1, P.A0.z, P.A0.hv, G, magic);
  SB();
  PRIO_LO();
  dma16(sb + 2048, da[2], srd);
  dma16(sb + 3072, da[3], srd);
  SB();
  section_x(P.A0, P.T, W, 0, A1.z, P.F, r0, r1, G, P.R, 1, P.RP, 1, P.m0, P.m1,
            [&]() { rows_of(RN, L, QN); },
            [&]() { const u32x4 eu = {__builtin_amdgcn_perm(erec, erec, L.esel), 0u, 0u, 0u}; P.T.ef = __builtin_bit_cast(bf16x8, eu); });
  P.ra = *(LDSP(const u32x2))(L.rec_a + t3 * 128);
  P.rb = *(LDSP(const u32x2))(L.rec_a + t3 * 128 + 64);
  SB();
  PRIO_HI();
  section_y(P.F, P.m0, P.m1, r0, r1, A1.z, A1.hv, G, magic);
  P.RP = P.R; P.R = RN;
  SB();
  PRIO_LO();
}
template <bool YOUNG> DI void edge_loop_fast(const Lane& L, const Wts& W, int wv, int ntile, f32x2 magic, __amdgpu_buffer_rsrc_t srd, unsigned long long* dbg) {
#define ST(i) do { } while (0)
  int tile = wv;
  if (tile < ntile) {
    Pipe P; Quads Q;
    if (YOUNG) __builtin_amdgcn_s_setprio(PXY); else __builtin_amdgcn_s_setprio(PXO);
    ST(0);
    asm volatile("s_waitcnt vmcnt(0)" ::: "memory");
    ST(1);
    issue_tile(L, min(tile + 8, ntile), 1, srd);
    load_tile<0>(P.T, Q, L, tile);
    rows_of(P.R, L, Q);
    mma(P.A0, P.T, W, 0);
    Q.q0 = Q.q1 = (98u * 256u) * 0x10001u;
    rows_of(P.RP, L, Q);
    P.F.n[0] = P.F.n[1] = f32x2{0.f, 0.f}; P.m0 = P.m1 = magic;
    P.F.d[0] = P.F.d[1] = f32x2{1.f, 1.f};
    { const int t2 = min(tile + 16, ntile);
      P.ra = *(LDSP(const u32x2))(L.rec_a + t2 * 128); P.rb = *(LDSP(const u32x2))(L.rec_a + t2 * 128 + 64); }
    ST(2);
    while (true) {
      step_fast<0, YOUNG>(L, W, P, min(tile + 8, ntile), min(tile + 24, ntile), magic, srd);
      tile += 8;
      if (tile >= ntile) break;
      step_fast<1, YOUNG>(L, W, P, min(tile + 8, ntile), min(tile + 24, ntile), magic, srd);
      tile += 8;
      if (tile >= ntile) break;
    }
    ST(3);
    const f32x2 z0 = fma2(P.F.n[0], rcpx2(P.F.d[0]), magic), z1 = fma2(P.F.n[1], rcpx2(P.F.d[1]), magic);
    lds_add_u32(P.RP.a[0] + AGG_REL, P.m0[0]); lds_add_u32(P.RP.a[1] + AGG_REL, P.m0[1]);
    lds_add_u32(P.RP.a[2] + AGG_REL, P.m1[0]); lds_add_u32(P.RP.a[3] + AGG_REL, P.m1[1]);
    lds_add_u32(P.RP.a[0] + AGG_REL + 128, z0[0]); lds_add_u32(P.RP.a[1] + AGG_REL + 128, z0[1]);
    lds_add_u32(P.RP.a[2] + AGG_REL + 128, z1[0]); lds_add_u32(P.RP.a[3] + AGG_REL + 128, z1[1]);
    asm volatile("s_waitcnt vmcnt(0)" ::: "memory");
    __builtin_amdgcn_s_setprio(0);
    ST(4);
  }
}
template <bool EXPEA> DI void edge_loop_plain(const Lane& L, const Wts& W, int wv, int ntile, f32x2 magic, __amdgpu_buffer_rsrc_t srd) {
  for (int tile = wv; tile < ntile; tile += 8) {
    Tile T; Quads Q;
    issue_tile(L, tile, 0, srd);
    asm volatile("s_waitcnt vmcnt(0)" ::: "memory");
    load_tile<0>(T, Q, L, tile);
#pragma unroll
    for (int c = 0; c < 2; c++) {
      Acc A; Gate G; Rows Rw;
      rows_of(Rw, L, Q);
      load_gate(G, Rw, c);
      mma(A, T, W, c);
      const f32x2 m0 = octet_plain<EXPEA>(A, 0, G.e0), m1 = octet_plain<EXPEA>(A, 1, G.e1);
      if (EXPEA) {
        const f32x2 i0 = m0 + magic, i1 = m1 + magic;
        lds_add_u32(Rw.a[0] + AGG_REL + 128 * c, i0[0]); lds_add_u32(Rw.a[1] + AGG_REL + 128 * c, i0[1]);
        lds_add_u32(Rw.a[2] + AGG_REL + 128 * c, i1[0]); lds_add_u32(Rw.a[3] + AGG_REL + 128 * c, i1[1]);
      } else {
        lds_add_f32(Rw.a[0] + AGG_REL + 128 * c, m0[0]); lds_add_f32(Rw.a[1] + AGG_REL + 128 * c, m0[1]);
        lds_add_f32(Rw.a[2] + AGG_REL + 128 * c, m1[0]); lds_add_f32(Rw.a[3] + AGG_REL + 128 * c, m1[1]);
      }
    }
  }
}

struct Recs { u32x4 rc[4]; uint32_t lc[4]; };
DI void load_recs(Recs& R, const uint32_t* sg, const char* ws, int g, int j, int done) {
  const __amdgpu_buffer_rsrc_t rsrd = __builtin_amdgcn_make_buffer_rsrc((void*)(ws + WS_REC), (short)0, NWG * CHB * 20, 0x00020000);
#pragma unroll
  for (int p = 0; p < 2; p++) {
#pragma unroll
    for (int r = 0; r < 2; r++) {
      uint32_t off = (uint32_t)((g + 128 * r) * (CHB * 20) + ((int)(sg[r] & 0xffff) + (done >> 2) + 4 * p + j) * 20);
      if ((int)(sg[r] >> 16) - done <= 16 * p + 4 * j) off = 0x40000000u;
      asm volatile("buffer_load_dwordx4 %0, %1, %2, 0 offen sc1" : "=&v"(R.rc[2 * r + p]) : "v"(off), "s"(rsrd) : "memory");
      asm volatile("buffer_load_dword %0, %1, %2, 0 offen offset:16 sc1" : "=&v"(R.lc[2 * r + p]) : "v"(off), "s"(rsrd) : "memory");
    }
  }
}
#define WAIT_RECS(R) asm volatile("s_waitcnt vmcnt(0)" : "+v"(R.rc[0]), "+v"(R.rc[1]), "+v"(R.rc[2]), "+v"(R.rc[3]), \
                                  "+v"(R.lc[0]), "+v"(R.lc[1]), "+v"(R.lc[2]), "+v"(R.lc[3]) :: "memory")

__global__ __launch_bounds__(512, 2) void k_mpnn(const int* __restrict__ src, const int* __restrict__ dst,
                                                 const uint16_t* __restrict__ ea, const uint16_t* __restrict__ x,
                                                 const uint16_t* __restrict__ uw, const uint16_t* __restrict__ vw,
                                                 const uint16_t* __restrict__ gw, char* __restrict__ ws,
                                                 uint16_t* __restrict__ out, int N, int E) {
  extern __shared__ __attribute__((aligned(128))) char smem[];
  const uint32_t lb = (uint32_t)(uintptr_t)(LDSP(char))smem;
  uint32_t* misc = (uint32_t*)(smem + L_MISC);
  const int t = threadIdx.x, lane = t & 63;
  const int w = blockIdx.x;
  const int b = (blockIdx.x & 7) * 32 + (blockIdx.x >> 3);
  const int wv = __builtin_amdgcn_readfirstlane(t >> 6);
  const int r31 = lane & 31, h = lane >> 5;
  TS_DECL; TS(0);

  const __amdgpu_buffer_rsrc_t srd = __builtin_amdgcn_make_buffer_rsrc((void*)x, (short)0, N * 128, 0x00020000);
  const int e0 = w * CH;
  const int n  = min(CH, E - e0);
  const int cnt = min(max(n - 8 * t, 0), 8);
  u32x4 d4[2], s4[2], e4; u32x2 xn;
  {
    const __amdgpu_buffer_rsrc_t dsrd = __builtin_amdgcn_make_buffer_rsrc((void*)dst, (short)0, E * 4, 0x00020000);
    const __amdgpu_buffer_rsrc_t ssrd = __builtin_amdgcn_make_buffer_rsrc((void*)src, (short)0, E * 4, 0x00020000);
    const __amdgpu_buffer_rsrc_t esrd = __builtin_amdgcn_make_buffer_rsrc((void*)ea, (short)0, (E * 2 + 3) & ~3, 0x00020000);
    const uint32_t eo = cnt > 0 ? (uint32_t)(e0 + 8 * t) : 0x20000000u;
    asm volatile("buffer_load_dwordx4 %0, %1, %2, 0 offen" : "=&v"(d4[0]) : "v"(eo * 4), "s"(dsrd) : "memory");
    asm volatile("buffer_load_dwordx4 %0, %1, %2, 0 offen offset:16" : "=&v"(d4[1]) : "v"(eo * 4), "s"(dsrd) : "memory");
    asm volatile("buffer_load_dwordx4 %0, %1, %2, 0 offen" : "=&v"(s4[0]) : "v"(eo * 4), "s"(ssrd) : "memory");
    asm volatile("buffer_load_dwordx4 %0, %1, %2, 0 offen offset:16" : "=&v"(s4[1]) : "v"(eo * 4), "s"(ssrd) : "memory");
    asm volatile("buffer_load_dwordx4 %0, %1, %2, 0 offen" : "=&v"(e4) : "v"(eo * 2), "s"(esrd) : "memory");
    asm volatile("global_load_dwordx2 %0, %1, %2 sc1" : "=&v"(xn) : "v"(0), "s"(ws + WS_NONCE) : "memory");
  }
  uint32_t* hist = (uint32_t*)(smem + A_HIST);
  uint32_t* base = (uint32_t*)(smem + A_BASE);
  if (t < 256) hist[t] = 0;
  else if (t < 384) { *(uint32_t*)(smem + L_CNT + (t - 256) * 4) = 0; *(uint32_t*)(smem + L_NQ + (t - 256) * 4) = 0; }
  else { misc[t - 384] = 0; if (t < 448) *(float*)(smem + L_EA + 98 * 256 + (t - 384) * 4) = 0.f; }
  LDS_BARRIER();
  asm volatile("s_waitcnt vmcnt(0)" : "+v"(xn), "+v"(d4[0]), "+v"(d4[1]), "+v"(s4[0]), "+v"(s4[1]), "+v"(e4) :: "memory");
  const uint32_t tag32 = xn[0] + 1u;
  const u64 tag40 = ((((u64)xn[1] << 32) | xn[0]) + 1ull) & 0xffffffffffull;
  uint32_t bk[8], rk0[8];
#pragma unroll
  for (int k = 0; k < 8; k++) {
    bk[k] = (d4[k >> 2][k & 3] * 21400u) >> 21;
    rk0[k] = 0;
    if (k < cnt) rk0[k] = __hip_atomic_fetch_add(&hist[bk[k]], 1u, RLX_WG);
  }
  TS(1);
  u32x4 wb; uint32_t c0, c1;
  asm volatile("global_load_dwordx4 %0, %1, %2" : "=&v"(wb) : "v"(t * 16), "s"(gw + 64 * 64) : "memory");
  asm volatile("global_load_ushort %0, %1, %2" : "=&v"(c0) : "v"(r31 * 2), "s"(gw + 128 * 64) : "memory");
  asm volatile("global_load_ushort %0, %1, %2 offset:64" : "=&v"(c1) : "v"(r31 * 2), "s"(gw + 128 * 64) : "memory");
  LDS_BARRIER();
  uint32_t pf = 0;
  if (wv == 7) {
    const u32x4 c = *(const u32x4*)(hist + 4 * lane);
    const u32x4 p = (c + 3) >> 2;
    const uint32_t s1 = p[0] + p[1], s2 = s1 + p[2], tot = s2 + p[3];
    const uint32_t ex = wave_scan_add(tot) - tot;
    *(u32x4*)(base + 4 * lane) = u32x4{ex, ex + p[0], ex + s1, ex + s2};
  } else {
    const __amdgpu_buffer_rsrc_t asrd = __builtin_amdgcn_make_buffer_rsrc((void*)gw, (short)0, 8192, 0x00020000);
    const __amdgpu_buffer_rsrc_t usrd = __builtin_amdgcn_make_buffer_rsrc((void*)uw, (short)0, 8192, 0x00020000);
    const __amdgpu_buffer_rsrc_t vsrd = __builtin_amdgcn_make_buffer_rsrc((void*)vw, (short)0, 8192, 0x00020000);
    const uint32_t cA = (uint32_t)(((lane & 7) ^ (lane >> 4)) * 16);
#pragma unroll
    for (int i = 0; i < 6; i++) {
      const int p = wv + 7 * i;
      const uint32_t wo = (lane * 16) ^ ((lane & 16) << 2);
      if (p < 8) dma16(lb + A_W + p * 1024, p * 1024 + wo, asrd);
      else if (p < 16) dma16(lb + A_W + 16384 + (p - 8) * 1024, (p - 8) * 1024 + wo, usrd);
      else if (p < 24) dma16(lb + A_W + 24576 + (p - 16) * 1024, (p - 16) * 1024 + wo, vsrd);
      else if (p < 37) { const int q = p - 24; dma16(lb + P_X + q * 1024, (b * NPB + 8 * q + (lane >> 3)) * 128 + ((q & 1) ? cA ^ 64u : cA), srd); }
    }
  }
  {
    uint32_t m2 = 0;
#pragma unroll
    for (int k = 0; k < 4; k++) m2 = max(m2, max(e4[k] & 0x7fffu, (e4[k] >> 16) & 0x7fffu));
    m2 = wave_max_u32(m2 << 16);
    if (lane == 63) __hip_atomic_fetch_max(&misc[M_MX + 2], m2, RLX_WG);
  }
  LDS_BARRIER();
#pragma unroll
  for (int k = 0; k < 8; k++) {
    if (k < cnt) {
      const uint32_t p = base[bk[k]] * 4 + rk0[k];
      const uint32_t o = (p >> 2) * 20 + (p & 3);
      const uint32_t ev = (k & 1) ? e4[k >> 1] & 0xffff0000u : e4[k >> 1] << 16;
      *(uint32_t*)(smem + A_ST + o + (p & 3) * 3) = (s4[k >> 2][k & 3] & 0xffffu) | ev;
      *(uint8_t*)(smem + A_ST + o + 16) = (uint8_t)(d4[k >> 2][k & 3] - bk[k] * NPB);
    }
  }
  LDS_BARRIER();
  TS(2);
  {
    const __amdgpu_buffer_rsrc_t rsrd = __builtin_amdgcn_make_buffer_rsrc((void*)(ws + WS_REC + (size_t)w * CHB * 20), (short)0, CHB * 20, 0x00020000);
    const uint32_t nb = base[255] + ((hist[255] + 3) >> 2);
#pragma unroll
    for (int i = 0; i < 3; i++) {
      const int c = t + 512 * i;
      if (c * 16 < (int)nb * 20) __builtin_amdgcn_raw_buffer_store_b128(*(const u32x4*)(smem + A_ST + c * 16), rsrd, c * 16, 0, AUX_SC1);
    }
    asm volatile("s_waitcnt vmcnt(0)" : "+v"(wb), "+v"(c0), "+v"(c1), "+v"(pf) :: "memory");
#pragma unroll
    for (int k = 0; k < 4; k++) wb[k] = cvtpk(bf2f(wb[k] & 0xffffu) * NLOG2E, __uint_as_float(wb[k] & 0xffff0000u) * NLOG2E);
    *(u32x4*)(smem + A_W + 8192 + ((t * 16) ^ ((t & 16) << 2))) = wb;
    if (arrive(&misc[M_CW], lane) == 7) {
      const u32x4 c = *(const u32x4*)(hist + 4 * lane), o = *(const u32x4*)(base + 4 * lane);
      gu64* g = (gu64*)(ws + WS_SEG) + w * NWG + 4 * lane;
#pragma unroll
      for (int k = 0; k < 4; k++) __hip_atomic_store(g + k, (u64)(o[k] | (c[k] << 12)) | (tag40 << 24), RLX_AGENT);
      if (lane == 0) TS_PUB(0);
    }
  }
  await_lds(&misc[M_CW], 8u);
  Wts W;
  const uint32_t troff = lb + A_W + (8 * h + ((lane & 15) >> 2)) * 128 + (16 * ((lane >> 4) & 1) + 4 * (lane & 3)) * 2, sw = (lane & 8) << 3;
  auto weights = [&](auto cc) {
    constexpr int c = decltype(cc)::value;
#pragma unroll
    for (int s = 0; s < 4; s++) {
      W.b[c][s] = tr_frag(troff + 8192 + s * 2048 + ((c * 64) ^ sw), 512);
      W.v[c][s] = tr_frag(troff + 24576 + s * 2048 + ((c * 64) ^ sw), 512);
    }
  };
  {
    const int rt = wv >> 1, row = 32 * rt + r31;
    const uint32_t xa = lb + P_X + row * 128 + ((h ^ ((row >> 1) & 7)) * 16);
    uint32_t xam = 0, mb = 0, mv = 0; float s2 = 0.f;
    auto job = [&](auto cc) {
      constexpr int c = decltype(cc)::value;
      bf16x8 a[4], fa[4], fu[4];
#pragma unroll
      for (int s = 0; s < 4; s++) {
        a[s] = *(LDSP(const bf16x8))(xa ^ (32u * s));
        fa[s] = tr_frag(troff + s * 2048 + ((c * 64) ^ sw), 512);
        fu[s] = tr_frag(troff + 16384 + s * 2048 + ((c * 64) ^ sw), 512);
      }
      weights(cc);
      f32x16 za, zu, zb, zv;
#pragma unroll
      for (int i = 0; i < 16; i++) { za[i] = 0.f; zu[i] = 0.f; zb[i] = 0.f; zv[i] = 0.f; }
#pragma unroll
      for (int s = 0; s < 4; s++) { za = mfma(a[s], fa[s], za); zu = mfma(a[s], fu[s], zu); zb = mfma(a[s], W.b[c][s], zb); zv = mfma(a[s], W.v[c][s], zv); }
      const uint32_t o = lb + L_EA + (32 * rt + 4 * h) * 256 + (32 * c + r31) * 4;
      if (rt < 3) {
#pragma unroll
        for (int i = 0; i < 16; i++) {
          const float v = za[i] * NLOG2E;
          *(LDSP(float))(o + crow(i, 0) * 256) = v; *(LDSP(float))(o + (L_XU - L_EA) + crow(i, 0) * 256) = zu[i];
          xam = max(xam, absbits(v)); mb = max(mb, absbits(zb[i])); mv = max(mv, absbits(zv[i])); s2 = fmaf(zv[i], zv[i], s2);
        }
      } else if (h == 0) {
#pragma unroll
        for (int i = 0; i < 2; i++) {
          const float v = za[i] * NLOG2E;
          *(LDSP(float))(o + i * 256) = v; *(LDSP(float))(o + (L_XU - L_EA) + i * 256) = zu[i];
          xam = max(xam, absbits(v)); mb = max(mb, absbits(zb[i])); mv = max(mv, absbits(zv[i])); s2 = fmaf(zv[i], zv[i], s2);
        }
      }
    };
    if (wv & 1) job(IC<1>{}); else job(IC<0>{});
    xam = wave_max_u32(xam); mb = wave_max_u32(mb); mv = wave_max_u32(mv); s2 = wave_sum_f32(s2);
    const uint32_t cm = wave_max_u32(max(absbits(bf2f(c0) * NLOG2E), absbits(bf2f(c1) * NLOG2E)));
    if (lane == 63) {
      __hip_atomic_fetch_max(&misc[M_XAM], xam, RLX_WG); __hip_atomic_fetch_max(&misc[M_MX], mb, RLX_WG);
      __hip_atomic_fetch_max(&misc[M_MX + 1], mv, RLX_WG); misc[M_MX + 3] = cm; misc[M_SQ + wv] = __float_as_uint(s2);
    }
    if (arrive(&misc[M_CB], lane) == 7 && lane == 0) {
      const u32x4 m = *(const u32x4*)(misc + M_MX), q0 = *(const u32x4*)(misc + M_SQ), q1 = *(const u32x4*)(misc + M_SQ + 4);
      const float sq = ((__uint_as_float(q0[0]) + __uint_as_float(q0[1])) + (__uint_as_float(q0[2]) + __uint_as_float(q0[3])))
                     + ((__uint_as_float(q1[0]) + __uint_as_float(q1[1])) + (__uint_as_float(q1[2]) + __uint_as_float(q1[3])));
      gu64* g = (gu64*)(ws + WS_BND) + 4 * w;
      __hip_atomic_store(g,     (u64)(min((m[0] + 0xffffu) >> 16, 0x7fffu) | (min((m[1] + 0xffffu) >> 16, 0x7fffu) << 16)) | ((u64)tag32 << 32), RLX_AGENT);
      __hip_atomic_store(g + 1, (u64)__float_as_uint(sq) | ((u64)tag32 << 32), RLX_AGENT);
      __hip_atomic_store(g + 2, (u64)m[2] | ((u64)tag32 << 32), RLX_AGENT);
    }
  }
  TS(3);

  auto thread_id = [&]() {
    int v = wv * 64 + (int)__builtin_amdgcn_mbcnt_hi(~0u, __builtin_amdgcn_mbcnt_lo(~0u, 0u));
    asm volatile("" : "+v"(v));
    return v;
  };
  const int tb = thread_id(), lb_ = tb & 63;
  uint32_t sg[2] = {0, 0};
  auto poll_segments = [&](int g) {
    gu64* gs = (gu64*)(ws + WS_SEG) + g * NWG + b;
    for (unsigned spins = 0;; spins++) {
      const u64 v0 = __hip_atomic_load(gs, RLX_AGENT), v1 = __hip_atomic_load(gs + 128 * NWG, RLX_AGENT);
      const bool ok = (v0 >> 24) == tag40 && (v1 >> 24) == tag40;
      sg[0] = ((uint32_t)v0 & 0xfffu) | ((((uint32_t)v0 >> 12) & 0xfffu) << 16);
      sg[1] = ((uint32_t)v1 & 0xfffu) | ((((uint32_t)v1 >> 12) & 0xfffu) << 16);
      if (__all(ok)) break;
      if (spins > SPIN_CAP) { sg[0] = sg[1] = 0; break; }
      __builtin_amdgcn_s_sleep(8);
    }
  };
  Recs R;
  int done = 0;
  uint32_t rk[4][2]; bool part1[2] = {false, false};
  auto post_total = [&](int dn, int j, int l) {
    uint32_t v = 0;
    if (j == 0) v = min(max((int)(sg[0] >> 16) - dn, 0), 32) + min(max((int)(sg[1] >> 16) - dn, 0), 32);
    v = wave_scan_add(v);
    if (l == 63) __hip_atomic_fetch_add(&misc[M_TOT], v, RLX_WG);
  };
  auto rank = [&](int tt, int dn, int take) {
    const int j = tt & 3;
    const uint32_t spare = (98u + (uint32_t)(tt & 63) % 30u) * 0x01010101u;
    bool more = false;
#pragma unroll
    for (int i = 0; i < 4; i++) {
      const int len = (int)(sg[i >> 1] >> 16) - dn;
      more |= len > take;
      const int v = min(max(min(len, take) - 16 * (i & 1) - 4 * j, 0), 4);
      const uint32_t drop = (uint32_t)(0xffffffffull << (8 * v));
      R.lc[i] = (R.lc[i] & ~drop) | (spare & drop);
      if (i & 1) part1[i >> 1] = __any(v > 0);
    }
    uint32_t r[2][4];
#pragma unroll
    for (int st = 0; st < 2; st++)
#pragma unroll
      for (int k = 0; k < 4; k++) r[st][k] = __hip_atomic_fetch_add((LDSP(uint32_t))(L_CNT + byte_x4(R.lc[2 * st], k)), 1u, RLX_WG);
#pragma unroll
    for (int st = 0; st < 2; st++) {
      rk[2 * st][0] = r[st][0] | (r[st][1] << 16); rk[2 * st][1] = r[st][2] | (r[st][3] << 16);
      rk[2 * st + 1][0] = rk[2 * st + 1][1] = 0;
      if (part1[st]) {
        uint32_t q[4];
#pragma unroll
        for (int k = 0; k < 4; k++) q[k] = __hip_atomic_fetch_add((LDSP(uint32_t))(L_CNT + byte_x4(R.lc[2 * st + 1], k)), 1u, RLX_WG);
        rk[2 * st + 1][0] = q[0] | (q[1] << 16); rk[2 * st + 1][1] = q[2] | (q[3] << 16);
      }
    }
    if (__any(more) && (tt & 63) == 0) misc[M_MORE] = 1;
  };
  auto scan = [&](int ln_) {
    u32x2 c = *(const u32x2*)(smem + L_CNT + ln_ * 8);
    if (ln_ >= NPB / 2) c = u32x2{0, 0};
    const uint32_t p0 = (c[0] + 3) & ~3u, p1 = (c[1] + 3) & ~3u;
    const uint32_t inc = wave_scan_add(p0 + p1), ex = inc - (p0 + p1);
    *(u32x2*)(smem + L_OFF + ln_ * 8) = ln_ < NPB / 2 ? u32x2{ex, ex + p0} : u32x2{SORT_CAP, SORT_CAP};
    const uint32_t T4 = (uint32_t)__builtin_amdgcn_readlane((int)inc, 63);
    const uint32_t T32 = (T4 + 31) & ~31u;
    if (T4 + ln_ < T32 + 32) *(uint32_t*)(smem + L_SORT + (T4 + ln_) * 4) = DUMMY;
    if ((T4 >> 2) + ln_ < (T32 >> 2) + 8) *(uint16_t*)(smem + L_QN + ((T4 >> 2) + ln_) * 2) = QN_DUMMY;
    if (ln_ == 0) misc[M_NTILE] = T32 >> 5;
  };
  auto scatter = [&](int tt) {
#pragma unroll
    for (int i = 0; i < 4; i++) {
      if ((i & 1) == 0 || part1[i >> 1]) {
#pragma unroll
        for (int k = 0; k < 4; k++) {
          const uint32_t pos = min(*(LDSP(const uint32_t))(L_OFF + byte_x4(R.lc[i], k)) + ((rk[i][k >> 1] >> (16 * (k & 1))) & 0xffffu), (uint32_t)(SORT_CAP - 1));
          *(LDSP(uint32_t))(lb + L_SORT + pos * 4) = R.rc[i][k];
        }
      }
    }
    if (tt < NPB * 4) {
      const uint32_t nd = tt >> 2, k = tt & 3;
      const uint32_t c = *(const uint32_t*)(smem + L_CNT + nd * 4), o = *(const uint32_t*)(smem + L_OFF + nd * 4);
      for (uint32_t q = k; 4 * q < c; q += 4) *(uint16_t*)(smem + L_QN + (o >> 1) + q * 2) = (uint16_t)(nd << 8);
      if (k == 3) *(uint32_t*)(smem + L_NQ + nd * 4) += (c + 3) >> 2;
      if (k < 3 && c + k < ((c + 3) & ~3u)) *(uint32_t*)(smem + L_SORT + (o + c + k) * 4) = DUMMY + ((uint32_t)tt >> 12);
    }
  };
  auto lane_consts = [&](int tt) {
    Lane L;
    const int ln_ = tt & 63, g8_ = ln_ >> 3, p8_ = ln_ & 7, r31_ = ln_ & 31, h_ = ln_ >> 5;
    L.rec_a = lb + L_SORT + (2 * (g8_ >> 2) + (g8_ & 1)) * 16 + ((g8_ >> 1) & 1) * 8;
    L.cA = (uint32_t)((p8_ ^ (g8_ >> 1)) * 16); L.cB = L.cA ^ 64u;
    L.stage = lb + L_STAGE + wv * 8192;
    const uint32_t af = L.stage + r31_ * 128 + ((h_ ^ ((r31_ >> 1) & 7)) * 16);
#pragma unroll
    for (int s = 0; s < 4; s++) L.afrag[s] = af ^ (32u * s);
    const int o = r31_ >> 4, m1 = (r31_ >> 3) & 1, hh = (r31_ >> 2) & 1, m0 = (r31_ >> 1) & 1, ab = r31_ & 1;
    L.erec  = lb + L_SORT + (4 * (4 * o + 2 * hh + ab) + 2 * m0 + m1) * 4;
    L.esel  = h_ == 0 ? 0x03020302u : 0x0c0c0c0cu;
    L.qn_a  = lb + L_QN + h_ * 4;
    L.col4  = lb + L_EA + r31_ * 4;
    return L;
  };

  int take = 32;
  poll_segments(tb >> 2);
  TS(4);
  load_recs(R, sg, ws, tb >> 2, tb & 3, 0);
  post_total(0, tb & 3, lb_);
  {
    uint32_t v = (tb & 3) == 0 ? (sg[0] >> 16) + (sg[1] >> 16) : 0u;
    v = wave_scan_add(v);
    if (lb_ == 63) __hip_atomic_fetch_add(&misc[M_ALL], v, RLX_WG);
  }
  if (wv & 1) weights(IC<0>{}); else weights(IC<1>{});
#pragma unroll
  for (int c = 0; c < 2; c++) {
    const float cv = bf2f(c ? c1 : c0) * NLOG2E;
    const uint32_t chi = cvtpk(cv, 0.f);
    const uint32_t clo = cvtpk(cv - bf2f(chi), 0.f);
    const u32x4 cu = {h == 0 ? (chi | (clo << 16)) : 0u, 0u, 0u, 0u};
    W.c[c] = __builtin_bit_cast(bf16x8, cu);
  }
  WAIT_RECS(R);
  TS(5);
  rank(tb, 0, take);
  LDS_BARRIER();
  if (w == 0 && tb == 0) __hip_atomic_store((gu64*)(ws + WS_NONCE), (((u64)xn[1] << 32) | xn[0]) + 1ull, RLX_AGENT);
  if (__builtin_amdgcn_readfirstlane(misc[M_TOT]) > (uint32_t)TAKE32_MAX) {
    LDS_BARRIER();
    if (tb < 128) *(uint32_t*)(smem + L_CNT + tb * 4) = 0;
    LDS_BARRIER();
    take = 16;
    rank(tb, 0, take);
    LDS_BARRIER();
  }
  if (wv == 0) scan(lb_);
  else if (wv == 1) {
    gu64* gt = (gu64*)(ws + WS_BND) + 4 * lb_;
    uint32_t mb, mv_, me; float sq;
    for (unsigned spins = 0;; spins++) {
      bool ok = true; mb = mv_ = me = 0; sq = 0.f;
#pragma unroll
      for (int i = 0; i < 4; i++) {
        const u64 v0 = __hip_atomic_load(gt + 256 * i, RLX_AGENT), v1 = __hip_atomic_load(gt + 256 * i + 1, RLX_AGENT), v2 = __hip_atomic_load(gt + 256 * i + 2, RLX_AGENT);
        ok &= (uint32_t)(v0 >> 32) == tag32 && (uint32_t)(v1 >> 32) == tag32 && (uint32_t)(v2 >> 32) == tag32;
        mb = max(mb, (uint32_t)v0 << 16); mv_ = max(mv_, (uint32_t)v0 & 0xffff0000u);
        me = max(me, (uint32_t)v2); sq += __uint_as_float((uint32_t)v1);
      }
      if (__all(ok)) break;
      if (spins > SPIN_CAP) break;
      __builtin_amdgcn_s_sleep(8);
    }
    mb = wave_max_u32(mb); mv_ = wave_max_u32(mv_); me = wave_max_u32(me); sq = wave_sum_f32(sq);
    const float mv = __uint_as_float(mv_);
    const float yb = (__uint_as_float(misc[M_XAM]) + __uint_as_float(mb) + __uint_as_float(me) * __uint_as_float(misc[M_MX + 3])) * 1.01f;
    const float all = (float)(misc[M_ALL] + 4u);
    const int p1 = (int)(__float_as_uint(4.1f * mv) >> 23) - 127 + 2, p2 = (int)(__float_as_uint(all * mv * 1.01f) >> 23) - 127 + 1 - 8;
    const int p = max(max(p1, p2), -80);
    const float dl = __uint_as_float((uint32_t)(p - 23 + 127) << 23);
    const bool fine = dl * 4096.f <= __builtin_amdgcn_sqrtf(sq * __builtin_amdgcn_rcpf(64.f * (float)N)) * 0.99f || mv == 0.f;
    if (lb_ == 63) {
      misc[M_FAST] = (yb <= 28.f) && (mv <= 1.0e12f) && fine;
      misc[M_EXP] = (uint32_t)(p + 127);
    }
  } else {
    for (int i = tb - 128; i < 99 * 16; i += 384) *(u32x4*)(smem + L_AGG + i * 16) = u32x4{0, 0, 0, 0};
  }
  LDS_BARRIER();
  TS(6);
  const bool fast = __builtin_amdgcn_readfirstlane(misc[M_FAST]);
  float delta; f32x2 magic;
  {
    const uint32_t pexp = __builtin_amdgcn_readfirstlane(misc[M_EXP]);
    delta = __uint_as_float((pexp - 23u) << 23);
    const float mg = __uint_as_float((pexp << 23) | 0x400000u);
    magic = f32x2{mg, mg};
  }
  scatter(tb);
  int again = __builtin_amdgcn_readfirstlane(misc[M_MORE]);
  LDS_BARRIER();
  int ntile = __builtin_amdgcn_readfirstlane(misc[M_NTILE]);
  if (fast) {
    const Lane L = lane_consts(tb);
    issue_tile(L, min(wv, ntile), 0, srd);
    for (int i = tb; i < NPB * 16; i += 512) {
      f32x4 v = *(const f32x4*)(smem + L_EA + i * 16);
      v[0] = __builtin_amdgcn_exp2f(v[0]); v[1] = __builtin_amdgcn_exp2f(v[1]); v[2] = __builtin_amdgcn_exp2f(v[2]); v[3] = __builtin_amdgcn_exp2f(v[3]);
      *(f32x4*)(smem + L_EA + i * 16) = v;
    }
    LDS_BARRIER();
    TS(7);
    if (wv < 4) edge_loop_fast<false>(L, W, wv, ntile, magic, srd, (unsigned long long*)(ws + WS_DBG));
    else        edge_loop_fast<true>(L, W, wv, ntile, magic, srd, (unsigned long long*)(ws + WS_DBG));
  } else {
    const Lane L = lane_consts(thread_id());
    edge_loop_plain<false>(L, W, wv, ntile, magic, srd);
  }
  while (again) {
    done += take;
    const int t2 = thread_id();
    poll_segments(t2 >> 2);
    load_recs(R, sg, ws, t2 >> 2, t2 & 3, done);
    WAIT_RECS(R);
    LDS_BARRIER();
    if (t2 < 128) *(uint32_t*)(smem + L_CNT + t2 * 4) = 0;
    if (t2 == 0) { misc[M_MORE] = 0; misc[M_TOT] = 0; }
    LDS_BARRIER();
    post_total(done, t2 & 3, t2 & 63);
    LDS_BARRIER();
    take = __builtin_amdgcn_readfirstlane(misc[M_TOT]) <= (uint32_t)TAKE32_MAX ? 32 : 16;
    rank(t2, done, take);
    LDS_BARRIER();
    if (wv == 0) scan(t2 & 63);
    LDS_BARRIER();
    scatter(t2);
    again = __builtin_amdgcn_readfirstlane(misc[M_MORE]);
    LDS_BARRIER();
    ntile = __builtin_amdgcn_readfirstlane(misc[M_NTILE]);
    const Lane L = lane_consts(t2);
    if (fast) edge_loop_plain<true>(L, W, wv, ntile, magic, srd);
    else      edge_loop_plain<false>(L, W, wv, ntile, magic, srd);
  }
  LDS_BARRIER();

  const int te = thread_id();
  for (int i = te; i < NPB * 8; i += 512) {
    const int node = b * NPB + (i >> 3);
    if (node < N) {
      float y[8];
      const uint32_t bias = *(const uint32_t*)(smem + L_NQ + (i >> 3) * 4) * __float_as_uint(magic[0]);
#pragma unroll
      for (int k = 0; k < 2; k++) {
        const u32x4 a4 = *(const u32x4*)(smem + L_AGG + i * 32 + k * 16);
        const f32x4 u4 = *(const f32x4*)(smem + L_XU + i * 32 + k * 16);
#pragma unroll
        for (int j = 0; j < 4; j++) {
          const float v = u4[j] + (fast ? (float)(int)(a4[j] - bias) * delta : __uint_as_float(a4[j]));
          y[4 * k + j] = v * __builtin_amdgcn_rcpf(1.f + __builtin_amdgcn_exp2f(v * NLOG2E));
        }
      }
      const u32x4 o = {cvtpk(y[0], y[1]), cvtpk(y[2], y[3]), cvtpk(y[4], y[5]), cvtpk(y[6], y[7])};
      *(u32x4*)(out + (size_t)node * 64 + (i & 7) * 8) = o;
    }
  }
  TS_OUT(256 + blockIdx.x);
}

}

extern "C" void kernel_launch(void* const* d_in, const int* in_sizes, int n_in,
                              void* d_out, int out_size, void* d_ws, size_t ws_size,
                              hipStream_t stream) {
  const uint16_t* x  = (const uint16_t*)d_in[0];
  const uint16_t* ea = (const uint16_t*)d_in[1];
  const int*      src = (const int*)d_in[2];
  const int*      dst = (const int*)d_in[3];
  const uint16_t* uw = (const uint16_t*)d_in[4];
  const uint16_t* vw = (const uint16_t*)d_in[5];
  const uint16_t* gw = (const uint16_t*)d_in[6];
  const int N = in_sizes[0] / 64, E = in_sizes[2];
  char* ws = (char*)d_ws;
  (void)hipFuncSetAttribute((const void*)k_mpnn, hipFuncAttributeMaxDynamicSharedMemorySize, L_TOTAL);
  k_mpnn<<<NWG, 512, L_TOTAL, stream>>>(src, dst, ea, x, uw, vw, gw, ws, (uint16_t*)d_out, N, E);
}
```

```cpp
#include <hip/hip_runtime.h>
#include <hip/hip_bf16.h>
#include <stdint.h>

#pragma clang diagnostic ignored "-Wint-to-pointer-cast"
#pragma clang diagnostic ignored "-Wpointer-to-int-cast"
namespace {

typedef float    f32x2  __attribute__((ext_vector_type(2)));
typedef float    f32x4  __attribute__((ext_vector_type(4)));
typedef float    f32x16 __attribute__((ext_vector_type(16)));
typedef short    bf16x8 __attribute__((ext_vector_type(8)));
typedef short    s16x4  __attribute__((ext_vector_type(4)));
typedef uint32_t u32x2  __attribute__((ext_vector_type(2)));
typedef uint32_t u32x4  __attribute__((ext_vector_type(4)));
typedef unsigned long long u64;
typedef __attribute__((address_space(1))) unsigned long long gu64;
#define LDSP(T) __attribute__((address_space(3))) T*
#define DI __device__ __forceinline__
#define RLX_AGENT __ATOMIC_RELAXED, __HIP_MEMORY_SCOPE_AGENT
#define RLX_WG    __ATOMIC_RELAXED, __HIP_MEMORY_SCOPE_WORKGROUP

constexpr int   NWG = 256;
constexpr int   NPB = 98;
constexpr int   CH  = 3128;
constexpr int   CHB = 976;
constexpr float NLOG2E = -1.4426950408889634f;
constexpr int   AUX_SC1 = 16;
constexpr unsigned SPIN_CAP = 1u << 18;
static_assert(4 * CHB >= CH + 3 * NWG && (CHB * 20) % 16 == 0, "blocks");

constexpr size_t WS_NONCE = 0;
constexpr size_t WS_SEG = 128;
constexpr size_t WS_BND = WS_SEG + (size_t)NWG * NWG * 8;
constexpr size_t WS_REC = WS_BND + (size_t)NWG * 32;
constexpr size_t WS_DBG = (WS_REC + (size_t)NWG * CHB * 20 + 1024 + 127) & ~(size_t)127;
static_assert(WS_REC % 128 == 0, "ws align");

constexpr int SORT_CAP = 4528;
constexpr int TAKE32_MAX = SORT_CAP - 3 * NPB - 64 - 8;
constexpr int L_CNT    = 0;
constexpr int L_OFF    = L_CNT + 512;
constexpr int L_NQ     = L_OFF + 512;
constexpr int L_MISC   = L_NQ + 512;
constexpr int L_QN     = L_MISC + 512;
constexpr int L_EA     = (L_QN + SORT_CAP / 2 + 15) & ~15;
constexpr int L_XU     = L_EA + 99 * 256;
constexpr int L_AGG    = (L_XU + 98 * 256 + 127) & ~127;
constexpr int L_STAGE  = L_AGG + 99 * 256;
constexpr int L_SORT   = L_STAGE + 8 * 8192;
constexpr int L_TOTAL  = L_SORT + SORT_CAP * 4;
static_assert(L_TOTAL <= 163840, "LDS");
static_assert((L_STAGE & 127) == 0 && (L_SORT & 15) == 0 && TAKE32_MAX >= 4096, "align");
constexpr int A_HIST = L_STAGE;
constexpr int A_BASE = A_HIST + 1024;
constexpr int A_ST   = A_BASE + 1024;
constexpr int A_W    = (A_ST + CHB * 20 + 127) & ~127;
constexpr int P_X    = L_AGG;
static_assert(A_W + 4 * 8192 <= L_SORT && P_X + 128 * 128 <= L_STAGE, "phase A images");
constexpr uint32_t DUMMY = 0x0000FFFFu;
constexpr uint32_t QN_DUMMY = 98u * 256u;
constexpr int M_MORE = 0, M_NTILE = 1, M_T4 = 2;
constexpr int M_CW = 3, M_CB = 4;
constexpr int M_TOT = 10, M_ALL = 11, M_XAM = 12;
constexpr int M_FAST = 13, M_EXP = 14;
constexpr int M_MX = 16;
constexpr int M_SQ = 24;

DI float    bf2f(uint32_t v16) { return __uint_as_float(v16 << 16); }
DI uint32_t cvtpk(float lo, float hi) {
  typedef __bf16 bf16x2 __attribute__((ext_vector_type(2)));
  return __builtin_bit_cast(uint32_t, __builtin_convertvector(f32x2{lo, hi}, bf16x2));
}
DI f32x16   mfma(bf16x8 a, bf16x8 b, f32x16 c) { return __builtin_amdgcn_mfma_f32_32x32x16_bf16(a, b, c, 0, 0, 0); }
DI int      crow(int r, int h) { return (r & 3) + 8 * (r >> 2) + 4 * h; }
DI uint32_t absbits(float v) { return __float_as_uint(v) & 0x7fffffffu; }

template <int CTRL, int RM> DI uint32_t dpp0(uint32_t v) { return (uint32_t)__builtin_amdgcn_update_dpp(0, (int)v, CTRL, RM, 0xf, false); }
DI uint32_t wave_scan_add(uint32_t v) {
  v += dpp0<0x111, 0xf>(v); v += dpp0<0x112, 0xf>(v); v += dpp0<0x114, 0xf>(v); v += dpp0<0x118, 0xf>(v);
  v += dpp0<0x142, 0xa>(v); v += dpp0<0x143, 0xc>(v);
  return v;
}
DI uint32_t wave_max_u32(uint32_t v) {
  v = max(v, dpp0<0x111, 0xf>(v)); v = max(v, dpp0<0x112, 0xf>(v)); v = max(v, dpp0<0x114, 0xf>(v)); v = max(v, dpp0<0x118, 0xf>(v));
  v = max(v, dpp0<0x142, 0xa>(v)); v = max(v, dpp0<0x143, 0xc>(v));
  return v;
}
DI float wave_sum_f32(float s) {
  s += __uint_as_float(dpp0<0x111, 0xf>(__float_as_uint(s))); s += __uint_as_float(dpp0<0x112, 0xf>(__float_as_uint(s)));
  s += __uint_as_float(dpp0<0x114, 0xf>(__float_as_uint(s))); s += __uint_as_float(dpp0<0x118, 0xf>(__float_as_uint(s)));
  s += __uint_as_float(dpp0<0x142, 0xa>(__float_as_uint(s))); s += __uint_as_float(dpp0<0x143, 0xc>(__float_as_uint(s)));
  return s;
}
#define SB() __builtin_amdgcn_sched_barrier(0)
#define LDS_BARRIER() asm volatile("s_waitcnt lgkmcnt(0)\n\ts_barrier" ::: "memory")
#define WAIT_VM(N) asm volatile("s_waitcnt vmcnt(" #N ")" ::: "memory")

DI uint32_t arrive(uint32_t* word, int lane) {
  asm volatile("s_waitcnt lgkmcnt(0)" ::: "memory");
  uint32_t v = 0;
  if (lane == 0) v = __hip_atomic_fetch_add(word, 1u, RLX_WG);
  return __builtin_amdgcn_readfirstlane(v);
}
DI void await_lds(uint32_t* word, uint32_t want) {
  for (unsigned spins = 0; spins < SPIN_CAP; spins++) {
    if (__builtin_amdgcn_readfirstlane(__hip_atomic_load(word, RLX_WG)) >= want) break;
    __builtin_amdgcn_s_sleep(1);
  }
  asm volatile("" ::: "memory");
}

DI void dma16(uint32_t ldsaddr, uint32_t voff, __amdgpu_buffer_rsrc_t srd) {
  asm volatile("s_mov_b32 m0, %0\n\ts_nop 0\n\tbuffer_load_dwordx4 %1, %2, 0 offen lds" :: "s"(ldsaddr), "v"(voff), "s"(srd) : "memory");
}
DI bf16x8 tr_frag(uint32_t addr, int four_rows) {
  const s16x4 lo = __builtin_amdgcn_ds_read_tr16_b64_v4i16((LDSP(s16x4))addr);
  const s16x4 hi = __builtin_amdgcn_ds_read_tr16_b64_v4i16((LDSP(s16x4))(addr + four_rows));
  return (bf16x8)__builtin_shufflevector(lo, hi, 0, 1, 2, 3, 4, 5, 6, 7);
}

#define TS(i) do { } while (0)
#define TS_DECL do { } while (0)
#define TS_OUT(slot) do { } while (0)
#define TS_PUB(i) do { } while (0)

DI f32x2 pair(const f32x16& v, int i) { f32x2 r = {v[i], v[i + 1]}; return r; }
DI f32x2 exp2x2(f32x2 v) { f32x2 r = {__builtin_amdgcn_exp2f(v[0]), __builtin_amdgcn_exp2f(v[1])}; return r; }
DI f32x2 rcpx2(f32x2 v) { f32x2 r = {__builtin_amdgcn_rcpf(v[0]), __builtin_amdgcn_rcpf(v[1])}; return r; }
DI f32x2 fma2(f32x2 a, f32x2 b, f32x2 c) { return __builtin_elementwise_fma(a, b, c); }

struct Wts { bf16x8 b[2][4], v[2][4], c[2]; };
template <int V> struct IC { static constexpr int value = V; };
struct Acc { f32x16 z, hv; };

struct Frac { f32x2 n[2], d[2]; };
DI void exp_all(f32x16& z) {
#pragma unroll
  for (int i = 0; i < 16; i++) z[i] = __builtin_amdgcn_exp2f(z[i]);
}
DI void combine(Frac& F, const f32x16& u, const f32x16& hv, int o, f32x2 ea) {
  const f32x2 one = {1.f, 1.f};
  const f32x2 h0 = pair(hv, 8 * o), h1 = pair(hv, 8 * o + 2), h2 = pair(hv, 8 * o + 4), h3 = pair(hv, 8 * o + 6);
  const f32x2 a0 = fma2(ea, pair(u, 8 * o), one),     a1 = fma2(ea, pair(u, 8 * o + 2), one);
  const f32x2 a2 = fma2(ea, pair(u, 8 * o + 4), one), a3 = fma2(ea, pair(u, 8 * o + 6), one);
  const f32x2 d01 = a0 * a1, n01 = fma2(h1, a0, h0 * a1);
  const f32x2 d23 = a2 * a3, n23 = fma2(h3, a2, h2 * a3);
  F.d[o] = d01 * d23; F.n[o] = fma2(n23, d01, n01 * d23);
}
template <bool EXPEA> DI f32x2 octet_plain(const Acc& A, int o, f32x2 ea) {
  const f32x2 one = {1.f, 1.f};
  const f32x16& z = A.z; const f32x16& hv = A.hv;
  const f32x2 h0 = pair(hv, 8 * o), h1 = pair(hv, 8 * o + 2), h2 = pair(hv, 8 * o + 4), h3 = pair(hv, 8 * o + 6);
  f32x2 r[4];
#pragma unroll
  for (int j = 0; j < 4; j++)
    r[j] = EXPEA ? rcpx2(fma2(ea, exp2x2(pair(z, 8 * o + 2 * j)), one)) : rcpx2(one + exp2x2(pair(z, 8 * o + 2 * j) + ea));
  const f32x2 r0 = r[0], r1 = r[1], r2 = r[2], r3 = r[3];
  return (h0 * r0 + h1 * r1) + (h2 * r2 + h3 * r3);
}

DI uint32_t madlo(uint32_t v, uint32_t mul, uint32_t add) {
  uint32_t r; asm("v_mad_u32_u16 %0, %1, %2, %3" : "=v"(r) : "v"(v), "s"(mul), "v"(add)); return r;
}
template <int MUL> DI uint32_t madlo_i(uint32_t v, uint32_t add) {
  uint32_t r; asm("v_mad_u32_u16 %0, %1, %3, %2" : "=v"(r) : "v"(v), "v"(add), "i"(MUL)); return r;
}
template <int MUL> DI uint32_t madhi_i(uint32_t v, uint32_t add) {
  uint32_t r; asm("v_mad_u32_u16 %0, %1, %3, %2 op_sel:[1,0,0,0]" : "=v"(r) : "v"(v), "v"(add), "i"(MUL)); return r;
}
template <int K> DI uint32_t byte_x4(uint32_t v) {
  uint32_t r;
  if (K == 0) asm("v_lshlrev_b32_sdwa %0, 2, %1 dst_sel:DWORD dst_unused:UNUSED_PAD src0_sel:DWORD src1_sel:BYTE_0" : "=v"(r) : "v"(v));
  if (K == 1) asm("v_lshlrev_b32_sdwa %0, 2, %1 dst_sel:DWORD dst_unused:UNUSED_PAD src0_sel:DWORD src1_sel:BYTE_1" : "=v"(r) : "v"(v));
  if (K == 2) asm("v_lshlrev_b32_sdwa %0, 2, %1 dst_sel:DWORD dst_unused:UNUSED_PAD src0_sel:DWORD src1_sel:BYTE_2" : "=v"(r) : "v"(v));
  if (K == 3) asm("v_lshlrev_b32_sdwa %0, 2, %1 dst_sel:DWORD dst_unused:UNUSED_PAD src0_sel:DWORD src1_sel:BYTE_3" : "=v"(r) : "v"(v));
  return r;
}
DI uint32_t byte_x4(uint32_t v, int k) { return k == 0 ? byte_x4<0>(v) : (k == 1 ? byte_x4<1>(v) : (k == 2 ? byte_x4<2>(v) : byte_x4<3>(v))); }
DI void lds_add_u32(uint32_t addr, float v) { __hip_atomic_fetch_add((LDSP(uint32_t))addr, __float_as_uint(v), RLX_WG); }
DI void lds_add_f32(uint32_t addr, float v) { __hip_atomic_fetch_add((LDSP(float))addr, v, RLX_WG); }

struct Lane {
  uint32_t rec_a;
  uint32_t cA, cB;
  uint32_t afrag[4];
  uint32_t erec;
  uint32_t esel;
  uint32_t qn_a;
  uint32_t col4;
  uint32_t stage;
};
struct Tile { bf16x8 a[4], ef; };
struct Quads { uint32_t q0, q1; };

DI void issue_tile(const Lane& L, int tile, int buf, __amdgpu_buffer_rsrc_t srd) {
  const u32x2 r0 = *(LDSP(const u32x2))(L.rec_a + tile * 128);
  const u32x2 r1 = *(LDSP(const u32x2))(L.rec_a + tile * 128 + 64);
  const uint32_t a0 = madlo(r0[0], 128u, L.cA), a1 = madlo(r0[1], 128u, L.cB);
  const uint32_t a2 = madlo(r1[0], 128u, L.cA), a3 = madlo(r1[1], 128u, L.cB);
  const uint32_t sb = L.stage + buf * 4096;
  dma16(sb, a0, srd); dma16(sb + 1024, a1, srd); dma16(sb + 2048, a2, srd); dma16(sb + 3072, a3, srd);
}
template <int BUF> DI void load_tile(Tile& T, Quads& Q, const Lane& L, int tile) {
  const uint32_t erec = *(LDSP(const uint32_t))(L.erec + tile * 128);
  Q.q0 = *(LDSP(const uint32_t))(L.qn_a + tile * 16);
  Q.q1 = *(LDSP(const uint32_t))(L.qn_a + tile * 16 + 8);
#pragma unroll
  for (int s = 0; s < 4; s++) T.a[s] = *(LDSP(const bf16x8))(L.afrag[s] + BUF * 4096);
  const u32x4 eu = {__builtin_amdgcn_perm(erec, erec, L.esel), 0u, 0u, 0u};
  T.ef = __builtin_bit_cast(bf16x8, eu);
}
DI void mma(Acc& A, const Tile& T, const Wts& W, int c) {
  f32x16 zero;
#pragma unroll
  for (int i = 0; i < 16; i++) zero[i] = 0.f;
  A.z  = mfma(T.a[0], W.b[c][0], zero);
  A.hv = mfma(T.a[0], W.v[c][0], zero);
#pragma unroll
  for (int s = 1; s < 4; s++) { A.z = mfma(T.a[s], W.b[c][s], A.z); A.hv = mfma(T.a[s], W.v[c][s], A.hv); }
  A.z  = mfma(T.ef, W.c[c], A.z);
}
struct Gate { f32x2 e0, e1; };
struct Rows { uint32_t a[4]; };
DI void rows_of(Rows& R, const Lane& L, const Quads& Q) {
  R.a[0] = madlo_i<1>(Q.q0, L.col4); R.a[1] = madhi_i<1>(Q.q0, L.col4); R.a[2] = madlo_i<1>(Q.q1, L.col4); R.a[3] = madhi_i<1>(Q.q1, L.col4);
}
DI void load_gate(Gate& G, const Rows& R, int c) {
  G.e0 = f32x2{*(LDSP(const float))(R.a[0] + 128 * c), *(LDSP(const float))(R.a[1] + 128 * c)};
  G.e1 = f32x2{*(LDSP(const float))(R.a[2] + 128 * c), *(LDSP(const float))(R.a[3] + 128 * c)};
}
constexpr int AGG_REL = L_AGG - L_EA;
#define PY 2
#define PYY PY
#define PXO 0
#define PXY 1
#define PRIO_HI() do { if (YOUNG) __builtin_amdgcn_s_setprio(PYY); else __builtin_amdgcn_s_setprio(PY); SB(); } while (0)
#define PRIO_LO() do { if (YOUNG) __builtin_amdgcn_s_setprio(PXY); else __builtin_amdgcn_s_setprio(PXO); SB(); } while (0)
#define PIN(x) asm volatile("" : "+v"(x))
#define EX(z, i) z[i] = __builtin_amdgcn_exp2f(z[i])
template <class F0, class F1>
DI void section_x(Acc& An, const Tile& T, const Wts& W, int cn, f32x16& z, Frac& F, f32x2& r0, f32x2& r1,
                  Gate& G, const Rows& Rg, int cg, const Rows& Ra, int ca, f32x2 m0, f32x2 m1, F0 extra0, F1 extra1) {
  f32x16 zero;
#pragma unroll
  for (int i = 0; i < 16; i++) zero[i] = 0.f;
  An.z = mfma(T.a[0], W.b[cn][0], zero); PIN(An.z);
  load_gate(G, Rg, cg);
  EX(z, 0); EX(z, 1); EX(z, 2);
  SB();
  An.hv = mfma(T.a[0], W.v[cn][0], zero); PIN(An.hv);
  lds_add_u32(Ra.a[0] + AGG_REL + 128 * ca, m0[0]); lds_add_u32(Ra.a[1] + AGG_REL + 128 * ca, m0[1]);
  EX(z, 3); EX(z, 4);
  SB();
  An.z = mfma(T.a[1], W.b[cn][1], An.z); PIN(An.z);
  lds_add_u32(Ra.a[2] + AGG_REL + 128 * ca, m1[0]); lds_add_u32(Ra.a[3] + AGG_REL + 128 * ca, m1[1]);
  EX(z, 5); EX(z, 6);
  SB();
  An.hv = mfma(T.a[1], W.v[cn][1], An.hv); PIN(An.hv);
  EX(z, 7); EX(z, 8);
  extra0();
  SB();
  An.z = mfma(T.a[2], W.b[cn][2], An.z); PIN(An.z);
  EX(z, 9); EX(z, 10);
  extra1();
  SB();
  An.hv = mfma(T.a[2], W.v[cn][2], An.hv); PIN(An.hv);
  EX(z, 11); EX(z, 12); EX(z, 13);
  SB();
  An.z = mfma(T.a[3], W.b[cn][3], An.z); PIN(An.z);
  EX(z, 14); EX(z, 15);
  SB();
  An.hv = mfma(T.a[3], W.v[cn][3], An.hv); PIN(An.hv);
  r0[0] = __builtin_amdgcn_rcpf(F.d[0][0]); r0[1] = __builtin_amdgcn_rcpf(F.d[0][1]); PIN(r0);
  SB();
  An.z = mfma(T.ef, W.c[cn], An.z); PIN(An.z);
  r1[0] = __builtin_amdgcn_rcpf(F.d[1][0]); r1[1] = __builtin_amdgcn_rcpf(F.d[1][1]); PIN(r1);
  PIN(G.e0); PIN(G.e1);
  SB();
}
DI void section_y(Frac& F, f32x2& m0, f32x2& m1, f32x2 r0, f32x2 r1, const f32x16& u, const f32x16& hv, const Gate& G, f32x2 magic) {
  const f32x2 one = {1.f, 1.f};
  f32x2 a[2][4], d01[2], d23[2], n01[2], n23[2];
  a[0][1] = fma2(G.e0, pair(u, 2), one);  a[1][1] = fma2(G.e1, pair(u, 10), one); SB();
  a[0][3] = fma2(G.e0, pair(u, 6), one);  a[1][3] = fma2(G.e1, pair(u, 14), one); SB();
  a[0][0] = fma2(G.e0, pair(u, 0), one);  a[1][0] = fma2(G.e1, pair(u, 8), one); SB();
  a[0][2] = fma2(G.e0, pair(u, 4), one);  a[1][2] = fma2(G.e1, pair(u, 12), one); SB();
  n01[0] = pair(hv, 0) * a[0][1]; n01[1] = pair(hv, 8) * a[1][1]; SB();
  n23[0] = pair(hv, 4) * a[0][3]; n23[1] = pair(hv, 12) * a[1][3]; SB();
  m0 = fma2(F.n[0], r0, magic); m1 = fma2(F.n[1], r1, magic); SB();
  d01[0] = a[0][0] * a[0][1]; d01[1] = a[1][0] * a[1][1]; SB();
  d23[0] = a[0][2] * a[0][3]; d23[1] = a[1][2] * a[1][3]; SB();
  n01[0] = fma2(pair(hv, 2), a[0][0], n01[0]); n01[1] = fma2(pair(hv, 10), a[1][0], n01[1]); SB();
  n23[0] = fma2(pair(hv, 6), a[0][2], n23[0]); n23[1] = fma2(pair(hv, 14), a[1][2], n23[1]); SB();
  F.d[0] = d01[0] * d23[0]; F.d[1] = d01[1] * d23[1]; SB();
  n01[0] = n01[0] * d23[0]; n01[1] = n01[1] * d23[1]; SB();
  n23[0] = n23[0] * d01[0]; n23[1] = n23[1] * d01[1]; SB();
  F.n[0] = n01[0] + n23[0]; F.n[1] = n01[1] + n23[1];
}
struct Pipe {
  Tile T; Acc A0; Rows R, RP;
  Frac F;
  f32x2 m0, m1;
  u32x2 ra, rb;
};
template <int BUF, bool YOUNG>
DI void step_fast(const Lane& L, const Wts& W, Pipe& P, int t1, int t3, f32x2 magic, __amdgpu_buffer_rsrc_t srd) {
  Acc A1; Gate G; f32x2 r0, r1; Quads QN; Rows RN; uint32_t erec, da[4];
  const uint32_t sb = L.stage + BUF * 4096;
  section_x(A1, P.T, W, 1, P.A0.z, P.F, r0, r1, G, P.R, 0, P.RP, 0, P.m0, P.m1,
            [&]() { da[0] = madlo(P.ra[0], 128u, L.cA); da[1] = madlo(P.ra[1], 128u, L.cB); },
            [&]() { da[2] = madlo(P.rb[0], 128u, L.cA); da[3] = madlo(P.rb[1], 128u, L.cB); });
  asm volatile("s_waitcnt vmcnt(0)" ::: "memory");
#pragma unroll
  for (int s = 0; s < 4; s++) P.T.a[s] = *(LDSP(const bf16x8))(L.afrag[s] + (BUF ^ 1) * 4096);
  erec  = *(LDSP(const uint32_t))(L.erec + t1 * 128);
  QN.q0 = *(LDSP(const uint32_t))(L.qn_a + t1 * 16);
  QN.q1 = *(LDSP(const uint32_t))(L.qn_a + t1 * 16 + 8);
  dma16(sb, da[0], srd);
  dma16(sb + 1024, da[1], srd);
  SB();
  PRIO_HI();
  section_y(P.F, P.m0, P.m1, r0, r1, P.A0.z, P.A0.hv, G, magic);
  SB();
  PRIO_LO();
  dma16(sb + 2048, da[2], srd);
  dma16(sb + 3072, da[3], srd);
  SB();
  section_x(P.A0, P.T, W, 0, A1.z, P.F, r0, r1, G, P.R, 1, P.RP, 1, P.m0, P.m1,
            [&]() { rows_of(RN, L, QN); },
            [&]() { const u32x4 eu = {__builtin_amdgcn_perm(erec, erec, L.esel), 0u, 0u, 0u}; P.T.ef = __builtin_bit_cast(bf16x8, eu); });
  P.ra = *(LDSP(const u32x2))(L.rec_a + t3 * 128);
  P.rb = *(LDSP(const u32x2))(L.rec_a + t3 * 128 + 64);
  SB();
  PRIO_HI();
  section_y(P.F, P.m0, P.m1, r0, r1, A1.z, A1.hv, G, magic);
  P.RP = P.R; P.R = RN;
  SB();
  PRIO_LO();
}
template <bool YOUNG> DI void edge_loop_fast(const Lane& L, const Wts& W, int wv, int ntile, f32x2 magic, __amdgpu_buffer_rsrc_t srd, unsigned long long* dbg) {
#define ST(i) do { } while (0)
  int tile = wv;
  if (tile < ntile) {
    Pipe P; Quads Q;
    if (YOUNG) __builtin_amdgcn_s_setprio(PXY); else __builtin_amdgcn_s_setprio(PXO);
    ST(0);
    asm volatile("s_waitcnt vmcnt(0)" ::: "memory");
    ST(1);
    issue_tile(L, min(tile + 8, ntile), 1, srd);
    load_tile<0>(P.T, Q, L, tile);
    rows_of(P.R, L, Q);
    mma(P.A0, P.T, W, 0);
    Q.q0 = Q.q1 = (98u * 256u) * 0x10001u;
    rows_of(P.RP, L, Q);
    P.F.n[0] = P.F.n[1] = f32x2{0.f, 0.f}; P.m0 = P.m1 = magic;
    P.F.d[0] = P.F.d[1] = f32x2{1.f, 1.f};
    { const int t2 = min(tile + 16, ntile);
      P.ra = *(LDSP(const u32x2))(L.rec_a + t2 * 128); P.rb = *(LDSP(const u32x2))(L.rec_a + t2 * 128 + 64); }
    ST(2);
    while (true) {
      step_fast<0, YOUNG>(L, W, P, min(tile + 8, ntile), min(tile + 24, ntile), magic, srd);
      tile += 8;
      if (tile >= ntile) break;
      step_fast<1, YOUNG>(L, W, P, min(tile + 8, ntile), min(tile + 24, ntile), magic, srd);
      tile += 8;
      if (tile >= ntile) break;
    }
    ST(3);
    const f32x2 z0 = fma2(P.F.n[0], rcpx2(P.F.d[0]), magic), z1 = fma2(P.F.n[1], rcpx2(P.F.d[1]), magic);
    lds_add_u32(P.RP.a[0] + AGG_REL, P.m0[0]); lds_add_u32(P.RP.a[1] + AGG_REL, P.m0[1]);
    lds_add_u32(P.RP.a[2] + AGG_REL, P.m1[0]); lds_add_u32(P.RP.a[3] + AGG_REL, P.m1[1]);
    lds_add_u32(P.RP.a[0] + AGG_REL + 128, z0[0]); lds_add_u32(P.RP.a[1] + AGG_REL + 128, z0[1]);
    lds_add_u32(P.RP.a[2] + AGG_REL + 128, z1[0]); lds_add_u32(P.RP.a[3] + AGG_REL + 128, z1[1]);
    asm volatile("s_waitcnt vmcnt(0)" ::: "memory");
    __builtin_amdgcn_s_setprio(0);
    ST(4);
  }
}
template <bool EXPEA> DI void edge_loop_plain(const Lane& L, const Wts& W, int wv, int ntile, f32x2 magic, __amdgpu_buffer_rsrc_t srd) {
  for (int tile = wv; tile < ntile; tile += 8) {
    Tile T; Quads Q;
    issue_tile(L, tile, 0, srd);
    asm volatile("s_waitcnt vmcnt(0)" ::: "memory");
    load_tile<0>(T, Q, L, tile);
#pragma unroll
    for (int c = 0; c < 2; c++) {
      Acc A; Gate G; Rows Rw;
      rows_of(Rw, L, Q);
      load_gate(G, Rw, c);
      mma(A, T, W, c);
      const f32x2 m0 = octet_plain<EXPEA>(A, 0, G.e0), m1 = octet_plain<EXPEA>(A, 1, G.e1);
      if (EXPEA) {
        const f32x2 i0 = m0 + magic, i1 = m1 + magic;
        lds_add_u32(Rw.a[0] + AGG_REL + 128 * c, i0[0]); lds_add_u32(Rw.a[1] + AGG_REL + 128 * c, i0[1]);
        lds_add_u32(Rw.a[2] + AGG_REL + 128 * c, i1[0]); lds_add_u32(Rw.a[3] + AGG_REL + 128 * c, i1[1]);
      } else {
        lds_add_f32(Rw.a[0] + AGG_REL + 128 * c, m0[0]); lds_add_f32(Rw.a[1] + AGG_REL + 128 * c, m0[1]);
        lds_add_f32(Rw.a[2] + AGG_REL + 128 * c, m1[0]); lds_add_f32(Rw.a[3] + AGG_REL + 128 * c, m1[1]);
      }
    }
  }
}

struct Recs { u32x4 rc[4]; uint32_t lc[4]; };
DI void load_recs(Recs& R, const uint32_t* sg, const char* ws, int g, int j, int done) {
  const __amdgpu_buffer_rsrc_t rsrd = __builtin_amdgcn_make_buffer_rsrc((void*)(ws + WS_REC), (short)0, NWG * CHB * 20, 0x00020000);
#pragma unroll
  for (int p = 0; p < 2; p++) {
#pragma unroll
    for (int r = 0; r < 2; r++) {
      uint32_t off = (uint32_t)((g + 128 * r) * (CHB * 20) + ((int)(sg[r] & 0xffff) + (done >> 2) + 4 * p + j) * 20);
      if ((int)(sg[r] >> 16) - done <= 16 * p + 4 * j) off = 0x40000000u;
      asm volatile("buffer_load_dwordx4 %0, %1, %2, 0 offen sc1" : "=&v"(R.rc[2 * r + p]) : "v"(off), "s"(rsrd) : "memory");
      asm volatile("buffer_load_dword %0, %1, %2, 0 offen offset:16 sc1" : "=&v"(R.lc[2 * r + p]) : "v"(off), "s"(rsrd) : "memory");
    }
  }
}
#define WAIT_RECS(R) asm volatile("s_waitcnt vmcnt(0)" : "+v"(R.rc[0]), "+v"(R.rc[1]), "+v"(R.rc[2]), "+v"(R.rc[3]), \
                                  "+v"(R.lc[0]), "+v"(R.lc[1]), "+v"(R.lc[2]), "+v"(R.lc[3]) :: "memory")

__global__ __launch_bounds__(512, 2) void k_mpnn(const int* __restrict__ src, const int* __restrict__ dst,
                                                 const uint16_t* __restrict__ ea, const uint16_t* __restrict__ x,
                                                 const uint16_t* __restrict__ uw, const uint16_t* __restrict__ vw,
                                                 const uint16_t* __restrict__ gw, char* __restrict__ ws,
                                                 uint16_t* __restrict__ out, int N, int E) {
  extern __shared__ __attribute__((aligned(128))) char smem[];
  const uint32_t lb = (uint32_t)(uintptr_t)(LDSP(char))smem;
  uint32_t* misc = (uint32_t*)(smem + L_MISC);
  const int t = threadIdx.x, lane = t & 63;
  const int w = blockIdx.x;
  const int b = (blockIdx.x & 7) * 32 + (blockIdx.x >> 3);
  const int wv = __builtin_amdgcn_readfirstlane(t >> 6);
  const int r31 = lane & 31, h = lane >> 5;
  TS_DECL; TS(0);

  const __amdgpu_buffer_rsrc_t srd = __builtin_amdgcn_make_buffer_rsrc((void*)x, (short)0, N * 128, 0x00020000);
  const int e0 = w * CH;
  const int n  = min(CH, E - e0);
  const int cnt = min(max(n - 8 * t, 0), 8);
  u32x4 d4[2], s4[2], e4; u32x2 xn;
  {
    const __amdgpu_buffer_rsrc_t dsrd = __builtin_amdgcn_make_buffer_rsrc((void*)dst, (short)0, E * 4, 0x00020000);
    const __amdgpu_buffer_rsrc_t ssrd = __builtin_amdgcn_make_buffer_rsrc((void*)src, (short)0, E * 4, 0x00020000);
    const __amdgpu_buffer_rsrc_t esrd = __builtin_amdgcn_make_buffer_rsrc((void*)ea, (short)0, (E * 2 + 3) & ~3, 0x00020000);
    const uint32_t eo = cnt > 0 ? (uint32_t)(e0 + 8 * t) : 0x20000000u;
    asm volatile("buffer_load_dwordx4 %0, %1, %2, 0 offen" : "=&v"(d4[0]) : "v"(eo * 4), "s"(dsrd) : "memory");
    asm volatile("buffer_load_dwordx4 %0, %1, %2, 0 offen offset:16" : "=&v"(d4[1]) : "v"(eo * 4), "s"(dsrd) : "memory");
    asm volatile("buffer_load_dwordx4 %0, %1, %2, 0 offen" : "=&v"(s4[0]) : "v"(eo * 4), "s"(ssrd) : "memory");
    asm volatile("buffer_load_dwordx4 %0, %1, %2, 0 offen offset:16" : "=&v"(s4[1]) : "v"(eo * 4), "s"(ssrd) : "memory");
    asm volatile("buffer_load_dwordx4 %0, %1, %2, 0 offen" : "=&v"(e4) : "v"(eo * 2), "s"(esrd) : "memory");
    asm volatile("global_load_dwordx2 %0, %1, %2 sc1" : "=&v"(xn) : "v"(0), "s"(ws + WS_NONCE) : "memory");
  }
  uint32_t codepf;
  {
    u64 pc; asm volatile("s_getpc_b64 %0" : "=s"(pc));
    const uint32_t o = (uint32_t)(wv * 60 + min(lane, 59)) * 64u;
    asm volatile("global_load_dword %0, %1, %2" : "=&v"(codepf) : "v"(o), "s"(pc & ~255ull) : "memory");
  }
  uint32_t* hist = (uint32_t*)(smem + A_HIST);
  uint32_t* base = (uint32_t*)(smem + A_BASE);
  if (t < 256) hist[t] = 0;
  else if (t < 384) { *(uint32_t*)(smem + L_CNT + (t - 256) * 4) = 0; *(uint32_t*)(smem + L_NQ + (t - 256) * 4) = 0; }
  else { misc[t - 384] = 0; if (t < 448) *(float*)(smem + L_EA + 98 * 256 + (t - 384) * 4) = 0.f; }
  LDS_BARRIER();
  asm volatile("s_waitcnt vmcnt(0)" : "+v"(xn), "+v"(d4[0]), "+v"(d4[1]), "+v"(s4[0]), "+v"(s4[1]), "+v"(e4), "+v"(codepf) :: "memory");
  const uint32_t tag32 = xn[0] + 1u;
  const u64 tag40 = ((((u64)xn[1] << 32) | xn[0]) + 1ull) & 0xffffffffffull;
  uint32_t bk[8], rk0[8];
#pragma unroll
  for (int k = 0; k < 8; k++) {
    bk[k] = (d4[k >> 2][k & 3] * 21400u) >> 21;
    rk0[k] = 0;
    if (k < cnt) rk0[k] = __hip_atomic_fetch_add(&hist[bk[k]], 1u, RLX_WG);
  }
  TS(1);
  u32x4 wb; uint32_t c0, c1;
  asm volatile("global_load_dwordx4 %0, %1, %2" : "=&v"(wb) : "v"(t * 16), "s"(gw + 64 * 64) : "memory");
  asm volatile("global_load_ushort %0, %1, %2" : "=&v"(c0) : "v"(r31 * 2), "s"(gw + 128 * 64) : "memory");
  asm volatile("global_load_ushort %0, %1, %2 offset:64" : "=&v"(c1) : "v"(r31 * 2), "s"(gw + 128 * 64) : "memory");
  LDS_BARRIER();
  if (wv == 7) {
    const u32x4 c = *(const u32x4*)(hist + 4 * lane);
    const u32x4 p = (c + 3) >> 2;
    const uint32_t s1 = p[0] + p[1], s2 = s1 + p[2], tot = s2 + p[3];
    const uint32_t ex = wave_scan_add(tot) - tot;
    *(u32x4*)(base + 4 * lane) = u32x4{ex, ex + p[0], ex + s1, ex + s2};
  } else {
    const __amdgpu_buffer_rsrc_t asrd = __builtin_amdgcn_make_buffer_rsrc((void*)gw, (short)0, 8192, 0x00020000);
    const __amdgpu_buffer_rsrc_t usrd = __builtin_amdgcn_make_buffer_rsrc((void*)uw, (short)0, 8192, 0x00020000);
    const __amdgpu_buffer_rsrc_t vsrd = __builtin_amdgcn_make_buffer_rsrc((void*)vw, (short)0, 8192, 0x00020000);
    const uint32_t cA = (uint32_t)(((lane & 7) ^ (lane >> 4)) * 16);
#pragma unroll
    for (int i = 0; i < 6; i++) {
      const int p = wv + 7 * i;
      const uint32_t wo = (lane * 16) ^ ((lane & 16) << 2);
      if (p < 8) dma16(lb + A_W + p * 1024, p * 1024 + wo, asrd);
      else if (p < 16) dma16(lb + A_W + 16384 + (p - 8) * 1024, (p - 8) * 1024 + wo, usrd);
      else if (p < 24) dma16(lb + A_W + 24576 + (p - 16) * 1024, (p - 16) * 1024 + wo, vsrd);
      else if (p < 37) { const int q = p - 24; dma16(lb + P_X + q * 1024, (b * NPB + 8 * q + (lane >> 3)) * 128 + ((q & 1) ? cA ^ 64u : cA), srd); }
    }
  }
  {
    uint32_t m2 = 0;
#pragma unroll
    for (int k = 0; k < 4; k++) m2 = max(m2, max(e4[k] & 0x7fffu, (e4[k] >> 16) & 0x7fffu));
    m2 = wave_max_u32(m2 << 16);
    if (lane == 63) __hip_atomic_fetch_max(&misc[M_MX + 2], m2, RLX_WG);
  }
  LDS_BARRIER();
#pragma unroll
  for (int k = 0; k < 8; k++) {
    if (k < cnt) {
      const uint32_t p = base[bk[k]] * 4 + rk0[k];
      const uint32_t o = (p >> 2) * 20 + (p & 3);
      const uint32_t ev = (k & 1) ? e4[k >> 1] & 0xffff0000u : e4[k >> 1] << 16;
      *(uint32_t*)(smem + A_ST + o + (p & 3) * 3) = (s4[k >> 2][k & 3] & 0xffffu) | ev;
      *(uint8_t*)(smem + A_ST + o + 16) = (uint8_t)(d4[k >> 2][k & 3] - bk[k] * NPB);
    }
  }
  LDS_BARRIER();
  TS(2);
  {
    const __amdgpu_buffer_rsrc_t rsrd = __builtin_amdgcn_make_buffer_rsrc((void*)(ws + WS_REC + (size_t)w * CHB * 20), (short)0, CHB * 20, 0x00020000);
    const uint32_t nb = base[255] + ((hist[255] + 3) >> 2);
#pragma unroll
    for (int i = 0; i < 3; i++) {
      const int c = t + 512 * i;
      if (c * 16 < (int)nb * 20) __builtin_amdgcn_raw_buffer_store_b128(*(const u32x4*)(smem + A_ST + c * 16), rsrd, c * 16, 0, AUX_SC1);
    }
    asm volatile("s_waitcnt vmcnt(0)" : "+v"(wb), "+v"(c0), "+v"(c1) :: "memory");
#pragma unroll
    for (int k = 0; k < 4; k++) wb[k] = cvtpk(bf2f(wb[k] & 0xffffu) * NLOG2E, __uint_as_float(wb[k] & 0xffff0000u) * NLOG2E);
    *(u32x4*)(smem + A_W + 8192 + ((t * 16) ^ ((t & 16) << 2))) = wb;
    if (arrive(&misc[M_CW], lane) == 7) {
      const u32x4 c = *(const u32x4*)(hist + 4 * lane), o = *(const u32x4*)(base + 4 * lane);
      gu64* g = (gu64*)(ws + WS_SEG) + w * NWG + 4 * lane;
#pragma unroll
      for (int k = 0; k < 4; k++) __hip_atomic_store(g + k, (u64)(o[k] | (c[k] << 12)) | (tag40 << 24), RLX_AGENT);
      if (lane == 0) TS_PUB(0);
    }
  }
  await_lds(&misc[M_CW], 8u);
  Wts W;
  const uint32_t troff = lb + A_W + (8 * h + ((lane & 15) >> 2)) * 128 + (16 * ((lane >> 4) & 1) + 4 * (lane & 3)) * 2, sw = (lane & 8) << 3;
  auto weights = [&](auto cc) {
    constexpr int c = decltype(cc)::value;
#pragma unroll
    for (int s = 0; s < 4; s++) {
      W.b[c][s] = tr_frag(troff + 8192 + s * 2048 + ((c * 64) ^ sw), 512);
      W.v[c][s] = tr_frag(troff + 24576 + s * 2048 + ((c * 64) ^ sw), 512);
    }
  };
  {
    const int rt = wv >> 1, row = 32 * rt + r31;
    const uint32_t xa = lb + P_X + row * 128 + ((h ^ ((row >> 1) & 7)) * 16);
    uint32_t xam = 0, mb = 0, mv = 0; float s2 = 0.f;
    auto job = [&](auto cc) {
      constexpr int c = decltype(cc)::value;
      bf16x8 a[4], fa[4], fu[4];
#pragma unroll
      for (int s = 0; s < 4; s++) {
        a[s] = *(LDSP(const bf16x8))(xa ^ (32u * s));
        fa[s] = tr_frag(troff + s * 2048 + ((c * 64) ^ sw), 512);
        fu[s] = tr_frag(troff + 16384 + s * 2048 + ((c * 64) ^ sw), 512);
      }
      weights(cc);
      f32x16 za, zu, zb, zv;
#pragma unroll
      for (int i = 0; i < 16; i++) { za[i] = 0.f; zu[i] = 0.f; zb[i] = 0.f; zv[i] = 0.f; }
#pragma unroll
      for (int s = 0; s < 4; s++) { za = mfma(a[s], fa[s], za); zu = mfma(a[s], fu[s], zu); zb = mfma(a[s], W.b[c][s], zb); zv = mfma(a[s], W.v[c][s], zv); }
      const uint32_t o = lb + L_EA + (32 * rt + 4 * h) * 256 + (32 * c + r31) * 4;
      if (rt < 3) {
#pragma unroll
        for (int i = 0; i < 16; i++) {
          const float v = za[i] * NLOG2E;
          *(LDSP(float))(o + crow(i, 0) * 256) = v; *(LDSP(float))(o + (L_XU - L_EA) + crow(i, 0) * 256) = zu[i];
          xam = max(xam, absbits(v)); mb = max(mb, absbits(zb[i])); mv = max(mv, absbits(zv[i])); s2 = fmaf(zv[i], zv[i], s2);
        }
      } else if (h == 0) {
#pragma unroll
        for (int i = 0; i < 2; i++) {
          const float v = za[i] * NLOG2E;
          *(LDSP(float))(o + i * 256) = v; *(LDSP(float))(o + (L_XU - L_EA) + i * 256) = zu[i];
          xam = max(xam, absbits(v)); mb = max(mb, absbits(zb[i])); mv = max(mv, absbits(zv[i])); s2 = fmaf(zv[i], zv[i], s2);
        }
      }
    };
    if (wv & 1) job(IC<1>{}); else job(IC<0>{});
    xam = wave_max_u32(xam); mb = wave_max_u32(mb); mv = wave_max_u32(mv); s2 = wave_sum_f32(s2);
    const uint32_t cm = wave_max_u32(max(absbits(bf2f(c0) * NLOG2E), absbits(bf2f(c1) * NLOG2E)));
    if (lane == 63) {
      __hip_atomic_fetch_max(&misc[M_XAM], xam, RLX_WG); __hip_atomic_fetch_max(&misc[M_MX], mb, RLX_WG);
      __hip_atomic_fetch_max(&misc[M_MX + 1], mv, RLX_WG); misc[M_MX + 3] = cm; misc[M_SQ + wv] = __float_as_uint(s2);
    }
    if (arrive(&misc[M_CB], lane) == 7 && lane == 0) {
      const u32x4 m = *(const u32x4*)(misc + M_MX), q0 = *(const u32x4*)(misc + M_SQ), q1 = *(const u32x4*)(misc + M_SQ + 4);
      const float sq = ((__uint_as_float(q0[0]) + __uint_as_float(q0[1])) + (__uint_as_float(q0[2]) + __uint_as_float(q0[3])))
                     + ((__uint_as_float(q1[0]) + __uint_as_float(q1[1])) + (__uint_as_float(q1[2]) + __uint_as_float(q1[3])));
      gu64* g = (gu64*)(ws + WS_BND) + 4 * w;
      __hip_atomic_store(g,     (u64)(min((m[0] + 0xffffu) >> 16, 0x7fffu) | (min((m[1] + 0xffffu) >> 16, 0x7fffu) << 16)) | ((u64)tag32 << 32), RLX_AGENT);
      __hip_atomic_store(g + 1, (u64)__float_as_uint(sq) | ((u64)tag32 << 32), RLX_AGENT);
      __hip_atomic_store(g + 2, (u64)m[2] | ((u64)tag32 << 32), RLX_AGENT);
    }
  }
  TS(3);

  auto thread_id = [&]() {
    int v = wv * 64 + (int)__builtin_amdgcn_mbcnt_hi(~0u, __builtin_amdgcn_mbcnt_lo(~0u, 0u));
    asm volatile("" : "+v"(v));
    return v;
  };
  const int tb = thread_id(), lb_ = tb & 63;
  uint32_t sg[2] = {0, 0};
  auto poll_segments = [&](int g) {
    gu64* gs = (gu64*)(ws + WS_SEG) + g * NWG + b;
    for (unsigned spins = 0;; spins++) {
      const u64 v0 = __hip_atomic_load(gs, RLX_AGENT), v1 = __hip_atomic_load(gs + 128 * NWG, RLX_AGENT);
      const bool ok = (v0 >> 24) == tag40 && (v1 >> 24) == tag40;
      sg[0] = ((uint32_t)v0 & 0xfffu) | ((((uint32_t)v0 >> 12) & 0xfffu) << 16);
      sg[1] = ((uint32_t)v1 & 0xfffu) | ((((uint32_t)v1 >> 12) & 0xfffu) << 16);
      if (__all(ok)) break;
      if (spins > SPIN_CAP) { sg[0] = sg[1] = 0; break; }
      __builtin_amdgcn_s_sleep(8);
    }
  };
  Recs R;
  int done = 0;
  uint32_t rk[4][2]; bool part1[2] = {false, false};
  auto post_total = [&](int dn, int j, int l) {
    uint32_t v = 0;
    if (j == 0) v = min(max((int)(sg[0] >> 16) - dn, 0), 32) + min(max((int)(sg[1] >> 16) - dn, 0), 32);
    v = wave_scan_add(v);
    if (l == 63) __hip_atomic_fetch_add(&misc[M_TOT], v, RLX_WG);
  };
  auto rank = [&](int tt, int dn, int take) {
    const int j = tt & 3;
    const uint32_t spare = (98u + (uint32_t)(tt & 63) % 30u) * 0x01010101u;
    bool more = false;
#pragma unroll
    for (int i = 0; i < 4; i++) {
      const int len = (int)(sg[i >> 1] >> 16) - dn;
      more |= len > take;
      const int v = min(max(min(len, take) - 16 * (i & 1) - 4 * j, 0), 4);
      const uint32_t drop = (uint32_t)(0xffffffffull << (8 * v));
      R.lc[i] = (R.lc[i] & ~drop) | (spare & drop);
      if (i & 1) part1[i >> 1] = __any(v > 0);
    }
    uint32_t r[2][4];
#pragma unroll
    for (int st = 0; st < 2; st++)
#pragma unroll
      for (int k = 0; k < 4; k++) r[st][k] = __hip_atomic_fetch_add((LDSP(uint32_t))(L_CNT + byte_x4(R.lc[2 * st], k)), 1u, RLX_WG);
#pragma unroll
    for (int st = 0; st < 2; st++) {
      rk[2 * st][0] = r[st][0] | (r[st][1] << 16); rk[2 * st][1] = r[st][2] | (r[st][3] << 16);
      rk[2 * st + 1][0] = rk[2 * st + 1][1] = 0;
      if (part1[st]) {
        uint32_t q[4];
#pragma unroll
        for (int k = 0; k < 4; k++) q[k] = __hip_atomic_fetch_add((LDSP(uint32_t))(L_CNT + byte_x4(R.lc[2 * st + 1], k)), 1u, RLX_WG);
        rk[2 * st + 1][0] = q[0] | (q[1] << 16); rk[2 * st + 1][1] = q[2] | (q[3] << 16);
      }
    }
    if (__any(more) && (tt & 63) == 0) misc[M_MORE] = 1;
  };
  auto scan = [&](int ln_) {
    u32x2 c = *(const u32x2*)(smem + L_CNT + ln_ * 8);
    if (ln_ >= NPB / 2) c = u32x2{0, 0};
    const uint32_t p0 = (c[0] + 3) & ~3u, p1 = (c[1] + 3) & ~3u;
    const uint32_t inc = wave_scan_add(p0 + p1), ex = inc - (p0 + p1);
    *(u32x2*)(smem + L_OFF + ln_ * 8) = ln_ < NPB / 2 ? u32x2{ex, ex + p0} : u32x2{SORT_CAP, SORT_CAP};
    const uint32_t T4 = (uint32_t)__builtin_amdgcn_readlane((int)inc, 63);
    const uint32_t T32 = (T4 + 31) & ~31u;
    if (T4 + ln_ < T32 + 32) *(uint32_t*)(smem + L_SORT + (T4 + ln_) * 4) = DUMMY;
    if ((T4 >> 2) + ln_ < (T32 >> 2) + 8) *(uint16_t*)(smem + L_QN + ((T4 >> 2) + ln_) * 2) = QN_DUMMY;
    if (ln_ == 0) misc[M_NTILE] = T32 >> 5;
  };
  auto scatter = [&](int tt) {
#pragma unroll
    for (int i = 0; i < 4; i++) {
      if ((i & 1) == 0 || part1[i >> 1]) {
#pragma unroll
        for (int k = 0; k < 4; k++) {
          const uint32_t pos = min(*(LDSP(const uint32_t))(L_OFF + byte_x4(R.lc[i], k)) + ((rk[i][k >> 1] >> (16 * (k & 1))) & 0xffffu), (uint32_t)(SORT_CAP - 1));
          *(LDSP(uint32_t))(lb + L_SORT + pos * 4) = R.rc[i][k];
        }
      }
    }
    if (tt < NPB * 4) {
      const uint32_t nd = tt >> 2, k = tt & 3;
      const uint32_t c = *(const uint32_t*)(smem + L_CNT + nd * 4), o = *(const uint32_t*)(smem + L_OFF + nd * 4);
      for (uint32_t q = k; 4 * q < c; q += 4) *(uint16_t*)(smem + L_QN + (o >> 1) + q * 2) = (uint16_t)(nd << 8);
      if (k == 3) *(uint32_t*)(smem + L_NQ + nd * 4) += (c + 3) >> 2;
      if (k < 3 && c + k < ((c + 3) & ~3u)) *(uint32_t*)(smem + L_SORT + (o + c + k) * 4) = DUMMY + ((uint32_t)tt >> 12);
    }
  };
  auto lane_consts = [&](int tt) {
    Lane L;
    const int ln_ = tt & 63, g8_ = ln_ >> 3, p8_ = ln_ & 7, r31_ = ln_ & 31, h_ = ln_ >> 5;
    L.rec_a = lb + L_SORT + (2 * (g8_ >> 2) + (g8_ & 1)) * 16 + ((g8_ >> 1) & 1) * 8;
    L.cA = (uint32_t)((p8_ ^ (g8_ >> 1)) * 16); L.cB = L.cA ^ 64u;
    L.stage = lb + L_STAGE + wv * 8192;
    const uint32_t af = L.stage + r31_ * 128 + ((h_ ^ ((r31_ >> 1) & 7)) * 16);
#pragma unroll
    for (int s = 0; s < 4; s++) L.afrag[s] = af ^ (32u * s);
    const int o = r31_ >> 4, m1 = (r31_ >> 3) & 1, hh = (r31_ >> 2) & 1, m0 = (r31_ >> 1) & 1, ab = r31_ & 1;
    L.erec  = lb + L_SORT + (4 * (4 * o + 2 * hh + ab) + 2 * m0 + m1) * 4;
    L.esel  = h_ == 0 ? 0x03020302u : 0x0c0c0c0cu;
    L.qn_a  = lb + L_QN + h_ * 4;
    L.col4  = lb + L_EA + r31_ * 4;
    return L;
  };

  int take = 32;
  poll_segments(tb >> 2);
  TS(4);
  load_recs(R, sg, ws, tb >> 2, tb & 3, 0);
  post_total(0, tb & 3, lb_);
  {
    uint32_t v = (tb & 3) == 0 ? (sg[0] >> 16) + (sg[1] >> 16) : 0u;
    v = wave_scan_add(v);
    if (lb_ == 63) __hip_atomic_fetch_add(&misc[M_ALL], v, RLX_WG);
  }
  if (wv & 1) weights(IC<0>{}); else weights(IC<1>{});
#pragma unroll
  for (int c = 0; c < 2; c++) {
    const float cv = bf2f(c ? c1 : c0) * NLOG2E;
    const uint32_t chi = cvtpk(cv, 0.f);
    const uint32_t clo = cvtpk(cv - bf2f(chi), 0.f);
    const u32x4 cu = {h == 0 ? (chi | (clo << 16)) : 0u, 0u, 0u, 0u};
    W.c[c] = __builtin_bit_cast(bf16x8, cu);
  }
  WAIT_RECS(R);
  TS(5);
  rank(tb, 0, take);
  LDS_BARRIER();
  if (w == 0 && tb == 0) __hip_atomic_store((gu64*)(ws + WS_NONCE), (((u64)xn[1] << 32) | xn[0]) + 1ull, RLX_AGENT);
  if (__builtin_amdgcn_readfirstlane(misc[M_TOT]) > (uint32_t)TAKE32_MAX) {
    LDS_BARRIER();
    if (tb < 128) *(uint32_t*)(smem + L_CNT + tb * 4) = 0;
    LDS_BARRIER();
    take = 16;
    rank(tb, 0, take);
    LDS_BARRIER();
  }
  if (wv == 0) scan(lb_);
  else if (wv == 1) {
    gu64* gt = (gu64*)(ws + WS_BND) + 4 * lb_;
    uint32_t mb, mv_, me; float sq;
    for (unsigned spins = 0;; spins++) {
      bool ok = true; mb = mv_ = me = 0; sq = 0.f;
#pragma unroll
      for (int i = 0; i < 4; i++) {
        const u64 v0 = __hip_atomic_load(gt + 256 * i, RLX_AGENT), v1 = __hip_atomic_load(gt + 256 * i + 1, RLX_AGENT), v2 = __hip_atomic_load(gt + 256 * i + 2, RLX_AGENT);
        ok &= (uint32_t)(v0 >> 32) == tag32 && (uint32_t)(v1 >> 32) == tag32 && (uint32_t)(v2 >> 32) == tag32;
        mb = max(mb, (uint32_t)v0 << 16); mv_ = max(mv_, (uint32_t)v0 & 0xffff0000u);
        me = max(me, (uint32_t)v2); sq += __uint_as_float((uint32_t)v1);
      }
      if (__all(ok)) break;
      if (spins > SPIN_CAP) break;
      __builtin_amdgcn_s_sleep(8);
    }
    mb = wave_max_u32(mb); mv_ = wave_max_u32(mv_); me = wave_max_u32(me); sq = wave_sum_f32(sq);
    const float mv = __uint_as_float(mv_);
    const float yb = (__uint_as_float(misc[M_XAM]) + __uint_as_float(mb) + __uint_as_float(me) * __uint_as_float(misc[M_MX + 3])) * 1.01f;
    const float all = (float)(misc[M_ALL] + 4u);
    const int p1 = (int)(__float_as_uint(4.1f * mv) >> 23) - 127 + 2, p2 = (int)(__float_as_uint(all * mv * 1.01f) >> 23) - 127 + 1 - 8;
    const int p = max(max(p1, p2), -80);
    const float dl = __uint_as_float((uint32_t)(p - 23 + 127) << 23);
    const bool fine = dl * 4096.f <= __builtin_amdgcn_sqrtf(sq * __builtin_amdgcn_rcpf(64.f * (float)N)) * 0.99f || mv == 0.f;
    if (lb_ == 63) {
      misc[M_FAST] = (yb <= 28.f) && (mv <= 1.0e12f) && fine;
      misc[M_EXP] = (uint32_t)(p + 127);
    }
  } else {
    for (int i = tb - 128; i < 99 * 16; i += 384) *(u32x4*)(smem + L_AGG + i * 16) = u32x4{0, 0, 0, 0};
  }
  LDS_BARRIER();
  TS(6);
  const bool fast = __builtin_amdgcn_readfirstlane(misc[M_FAST]);
  float delta; f32x2 magic;
  {
    const uint32_t pexp = __builtin_amdgcn_readfirstlane(misc[M_EXP]);
    delta = __uint_as_float((pexp - 23u) << 23);
    const float mg = __uint_as_float((pexp << 23) | 0x400000u);
    magic = f32x2{mg, mg};
  }
  scatter(tb);
  int again = __builtin_amdgcn_readfirstlane(misc[M_MORE]);
  LDS_BARRIER();
  int ntile = __builtin_amdgcn_readfirstlane(misc[M_NTILE]);
  if (fast) {
    const Lane L = lane_consts(tb);
    issue_tile(L, min(wv, ntile), 0, srd);
    for (int i = tb; i < NPB * 16; i += 512) {
      f32x4 v = *(const f32x4*)(smem + L_EA + i * 16);
      v[0] = __builtin_amdgcn_exp2f(v[0]); v[1] = __builtin_amdgcn_exp2f(v[1]); v[2] = __builtin_amdgcn_exp2f(v[2]); v[3] = __builtin_amdgcn_exp2f(v[3]);
      *(f32x4*)(smem + L_EA + i * 16) = v;
    }
    LDS_BARRIER();
    TS(7);
    if (wv < 4) edge_loop_fast<false>(L, W, wv, ntile, magic, srd, (unsigned long long*)(ws + WS_DBG));
    else        edge_loop_fast<true>(L, W, wv, ntile, magic, srd, (unsigned long long*)(ws + WS_DBG));
  } else {
    const Lane L = lane_consts(thread_id());
    edge_loop_plain<false>(L, W, wv, ntile, magic, srd);
  }
  while (again) {
    done += take;
    const int t2 = thread_id();
    poll_segments(t2 >> 2);
    load_recs(R, sg, ws, t2 >> 2, t2 & 3, done);
    WAIT_RECS(R);
    LDS_BARRIER();
    if (t2 < 128) *(uint32_t*)(smem + L_CNT + t2 * 4) = 0;
    if (t2 == 0) { misc[M_MORE] = 0; misc[M_TOT] = 0; }
    LDS_BARRIER();
    post_total(done, t2 & 3, t2 & 63);
    LDS_BARRIER();
    take = __builtin_amdgcn_readfirstlane(misc[M_TOT]) <= (uint32_t)TAKE32_MAX ? 32 : 16;
    rank(t2, done, take);
    LDS_BARRIER();
    if (wv == 0) scan(t2 & 63);
    LDS_BARRIER();
    scatter(t2);
    again = __builtin_amdgcn_readfirstlane(misc[M_MORE]);
    LDS_BARRIER();
    ntile = __builtin_amdgcn_readfirstlane(misc[M_NTILE]);
    const Lane L = lane_consts(t2);
    if (fast) edge_loop_plain<true>(L, W, wv, ntile, magic, srd);
    else      edge_loop_plain<false>(L, W, wv, ntile, magic, srd);
  }
  LDS_BARRIER();

  const int te = thread_id();
  for (int i = te; i < NPB * 8; i += 512) {
    const int node = b * NPB + (i >> 3);
    if (node < N) {
      float y[8];
      const uint32_t bias = *(const uint32_t*)(smem + L_NQ + (i >> 3) * 4) * __float_as_uint(magic[0]);
#pragma unroll
      for (int k = 0; k < 2; k++) {
        const u32x4 a4 = *(const u32x4*)(smem + L_AGG + i * 32 + k * 16);
        const f32x4 u4 = *(const f32x4*)(smem + L_XU + i * 32 + k * 16);
#pragma unroll
        for (int j = 0; j < 4; j++) {
          const float v = u4[j] + (fast ? (float)(int)(a4[j] - bias) * delta : __uint_as_float(a4[j]));
          y[4 * k + j] = v * __builtin_amdgcn_rcpf(1.f + __builtin_amdgcn_exp2f(v * NLOG2E));
        }
      }
      const u32x4 o = {cvtpk(y[0], y[1]), cvtpk(y[2], y[3]), cvtpk(y[4], y[5]), cvtpk(y[6], y[7])};
      *(u32x4*)(out + (size_t)node * 64 + (i & 7) * 8) = o;
    }
  }
  TS_OUT(256 + blockIdx.x);
}

}

extern "C" void kernel_launch(void* const* d_in, const int* in_sizes, int n_in,
                              void* d_out, int out_size, void* d_ws, size_t ws_size,
                              hipStream_t stream) {
  const uint16_t* x  = (const uint16_t*)d_in[0];
  const uint16_t* ea = (const uint16_t*)d_in[1];
  const int*      src = (const int*)d_in[2];
  const int*      dst = (const int*)d_in[3];
  const uint16_t* uw = (const uint16_t*)d_in[4];
  const uint16_t* vw = (const uint16_t*)d_in[5];
  const uint16_t* gw = (const uint16_t*)d_in[6];
  const int N = in_sizes[0] / 64, E = in_sizes[2];
  char* ws = (char*)d_ws;
  (void)hipFuncSetAttribute((const void*)k_mpnn, hipFuncAttributeMaxDynamicSharedMemorySize, L_TOTAL);
  k_mpnn<<<NWG, 512, L_TOTAL, stream>>>(src, dst, ea, x, uw, vw, gw, ws, (uint16_t*)d_out, N, E);
}
```

```cpp
#include <hip/hip_runtime.h>
#include <hip/hip_bf16.h>
#include <stdint.h>

#pragma clang diagnostic ignored "-Wint-to-pointer-cast"
#pragma clang diagnostic ignored "-Wpointer-to-int-cast"
namespace {

typedef float    f32x2  __attribute__((ext_vector_type(2)));
typedef float    f32x4  __attribute__((ext_vector_type(4)));
typedef float    f32x16 __attribute__((ext_vector_type(16)));
typedef short    bf16x8 __attribute__((ext_vector_type(8)));
typedef short    s16x4  __attribute__((ext_vector_type(4)));
typedef uint32_t u32x2  __attribute__((ext_vector_type(2)));
typedef uint32_t u32x4  __attribute__((ext_vector_type(4)));
typedef unsigned long long u64;
typedef __attribute__((address_space(1))) unsigned long long gu64;
#define LDSP(T) __attribute__((address_space(3))) T*
#define DI __device__ __forceinline__
#define RLX_AGENT __ATOMIC_RELAXED, __HIP_MEMORY_SCOPE_AGENT
#define RLX_WG    __ATOMIC_RELAXED, __HIP_MEMORY_SCOPE_WORKGROUP

constexpr int   NCH = 256;
constexpr int   NB  = 128;
constexpr int   NPB = 196;
constexpr int   CH  = 3128;
constexpr int   CHB = 880;
constexpr int   NMAX = NB * NPB;
constexpr int   NTILE = NMAX / 32;
constexpr float NLOG2E = -1.4426950408889634f;
constexpr int   AUX_SC1 = 16;
constexpr unsigned SPIN_CAP = 1u << 18;
static_assert(4 * CHB >= CH + 3 * NB && (CHB * 20) % 16 == 0, "blocks");

constexpr size_t WS_NONCE = 0;
constexpr size_t WS_SEG = 128;
constexpr size_t WS_TBG = WS_SEG + (size_t)NCH * NB * 8;
constexpr size_t WS_TRG = WS_TBG + (size_t)NCH * 16;
constexpr size_t WS_REC = WS_TRG + (size_t)NCH * 8;
constexpr size_t WS_TAB = (WS_REC + (size_t)NCH * CHB * 20 + 1024 + 127) & ~(size_t)127;
constexpr size_t WS_DBG = WS_TAB + (size_t)2 * NMAX * 128;
static_assert(WS_REC % 128 == 0 && WS_TAB % 128 == 0, "ws align");

constexpr int AGG_ROW  = 160;
constexpr int SORT_CAP = 8128;
constexpr int TAKE_MAX = SORT_CAP - 3 * NPB - 48;
constexpr int L_CNT    = 0;
constexpr int L_OFF    = L_CNT + 1024;
constexpr int L_NQ     = L_OFF + 1024;
constexpr int L_MISC   = L_NQ + 1024;
constexpr int L_QN     = L_MISC + 512;
constexpr int L_EA     = L_QN + SORT_CAP;
constexpr int L_XU     = L_EA + 197 * 128;
constexpr int L_AGG    = (L_XU + 196 * 128 + 127) & ~127;
constexpr int L_SORT   = (L_AGG + 197 * AGG_ROW + 127) & ~127;
constexpr int L_TOTAL  = L_SORT + SORT_CAP * 8;
static_assert(L_TOTAL <= 163840, "LDS");
static_assert((L_QN & 15) == 0 && (L_EA & 15) == 0 && (L_XU & 15) == 0 && TAKE_MAX >= 4096, "align");
constexpr int A_HIST = L_SORT;
constexpr int A_BASE = A_HIST + 512;
constexpr int A_ST   = A_BASE + 512;
constexpr int A_W    = (A_ST + CHB * 20 + 127) & ~127;
constexpr int A_IMG  = A_W + 16384;
constexpr int P_W    = A_IMG + 16384;
constexpr int P_X    = L_AGG;
static_assert(P_W + 8192 <= L_TOTAL && P_X + 224 * 128 <= L_SORT, "phase A images");
constexpr uint32_t QN_DUMMY = (196u * 128u) | ((196u * AGG_ROW) << 16);
constexpr uint32_t OFF_DUMMY = 0x7fffff00u;
constexpr int M_MORE = 0, M_NSTEP = 1, M_T4 = 2;
constexpr int M_CW = 3, M_CB = 4, M_CT = 5, M_PUB = 6;
constexpr int M_MX = 8;
constexpr int M_TOT = 16, M_ALL = 32, M_XAM = 48;
constexpr int M_BND = 56;
constexpr int M_CMX = 60;

DI float    bf2f(uint32_t v16) { return __uint_as_float(v16 << 16); }
DI uint32_t cvtpk(float lo, float hi) {
  typedef __bf16 bf16x2 __attribute__((ext_vector_type(2)));
  return __builtin_bit_cast(uint32_t, __builtin_convertvector(f32x2{lo, hi}, bf16x2));
}
DI f32x16   mfma(bf16x8 a, bf16x8 b, f32x16 c) { return __builtin_amdgcn_mfma_f32_32x32x16_bf16(a, b, c, 0, 0, 0); }
DI int      crow(int r, int h) { return (r & 3) + 8 * (r >> 2) + 4 * h; }
DI uint32_t absbits(float v) { return __float_as_uint(v) & 0x7fffffffu; }

template <int CTRL, int RM> DI uint32_t dpp0(uint32_t v) { return (uint32_t)__builtin_amdgcn_update_dpp(0, (int)v, CTRL, RM, 0xf, false); }
DI uint32_t wave_scan_add(uint32_t v) {
  v += dpp0<0x111, 0xf>(v); v += dpp0<0x112, 0xf>(v); v += dpp0<0x114, 0xf>(v); v += dpp0<0x118, 0xf>(v);
  v += dpp0<0x142, 0xa>(v); v += dpp0<0x143, 0xc>(v);
  return v;
}
DI uint32_t wave_max_u32(uint32_t v) {
  v = max(v, dpp0<0x111, 0xf>(v)); v = max(v, dpp0<0x112, 0xf>(v)); v = max(v, dpp0<0x114, 0xf>(v)); v = max(v, dpp0<0x118, 0xf>(v));
  v = max(v, dpp0<0x142, 0xa>(v)); v = max(v, dpp0<0x143, 0xc>(v));
  return v;
}
DI float wave_sum_f32(float s) {
  s += __uint_as_float(dpp0<0x111, 0xf>(__float_as_uint(s))); s += __uint_as_float(dpp0<0x112, 0xf>(__float_as_uint(s)));
  s += __uint_as_float(dpp0<0x114, 0xf>(__float_as_uint(s))); s += __uint_as_float(dpp0<0x118, 0xf>(__float_as_uint(s)));
  s += __uint_as_float(dpp0<0x142, 0xa>(__float_as_uint(s))); s += __uint_as_float(dpp0<0x143, 0xc>(__float_as_uint(s)));
  return s;
}
#define LDS_BARRIER() asm volatile("s_waitcnt lgkmcnt(0)\n\ts_barrier" ::: "memory")
#define WAIT_VM(N) asm volatile("s_waitcnt vmcnt(" #N ")" ::: "memory")

DI uint32_t arrive(uint32_t* word, int lane) {
  asm volatile("s_waitcnt lgkmcnt(0)" ::: "memory");
  uint32_t v = 0;
  if (lane == 0) v = __hip_atomic_fetch_add(word, 1u, RLX_WG);
  return __builtin_amdgcn_readfirstlane(v);
}
DI void await_lds(uint32_t* word, uint32_t want) {
  for (unsigned spins = 0; spins < SPIN_CAP; spins++) {
    if (__builtin_amdgcn_readfirstlane(__hip_atomic_load(word, RLX_WG)) >= want) break;
    __builtin_amdgcn_s_sleep(1);
  }
  asm volatile("" ::: "memory");
}

DI void dma16(uint32_t ldsaddr, uint32_t voff, __amdgpu_buffer_rsrc_t srd) {
  asm volatile("s_mov_b32 m0, %0\n\ts_nop 0\n\tbuffer_load_dwordx4 %1, %2, 0 offen lds" :: "s"(ldsaddr), "v"(voff), "s"(srd) : "memory");
}
DI bf16x8 tr_frag(uint32_t addr, int four_rows) {
  const s16x4 lo = __builtin_amdgcn_ds_read_tr16_b64_v4i16((LDSP(s16x4))addr);
  const s16x4 hi = __builtin_amdgcn_ds_read_tr16_b64_v4i16((LDSP(s16x4))(addr + four_rows));
  return (bf16x8)__builtin_shufflevector(lo, hi, 0, 1, 2, 3, 4, 5, 6, 7);
}

#define TS(i) do { } while (0)
#define TS_DECL do { } while (0)
#define TS_OUT(slot) do { } while (0)
#define TS_PUB(i) do { } while (0)

DI uint32_t shape(float v, uint32_t low) {
  const uint32_t b = __float_as_uint(v);
  const uint32_t t = min(__builtin_elementwise_sub_sat((b & 0x7fffffffu) + 0x8000u, low), 0x7fffffffu);
  return (b & 0x80000000u) | (t & 0xffff0000u) | low;
}
DI void table_half(u32x4* o, const bf16x8* xf, uint32_t troff, int c, float& mb, float& mv, float& s2) {
  f32x16 zb, hv;
#pragma unroll
  for (int i = 0; i < 16; i++) { zb[i] = 0.f; hv[i] = 0.f; }
#pragma unroll
  for (int s = 0; s < 4; s++) {
    zb = mfma(tr_frag(troff + s * 2048 + c * 64, 512), xf[s], zb);
    hv = mfma(tr_frag(troff + 8192 + s * 2048 + c * 64, 512), xf[s], hv);
  }
#pragma unroll
  for (int q = 0; q < 4; q++) {
#pragma unroll
    for (int p = 0; p < 2; p++) {
      const int i = 4 * q + 2 * p;
      const float b0 = zb[i] * NLOG2E, b1 = zb[i + 1] * NLOG2E, v0 = hv[i], v1 = hv[i + 1];
      mb = fmaxf(mb, fmaxf(fabsf(b0), fabsf(b1))); mv = fmaxf(mv, fmaxf(fabsf(v0), fabsf(v1)));
      s2 = fmaf(v0, v0, s2); s2 = fmaf(v1, v1, s2);
      const uint32_t d0 = shape(b0, cvtpk(v0, 0.f));
      const uint32_t w1 = shape(v1, d0 >> 16);
      o[q][2 * p] = d0; o[q][2 * p + 1] = shape(b1, w1 >> 16);
    }
  }
}
DI void table_image(const u32x4* o, uint32_t img, int lane) {
  const int n31 = lane & 31, h = lane >> 5;
#pragma unroll
  for (int q = 0; q < 4; q++) *(LDSP(u32x4))(img + n31 * 128 + (((2 * q + h) ^ (n31 & 7)) * 16)) = o[q];
  asm volatile("s_waitcnt lgkmcnt(0)" ::: "memory");
}
DI void table_store(uint32_t img, int tile, int c, __amdgpu_buffer_rsrc_t tsrd, int lane) {
  u32x4 r[4];
#pragma unroll
  for (int i = 0; i < 4; i++) { const int row = 8 * i + (lane >> 3); r[i] = *(LDSP(const u32x4))(img + row * 128 + (((lane & 7) ^ (row & 7)) * 16)); }
  asm volatile("s_waitcnt lgkmcnt(0)" : "+v"(r[0]), "+v"(r[1]), "+v"(r[2]), "+v"(r[3]) :: "memory");
#pragma unroll
  for (int i = 0; i < 4; i++)
    __builtin_amdgcn_raw_buffer_store_b128(r[i], tsrd, (c * NMAX + tile * 32 + 8 * i + (lane >> 3)) * 128 + (lane & 7) * 16, 0, AUX_SC1);
}

DI f32x2 exp2x2(f32x2 v) { f32x2 r = {__builtin_amdgcn_exp2f(v[0]), __builtin_amdgcn_exp2f(v[1])}; return r; }
DI f32x2 rcpx2(f32x2 v) { f32x2 r = {__builtin_amdgcn_rcpf(v[0]), __builtin_amdgcn_rcpf(v[1])}; return r; }
DI f32x2 fma2(f32x2 a, f32x2 b, f32x2 c) { return __builtin_elementwise_fma(a, b, c); }
template <int MUL> DI uint32_t madlo_i(uint32_t v, uint32_t add) {
  uint32_t r; asm("v_mad_u32_u16 %0, %1, %3, %2" : "=v"(r) : "v"(v), "v"(add), "i"(MUL)); return r;
}
template <int MUL> DI uint32_t madhi_i(uint32_t v, uint32_t add) {
  uint32_t r; asm("v_mad_u32_u16 %0, %1, %3, %2 op_sel:[1,0,0,0]" : "=v"(r) : "v"(v), "v"(add), "i"(MUL)); return r;
}
DI void lds_add_u32(uint32_t addr, float v) { __hip_atomic_fetch_add((LDSP(uint32_t))addr, __float_as_uint(v), RLX_WG); }
DI void lds_add_f32(uint32_t addr, float v) { __hip_atomic_fetch_add((LDSP(float))addr, v, RLX_WG); }

struct Lane {
  uint32_t rec;
  uint32_t qn;
  uint32_t c16;
  uint32_t ea, agg;
  f32x2 c01, c23;
  f32x2 magic;
};
struct Quad {
  u32x4 d[4];
  u32x4 r0, r1;
  uint32_t q;
};
template <int S> DI void fetch(Quad& Q, const Lane& L, __amdgpu_buffer_rsrc_t srd) {
  Q.r0 = *(LDSP(const u32x4))(L.rec + S * 32);
  Q.r1 = *(LDSP(const u32x4))(L.rec + S * 32 + 16);
  Q.q  = *(LDSP(const uint32_t))(L.qn + S * 4);
  Q.d[0] = __builtin_amdgcn_raw_buffer_load_b128(srd, Q.r0[0] + L.c16, 0, AUX_SC1);
  Q.d[1] = __builtin_amdgcn_raw_buffer_load_b128(srd, Q.r0[2] + L.c16, 0, AUX_SC1);
  Q.d[2] = __builtin_amdgcn_raw_buffer_load_b128(srd, Q.r1[0] + L.c16, 0, AUX_SC1);
  Q.d[3] = __builtin_amdgcn_raw_buffer_load_b128(srd, Q.r1[2] + L.c16, 0, AUX_SC1);
  asm volatile("" ::: "memory");
}
DI f32x2 fma_e(u32x2 rec, f32x2 c, u32x2 xb) {
  f32x2 r; asm("v_pk_fma_f32 %0, %1, %2, %3 op_sel:[1,0,0] op_sel_hi:[1,1,1]" : "=v"(r) : "v"(rec), "v"(c), "v"(xb)); return r;
}
DI f32x2 vpair(u32x2 d) {
  f32x2 r; asm("v_lshlrev_b64 %0, 16, %1" : "=v"(r) : "v"(d)); return r;
}
template <int MODE> DI void gate(const Quad& Q, const Lane& L) {
  const f32x4 ea = *(LDSP(const f32x4))(madlo_i<1>(Q.q, L.ea));
  const uint32_t ga = madhi_i<1>(Q.q, L.agg);
  const u32x2 rec[4] = {u32x2{Q.r0[0], Q.r0[1]}, u32x2{Q.r0[2], Q.r0[3]}, u32x2{Q.r1[0], Q.r1[1]}, u32x2{Q.r1[2], Q.r1[3]}};
  const f32x2 one = {1.f, 1.f};
#pragma unroll
  for (int p = 0; p < 2; p++) {
    const f32x2 c = p ? L.c23 : L.c01, eap = {ea[2 * p], ea[2 * p + 1]};
    f32x2 a[4], hv[4];
#pragma unroll
    for (int k = 0; k < 4; k++) {
      const u32x2 dd = {Q.d[k][2 * p], Q.d[k][2 * p + 1]};
      hv[k] = vpair(dd);
      const f32x2 y = fma_e(rec[k], c, dd);
      a[k] = MODE == 2 ? one + exp2x2(y + eap) : fma2(eap, exp2x2(y), one);
    }
    if (MODE == 0) {
      const f32x2 d01 = a[0] * a[1], n01 = fma2(hv[1], a[0], hv[0] * a[1]);
      const f32x2 d23 = a[2] * a[3], n23 = fma2(hv[3], a[2], hv[2] * a[3]);
      const f32x2 m = fma2(fma2(n23, d01, n01 * d23), rcpx2(d01 * d23), L.magic);
      lds_add_u32(ga + 64 * p, m[0]); lds_add_u32(ga + 64 * p + 32, m[1]);
    } else {
      const f32x2 m = (hv[0] * rcpx2(a[0]) + hv[1] * rcpx2(a[1])) + (hv[2] * rcpx2(a[2]) + hv[3] * rcpx2(a[3]));
      lds_add_f32(ga + 64 * p, m[0]); lds_add_f32(ga + 64 * p + 32, m[1]);
    }
  }
}
template <int MODE> DI void edge_loop(Lane& L, int n, __amdgpu_buffer_rsrc_t srd) {
  if (n <= 0) return;
  Quad A, B;
  fetch<0>(A, L, srd);
  while (true) {
    fetch<1>(B, L, srd);
    gate<MODE>(A, L);
    if (--n == 0) break;
    fetch<2>(A, L, srd);
    gate<MODE>(B, L);
    if (--n == 0) break;
    L.rec += 64; L.qn += 8;
  }
}

struct Recs { u32x4 rc[6]; uint32_t lc[6]; };
DI void load_recs(Recs& R, const uint32_t* sg, const char* ws, int g, int j, int done) {
  const __amdgpu_buffer_rsrc_t rsrd = __builtin_amdgcn_make_buffer_rsrc((void*)(ws + WS_REC), (short)0, NCH * CHB * 20, 0x00020000);
#pragma unroll
  for (int p = 0; p < 2; p++) {
#pragma unroll
    for (int r = 0; r < 3; r++) {
      uint32_t off = (uint32_t)((g + 96 * r) * (CHB * 20) + ((int)(sg[r] & 0xffff) + (done >> 2) + 8 * p + j) * 20);
      if ((int)(sg[r] >> 16) - done <= 32 * p + 4 * j) off = 0x40000000u;
      asm volatile("buffer_load_dwordx4 %0, %1, %2, 0 offen sc1" : "=&v"(R.rc[2 * r + p]) : "v"(off), "s"(rsrd) : "memory");
      asm volatile("buffer_load_dword %0, %1, %2, 0 offen offset:16 sc1" : "=&v"(R.lc[2 * r + p]) : "v"(off), "s"(rsrd) : "memory");
    }
  }
}
#define WAIT_RECS(R) asm volatile("s_waitcnt vmcnt(0)" : "+v"(R.rc[0]), "+v"(R.rc[1]), "+v"(R.rc[2]), "+v"(R.rc[3]), "+v"(R.rc[4]), "+v"(R.rc[5]), \
                                  "+v"(R.lc[0]), "+v"(R.lc[1]), "+v"(R.lc[2]), "+v"(R.lc[3]), "+v"(R.lc[4]), "+v"(R.lc[5]) :: "memory")

__global__ __launch_bounds__(1024) void k_mpnn(const int* __restrict__ src, const int* __restrict__ dst,
                                               const uint16_t* __restrict__ ea, const uint16_t* __restrict__ x,
                                               const uint16_t* __restrict__ uw, const uint16_t* __restrict__ vw,
                                               const uint16_t* __restrict__ gw, char* __restrict__ ws,
                                               uint16_t* __restrict__ out, int N, int E) {
  extern __shared__ __attribute__((aligned(128))) char smem[];
  const uint32_t lb = (uint32_t)(uintptr_t)(LDSP(char))smem;
  uint32_t* misc = (uint32_t*)(smem + L_MISC);
  const int t = threadIdx.x, lane = t & 63;
  const int w = blockIdx.x;
  const int half = (blockIdx.x & 7) >> 2, b = (blockIdx.x & 3) * 32 + (blockIdx.x >> 3);
  const int wv = __builtin_amdgcn_readfirstlane(t >> 6);
  const int r31 = lane & 31, h = lane >> 5;
  TS_DECL; TS(0);

  const int e0 = w * CH;
  const int n  = min(CH, E - e0);
  const int cnt = min(max(n - 4 * t, 0), 4);
  u32x2 xn; u32x4 d4, s4; u32x2 e2;
  {
    const __amdgpu_buffer_rsrc_t dsrd = __builtin_amdgcn_make_buffer_rsrc((void*)dst, (short)0, E * 4, 0x00020000);
    const __amdgpu_buffer_rsrc_t ssrd = __builtin_amdgcn_make_buffer_rsrc((void*)src, (short)0, E * 4, 0x00020000);
    const __amdgpu_buffer_rsrc_t esrd = __builtin_amdgcn_make_buffer_rsrc((void*)ea, (short)0, (E * 2 + 3) & ~3, 0x00020000);
    const uint32_t eo = cnt > 0 ? (uint32_t)(e0 + 4 * t) : 0x20000000u;
    asm volatile("buffer_load_dwordx4 %0, %1, %2, 0 offen" : "=&v"(d4) : "v"(eo * 4), "s"(dsrd) : "memory");
    asm volatile("buffer_load_dwordx4 %0, %1, %2, 0 offen" : "=&v"(s4) : "v"(eo * 4), "s"(ssrd) : "memory");
    asm volatile("buffer_load_dwordx2 %0, %1, %2, 0 offen" : "=&v"(e2) : "v"(eo * 2), "s"(esrd) : "memory");
    asm volatile("global_load_dwordx2 %0, %1, %2 sc1" : "=&v"(xn) : "v"(0), "s"(ws + WS_NONCE) : "memory");
  }
  uint32_t* hist = (uint32_t*)(smem + A_HIST);
  uint32_t* base = (uint32_t*)(smem + A_BASE);
  if (t < 128) hist[t] = 0;
  if (t < 512) *(uint32_t*)(smem + (t < 256 ? L_CNT + t * 4 : L_NQ + (t - 256) * 4)) = 0;
  if (t >= 768 && t < 896) misc[t - 768] = 0;
  if (t >= 896 && t < 928) *(float*)(smem + L_EA + 196 * 128 + (t - 896) * 4) = 0.f;
  LDS_BARRIER();
  asm volatile("s_waitcnt vmcnt(0)" : "+v"(xn), "+v"(d4), "+v"(s4), "+v"(e2) :: "memory");
  const uint32_t tag32 = xn[0] + 1u;
  const u64 tag40 = ((((u64)xn[1] << 32) | xn[0]) + 1ull) & 0xffffffffffull;
  const uint32_t ev[4] = {e2[0] & 0xffffu, e2[0] >> 16, e2[1] & 0xffffu, e2[1] >> 16};
  uint32_t bk[4], rk0[4];
#pragma unroll
  for (int k = 0; k < 4; k++) {
    bk[k] = (d4[k] * 21400u) >> 22;
    rk0[k] = 0;
    if (k < cnt) rk0[k] = __hip_atomic_fetch_add(&hist[bk[k]], 1u, RLX_WG);
  }
  u32x4 wc; u32x2 cw;
  const __amdgpu_buffer_rsrc_t xsrd = __builtin_amdgcn_make_buffer_rsrc((void*)x, (short)0, N * 128, 0x00020000);
  const int tile = wv >= 13 ? w + 256 * (wv - 13) : (wv == 12 ? w + 768 : NTILE);
  const uint32_t img = lb + A_IMG + (wv & 3) * 4096;
  {
    const uint16_t* wp = t < 512 ? gw + 64 * 64 + t * 8 : vw + (t - 512) * 8;
    asm volatile("global_load_dwordx4 %0, %1, off" : "=&v"(wc) : "v"(wp) : "memory");
    asm volatile("global_load_dwordx2 %0, %1, %2" : "=&v"(cw) : "v"((128 * 64 + 32 * half + 4 * (t & 7)) * 2), "s"(gw) : "memory");
    const uint32_t cA = (uint32_t)(((lane & 7) ^ (lane >> 4)) * 16);
    if (wv >= 12) {
      if (tile < NTILE) {
#pragma unroll
        for (int i = 0; i < 4; i++) dma16(img + i * 1024, (tile * 32 + 8 * i + (lane >> 3)) * 128 + ((i & 1) ? cA ^ 64u : cA), xsrd);
      }
    } else {
      const __amdgpu_buffer_rsrc_t asrd = __builtin_amdgcn_make_buffer_rsrc((void*)gw, (short)0, 64 * 128, 0x00020000);
      const __amdgpu_buffer_rsrc_t usrd = __builtin_amdgcn_make_buffer_rsrc((void*)uw, (short)0, 64 * 128, 0x00020000);
      const uint32_t woff = (lane >> 2) * 128 + half * 64 + (lane & 3) * 16;
#pragma unroll
      for (int i = 0; i < 3; i++) {
        const int p = wv + 12 * i;
        if (p < 4) dma16(lb + P_W + p * 1024, p * 2048 + woff, asrd);
        else if (p < 8) dma16(lb + P_W + p * 1024, (p - 4) * 2048 + woff, usrd);
        else if (p < 33) { const int q = p - 8; dma16(lb + P_X + q * 1024, (b * NPB + 8 * q + (lane >> 3)) * 128 + ((q & 1) ? cA ^ 64u : cA), xsrd); }
      }
    }
  }
  TS(1);
  LDS_BARRIER();
  if (t < 64) {
    const u32x2 c = *(const u32x2*)(hist + 2 * t);
    const uint32_t p0 = (c[0] + 3) >> 2, tot = p0 + ((c[1] + 3) >> 2);
    const uint32_t ex = wave_scan_add(tot) - tot;
    *(u32x2*)(base + 2 * t) = u32x2{ex, ex + p0};
  }
  if (wv < 12) WAIT_VM(2); else if (tile < NTILE) WAIT_VM(4); else WAIT_VM(0);
  asm volatile("" : "+v"(wc), "+v"(cw) :: "memory");
  *(u32x4*)(smem + A_W + t * 16) = wc;
  LDS_BARRIER();
#pragma unroll
  for (int k = 0; k < 4; k++) {
    if (k < cnt) {
      const uint32_t p = base[bk[k]] * 4 + rk0[k];
      const uint32_t o = (p >> 2) * 20 + (p & 3);
      *(uint32_t*)(smem + A_ST + o + (p & 3) * 3) = (s4[k] & 0xffffu) | (ev[k] << 16);
      *(uint8_t*)(smem + A_ST + o + 16) = (uint8_t)(d4[k] - bk[k] * NPB);
    }
  }
  if (wv <= 12) {
    const uint32_t m2 = wave_max_u32(max(max(ev[0] & 0x7fffu, ev[1] & 0x7fffu), max(ev[2] & 0x7fffu, ev[3] & 0x7fffu)) << 16);
    if (lane == 63) __hip_atomic_fetch_max(&misc[M_MX + 2], m2, RLX_WG);
  }
  LDS_BARRIER();
  TS(2);
  if (wv <= 12) {
    const __amdgpu_buffer_rsrc_t rsrd = __builtin_amdgcn_make_buffer_rsrc((void*)(ws + WS_REC + (size_t)w * CHB * 20), (short)0, CHB * 20, 0x00020000);
    const uint32_t nb = base[127] + ((hist[127] + 3) >> 2);
#pragma unroll
    for (int i = 0; i < 2; i++) {
      const int c = t + 832 * i;
      if (c * 16 < (int)nb * 20) __builtin_amdgcn_raw_buffer_store_b128(*(const u32x4*)(smem + A_ST + c * 16), rsrd, c * 16, 0, AUX_SC1);
    }
    WAIT_VM(0);
    if (arrive(&misc[M_CW], lane) == 12) {
      const u32x2 c = *(const u32x2*)(hist + 2 * lane), o = *(const u32x2*)(base + 2 * lane);
      gu64* g = (gu64*)(ws + WS_SEG) + w * NB + 2 * lane;
      __hip_atomic_store(g,     (u64)(o[0] | (c[0] << 12)) | (tag40 << 24), RLX_AGENT);
      __hip_atomic_store(g + 1, (u64)(o[1] | (c[1] << 12)) | (tag40 << 24), RLX_AGENT);
      if (lane == 0) { __hip_atomic_store(&misc[M_PUB], 1u, RLX_WG); TS_PUB(0); }
    }
  }
  Lane L;
  L.c01 = f32x2{bf2f(cw[0] & 0xffffu) * NLOG2E, __uint_as_float(cw[0] & 0xffff0000u) * NLOG2E};
  L.c23 = f32x2{bf2f(cw[1] & 0xffffu) * NLOG2E, __uint_as_float(cw[1] & 0xffff0000u) * NLOG2E};
  const uint32_t ntw = w < NTILE - 768 ? 4u : 3u;
  if (wv >= 5 && wv < 12) {
    await_lds(&misc[M_CW], 13u);
    const int rt = wv - 5, row = 32 * rt + r31;
    const uint32_t xa = lb + P_X + row * 128 + ((h ^ ((row >> 1) & 7)) * 16);
    const uint32_t troff = lb + P_W + (8 * h + ((lane & 15) >> 2)) * 64 + (16 * ((lane >> 4) & 1) + 4 * (lane & 3)) * 2;
    bf16x8 a[4];
#pragma unroll
    for (int s = 0; s < 4; s++) a[s] = *(LDSP(const bf16x8))(xa ^ (32u * s));
    uint32_t xam = 0;
#pragma unroll
    for (int part = 0; part < 2; part++) {
      f32x16 acc;
#pragma unroll
      for (int i = 0; i < 16; i++) acc[i] = 0.f;
#pragma unroll
      for (int s = 0; s < 4; s++) acc = mfma(a[s], tr_frag(troff + part * 4096 + s * 1024, 256), acc);
      const uint32_t o = lb + (part ? L_XU : L_EA) + (32 * rt + 4 * h) * 128 + r31 * 4;
#pragma unroll
      for (int i = 0; i < 16; i++) {
        const float v = part ? acc[i] : acc[i] * NLOG2E;
        if (rt < 6 || crow(i, 0) + 4 * h < 4) { *(LDSP(float))(o + crow(i, 0) * 128) = v; if (!part) xam = max(xam, absbits(v)); }
      }
    }
    xam = wave_max_u32(xam);
    if (lane == 63) misc[M_XAM + rt] = xam;
  } else if (wv == 4) {
    const uint32_t cm = wave_max_u32(max(max(absbits(L.c01[0]), absbits(L.c01[1])), max(absbits(L.c23[0]), absbits(L.c23[1]))));
    if (lane == 63) misc[M_CMX] = cm;
  } else if (wv >= 12 && tile < NTILE) {
    WAIT_VM(0);
    bf16x8 xf[4];
    const uint32_t xa = img + r31 * 128 + ((h ^ ((r31 >> 1) & 7)) * 16);
#pragma unroll
    for (int s = 0; s < 4; s++) xf[s] = *(LDSP(const bf16x8))(xa ^ (32u * s));
    const uint32_t troff = lb + A_W + (8 * h + ((lane & 15) >> 2)) * 128 + (16 * ((lane >> 4) & 1) + 4 * (lane & 3)) * 2;
    float mb = 0.f, mv = 0.f, s2 = 0.f;
    u32x4 o1[4];
    table_half(o1, xf, troff, 0, mb, mv, s2);
    table_image(o1, img, lane);
    table_half(o1, xf, troff, 1, mb, mv, s2);
    const uint32_t m0 = wave_max_u32(__float_as_uint(mb)), m1 = wave_max_u32(__float_as_uint(mv));
    s2 = wave_sum_f32(s2);
    if (lane == 63) {
      __hip_atomic_fetch_max(&misc[M_MX], m0, RLX_WG); __hip_atomic_fetch_max(&misc[M_MX + 1], m1, RLX_WG);
      misc[M_MX + 4 + (wv & 3)] = __float_as_uint(s2);
    }
    if (arrive(&misc[M_CB], lane) == ntw - 1 && lane == 0) {
      const u32x4 m = *(const u32x4*)(misc + M_MX), q = *(const u32x4*)(misc + M_MX + 4);
      const float sq = (__uint_as_float(q[0]) + __uint_as_float(q[1])) + (__uint_as_float(q[2]) + __uint_as_float(q[3]));
      const uint32_t g0 = min((m[0] + 0xffffu) >> 16, 0x7fffu) | (min((m[1] + 0xffffu) >> 16, 0x7fffu) << 16);
      const uint32_t g1 = (m[2] >> 16) | (__float_as_uint(sq) & 0xffff0000u);
      gu64* g = (gu64*)(ws + WS_TBG) + 2 * w;
      __hip_atomic_store(g,     (u64)g0 | ((u64)tag32 << 32), RLX_AGENT);
      __hip_atomic_store(g + 1, (u64)g1 | ((u64)tag32 << 32), RLX_AGENT);
    }
    await_lds(&misc[M_PUB], 1u);
    const __amdgpu_buffer_rsrc_t wsrd = __builtin_amdgcn_make_buffer_rsrc((void*)(ws + WS_TAB), (short)0, 2 * NMAX * 128, 0x00020000);
    table_store(img, tile, 0, wsrd, lane);
    table_image(o1, img, lane);
    table_store(img, tile, 1, wsrd, lane);
  }
  TS(3);

  auto thread_id = [&]() {
    int v = wv * 64 + (int)__builtin_amdgcn_mbcnt_hi(~0u, __builtin_amdgcn_mbcnt_lo(~0u, 0u));
    asm volatile("" : "+v"(v));
    return v;
  };
  const int tb = thread_id(), lb_ = tb & 63;
  const __amdgpu_buffer_rsrc_t tsrd = __builtin_amdgcn_make_buffer_rsrc((void*)(ws + WS_TAB + (size_t)half * NMAX * 128), (short)0, N * 128, 0x00020000);
  uint32_t sg[3] = {0, 0, 0};
  auto poll_segments = [&](int g) {
    gu64* gs = (gu64*)(ws + WS_SEG) + g * NB + b;
    const bool third = g + 192 < NCH;
    for (unsigned spins = 0;; spins++) {
      const u64 v0 = __hip_atomic_load(gs, RLX_AGENT), v1 = __hip_atomic_load(gs + 96 * NB, RLX_AGENT);
      const u64 v2 = __hip_atomic_load(gs + (third ? 192 * NB : 0), RLX_AGENT);
      const bool ok = (v0 >> 24) == tag40 && (v1 >> 24) == tag40 && (v2 >> 24) == tag40;
      sg[0] = ((uint32_t)v0 & 0xfffu) | ((((uint32_t)v0 >> 12) & 0xfffu) << 16);
      sg[1] = ((uint32_t)v1 & 0xfffu) | ((((uint32_t)v1 >> 12) & 0xfffu) << 16);
      sg[2] = third ? ((uint32_t)v2 & 0xfffu) | ((((uint32_t)v2 >> 12) & 0xfffu) << 16) : 0u;
      if (__all(ok)) break;
      if (spins > SPIN_CAP) { sg[0] = sg[1] = sg[2] = 0; break; }
      __builtin_amdgcn_s_sleep(8);
    }
  };
  Recs R;
  int done = 0;
  uint32_t rk[6][2]; bool part1 = false;
  auto post_total = [&](int dn, int j, int l) {
    uint32_t v = 0;
    if (j == 0) v = min(max((int)(sg[0] >> 16) - dn, 0), 64) + min(max((int)(sg[1] >> 16) - dn, 0), 64) + min(max((int)(sg[2] >> 16) - dn, 0), 64);
    v = wave_scan_add(v);
    if (l == 63) misc[M_TOT + wv] = v;
  };
  auto rank = [&](int tt, int dn, int take) {
    const int j = tt & 7;
    const uint32_t spare = (196u + (uint32_t)(tt & 63) % 60u) * 0x01010101u;
    bool more = false, p1 = false;
#pragma unroll
    for (int i = 0; i < 6; i++) {
      const int len = (int)(sg[i >> 1] >> 16) - dn;
      more |= len > take;
      const int v = min(max(min(len, take) - 32 * (i & 1) - 4 * j, 0), 4);
      const uint32_t keep = v == 4 ? 0xffffffffu : (1u << (8 * v)) - 1u;
      R.lc[i] = (R.lc[i] & keep) | (spare & ~keep);
      if (i & 1) p1 |= v > 0;
    }
    part1 = __any(p1);
#pragma unroll
    for (int pt = 0; pt < 2; pt++) {
      if (pt == 0 || part1) {
        uint32_t r[3][4];
#pragma unroll
        for (int st = 0; st < 3; st++)
#pragma unroll
          for (int k = 0; k < 4; k++) r[st][k] = __hip_atomic_fetch_add((uint32_t*)(smem + L_CNT) + ((R.lc[2 * st + pt] >> (8 * k)) & 0xffu), 1u, RLX_WG);
#pragma unroll
        for (int st = 0; st < 3; st++) {
          rk[2 * st + pt][0] = r[st][0] | (r[st][1] << 16); rk[2 * st + pt][1] = r[st][2] | (r[st][3] << 16);
          asm volatile("" : "+v"(rk[2 * st + pt][0]), "+v"(rk[2 * st + pt][1]));
        }
      } else {
#pragma unroll
        for (int st = 0; st < 3; st++) rk[2 * st + 1][0] = rk[2 * st + 1][1] = 0;
      }
    }
    if (__any(more) && (tt & 63) == 0) misc[M_MORE] = 1;
  };
  auto total = [&]() {
    const u32x4 s = *(const u32x4*)(misc + M_TOT) + *(const u32x4*)(misc + M_TOT + 4) + *(const u32x4*)(misc + M_TOT + 8);
    return (int)__builtin_amdgcn_readfirstlane((s[0] + s[1]) + (s[2] + s[3]));
  };
  auto scan = [&](int ln_) {
    u32x4 c = *(const u32x4*)(smem + L_CNT + ln_ * 16);
    if (ln_ >= NPB / 4) c = u32x4{0, 0, 0, 0};
    const u32x4 p = (c + 3) & ~3u;
    const uint32_t s1 = p[0] + p[1], s2 = s1 + p[2], tot = s2 + p[3];
    const uint32_t inc = wave_scan_add(tot), ex = inc - tot;
    *(u32x4*)(smem + L_OFF + ln_ * 16) = ln_ < NPB / 4 ? u32x4{ex, ex + p[0], ex + s1, ex + s2} : u32x4{SORT_CAP, SORT_CAP, SORT_CAP, SORT_CAP};
    const uint32_t T4 = (uint32_t)__builtin_amdgcn_readlane((int)inc, 63);
    const uint32_t T32 = (T4 + 31) & ~31u;
    if ((T4 >> 2) + ln_ < (T32 >> 2) + 2) *(uint32_t*)(smem + L_QN + ((T4 >> 2) + ln_) * 4) = QN_DUMMY;
    if (ln_ == 0) { misc[M_NSTEP] = T32 >> 5; misc[M_T4] = T4; }
  };
  auto scatter = [&](int tt) {
    if (wv < 12) {
#pragma unroll
      for (int i = 0; i < 6; i++) {
        if ((i & 1) == 0 || part1) {
#pragma unroll
          for (int k = 0; k < 4; k++) {
            const uint32_t pos = min(*(const uint32_t*)(smem + L_OFF + ((R.lc[i] >> (8 * k)) & 0xffu) * 4) + ((rk[i][k >> 1] >> (16 * (k & 1))) & 0xffffu), (uint32_t)(SORT_CAP - 1));
            const uint32_t rec = R.rc[i][k];
            *(u32x2*)(smem + L_SORT + pos * 8) = u32x2{(rec & 0xffffu) << 7, rec & 0xffff0000u};
          }
        }
      }
    }
    u32x2 dummy = {(uint32_t)tt, 0u};
    asm volatile("" : "+v"(dummy));
    dummy = u32x2{dummy[0] * 0u + OFF_DUMMY, 0u};
    const int u = 1023 - tt;
    if (u < NPB * 4) {
      const uint32_t nd = u >> 2, k = u & 3;
      const uint32_t c = *(const uint32_t*)(smem + L_CNT + nd * 4), o = *(const uint32_t*)(smem + L_OFF + nd * 4);
      const uint32_t val = nd * (128u + (AGG_ROW << 16));
      for (uint32_t q = k; 4 * q < c; q += 4) *(uint32_t*)(smem + L_QN + o + q * 4) = val;
      if (k == 3) *(uint32_t*)(smem + L_NQ + nd * 4) += (c + 3) >> 2;
      if (k < 3 && c + k < ((c + 3) & ~3u)) *(u32x2*)(smem + L_SORT + (o + c + k) * 8) = dummy;
    } else if (u < NPB * 4 + 64) {
      const uint32_t T4 = misc[M_T4], T32 = (T4 + 31) & ~31u, i = T4 + (u - NPB * 4);
      if (i < T32 + 8) *(u32x2*)(smem + L_SORT + i * 8) = dummy;
    }
  };
  auto lane_consts = [&](int tt, int nstep) {
    const int n_ = (nstep + 15 - wv) >> 4, first = 8 * (wv * (nstep >> 4) + min(wv, nstep & 15));
    const int g = (tt & 63) >> 3, j = tt & 7;
    L.rec = lb + L_SORT + (first + g * n_) * 32;
    L.qn  = lb + L_QN + (first + g * n_) * 4;
    L.c16 = j * 16;
    L.ea  = lb + L_EA + j * 16;
    L.agg = lb + L_AGG + j * 4;
    asm volatile("" : "+v"(L.rec), "+v"(L.qn), "+v"(L.ea), "+v"(L.agg));
    return n_;
  };

  int take = 64;
  if (wv < 12) {
    poll_segments(tb >> 3);
    TS(4);
    load_recs(R, sg, ws, tb >> 3, tb & 7, 0);
    post_total(0, tb & 7, lb_);
    uint32_t v = (tb & 7) == 0 ? (sg[0] >> 16) + (sg[1] >> 16) + (sg[2] >> 16) : 0u;
    v = wave_scan_add(v);
    if (lb_ == 63) misc[M_ALL + wv] = v;
    WAIT_RECS(R);
    TS(5);
    rank(tb, 0, take);
  }
  LDS_BARRIER();
  if (w == 0 && tb == 0) __hip_atomic_store((gu64*)(ws + WS_NONCE), (((u64)xn[1] << 32) | xn[0]) + 1ull, RLX_AGENT);
  if (total() > TAKE_MAX) {
    LDS_BARRIER();
    if (tb < 256) *(uint32_t*)(smem + L_CNT + tb * 4) = 0;
    LDS_BARRIER();
    take = 16;
    if (wv < 12) rank(tb, 0, take);
    LDS_BARRIER();
  }
  if (wv == 0) scan(lb_);
  else if (wv == 11) {
    gu64* gt = (gu64*)(ws + WS_TBG) + 2 * lb_;
    uint32_t mb, mv, me; float sq;
    for (unsigned spins = 0;; spins++) {
      bool ok = true; mb = mv = me = 0; sq = 0.f;
#pragma unroll
      for (int i = 0; i < 4; i++) {
        const u64 v0 = __hip_atomic_load(gt + 128 * i, RLX_AGENT), v1 = __hip_atomic_load(gt + 128 * i + 1, RLX_AGENT);
        ok &= (uint32_t)(v0 >> 32) == tag32 && (uint32_t)(v1 >> 32) == tag32;
        mb = max(mb, (uint32_t)v0 << 16); mv = max(mv, (uint32_t)v0 & 0xffff0000u);
        me = max(me, (uint32_t)v1 << 16); sq += __uint_as_float((uint32_t)v1 & 0xffff0000u);
      }
      if (__all(ok)) break;
      if (spins > SPIN_CAP) break;
      __builtin_amdgcn_s_sleep(8);
    }
    mb = wave_max_u32(mb); mv = wave_max_u32(mv); me = wave_max_u32(me); sq = wave_sum_f32(sq);
    if (lb_ == 63) *(u32x4*)(misc + M_BND) = u32x4{mb, mv, me, __float_as_uint(sq)};
  }
  LDS_BARRIER();
  TS(6);
  bool fast; float delta;
  {
    const u32x4 bd = *(const u32x4*)(misc + M_BND);
    const u32x4 x0 = *(const u32x4*)(misc + M_XAM), x1 = *(const u32x4*)(misc + M_XAM + 4);
    const uint32_t ma = max(max(max(x0[0], x0[1]), max(x0[2], x0[3])), max(max(x1[0], x1[1]), x1[2]));
    const float mv = __uint_as_float(bd[1]);
    const float yb = (__uint_as_float(ma) + __uint_as_float(bd[0]) + __uint_as_float(bd[2]) * __uint_as_float(misc[M_CMX])) * 1.01f;
    const u32x4 s = *(const u32x4*)(misc + M_ALL) + *(const u32x4*)(misc + M_ALL + 4) + *(const u32x4*)(misc + M_ALL + 8);
    const float all = (float)((s[0] + s[1]) + (s[2] + s[3]) + 4u);
    const int p1 = (int)(__float_as_uint(4.1f * mv) >> 23) - 127 + 2, p2 = (int)(__float_as_uint(all * mv * 1.01f) >> 23) - 127 + 1 - 8;
    const int p = max(max(p1, p2), -80);
    delta = __uint_as_float((uint32_t)(p - 23 + 127) << 23);
    const float mg = __uint_as_float(((uint32_t)(p + 127) << 23) | 0x400000u);
    const float mgs = __uint_as_float(__builtin_amdgcn_readfirstlane(__float_as_uint(mg)));
    L.magic = f32x2{mgs, mgs};
    delta = __uint_as_float(__builtin_amdgcn_readfirstlane(__float_as_uint(delta)));
    const bool fine = delta * 4096.f <= __builtin_sqrtf(__uint_as_float(bd[3]) / (64.f * (float)N)) || mv == 0.f;
    fast = __builtin_amdgcn_readfirstlane((yb <= 28.f) && (mv <= 1.0e12f) && fine);
  }
  if (wv >= 12 && tile < NTILE) {
    WAIT_VM(0);
    if (arrive(&misc[M_CT], lb_) == ntw - 1 && lb_ == 0) { __hip_atomic_store((gu64*)(ws + WS_TRG) + w, (u64)tag32 << 32, RLX_AGENT); TS_PUB(1); }
  }
  scatter(tb);
  for (int i = tb; i < 197 * AGG_ROW / 16; i += 1024) *(u32x4*)(smem + L_AGG + i * 16) = u32x4{0, 0, 0, 0};
  if (fast) {
    for (int i = tb; i < NPB * 8; i += 1024) {
      f32x4 v = *(const f32x4*)(smem + L_EA + i * 16);
      v[0] = __builtin_amdgcn_exp2f(v[0]); v[1] = __builtin_amdgcn_exp2f(v[1]); v[2] = __builtin_amdgcn_exp2f(v[2]); v[3] = __builtin_amdgcn_exp2f(v[3]);
      *(f32x4*)(smem + L_EA + i * 16) = v;
    }
  }
  if (wv == 11) {
    gu64* gt = (gu64*)(ws + WS_TRG) + lb_;
    for (unsigned spins = 0; spins < SPIN_CAP; spins++) {
      bool ok = true;
#pragma unroll
      for (int i = 0; i < 4; i++) ok &= (uint32_t)(__hip_atomic_load(gt + 64 * i, RLX_AGENT) >> 32) == tag32;
      if (__all(ok)) break;
      __builtin_amdgcn_s_sleep(4);
    }
  }
  int again = __builtin_amdgcn_readfirstlane(misc[M_MORE]);
  LDS_BARRIER();
  int nstep = __builtin_amdgcn_readfirstlane(misc[M_NSTEP]);
  TS(7);
  {
    const int n_ = lane_consts(tb, nstep);
    if (fast) edge_loop<0>(L, n_, tsrd);
    else      edge_loop<2>(L, n_, tsrd);
  }
  while (again) {
    done += take;
    const int t2 = thread_id();
    if (wv < 12) { poll_segments(t2 >> 3); load_recs(R, sg, ws, t2 >> 3, t2 & 7, done); WAIT_RECS(R); }
    LDS_BARRIER();
    if (t2 < 256) *(uint32_t*)(smem + L_CNT + t2 * 4) = 0;
    if (t2 == 0) misc[M_MORE] = 0;
    if (wv < 12) post_total(done, t2 & 7, t2 & 63);
    LDS_BARRIER();
    take = total() <= TAKE_MAX ? 64 : 16;
    if (wv < 12) rank(t2, done, take);
    LDS_BARRIER();
    if (wv == 0) scan(t2 & 63);
    LDS_BARRIER();
    scatter(t2);
    again = __builtin_amdgcn_readfirstlane(misc[M_MORE]);
    LDS_BARRIER();
    nstep = __builtin_amdgcn_readfirstlane(misc[M_NSTEP]);
    const int n_ = lane_consts(t2, nstep);
    if (fast) edge_loop<0>(L, n_, tsrd);
    else      edge_loop<2>(L, n_, tsrd);
  }
  LDS_BARRIER();

  const int te = thread_id();
  if (te < NPB * 4) {
    const int nl = te >> 2, u = te & 3, node = b * NPB + nl;
    if (node < N) {
      float y[8];
      const uint32_t bias = *(const uint32_t*)(smem + L_NQ + nl * 4) * __float_as_uint(L.magic[0]);
      const f32x4 u0 = *(const f32x4*)(smem + L_XU + nl * 128 + u * 32), u1 = *(const f32x4*)(smem + L_XU + nl * 128 + u * 32 + 16);
#pragma unroll
      for (int i = 0; i < 4; i++) {
        const u32x2 v2 = *(const u32x2*)(smem + L_AGG + nl * AGG_ROW + (8 * i + 2 * u) * 4);
#pragma unroll
        for (int j = 0; j < 2; j++) {
          const float agg = fast ? (float)(int)(v2[j] - bias) * delta : __uint_as_float(v2[j]);
          const float v = (j ? u1[i] : u0[i]) + agg;
          y[4 * j + i] = v * __builtin_amdgcn_rcpf(1.f + __builtin_amdgcn_exp2f(v * NLOG2E));
        }
      }
      const u32x4 o = {cvtpk(y[0], y[1]), cvtpk(y[2], y[3]), cvtpk(y[4], y[5]), cvtpk(y[6], y[7])};
      *(u32x4*)(out + (size_t)node * 64 + 32 * half + 8 * u) = o;
    }
  }
  TS_OUT(256 + blockIdx.x);
}

}

extern "C" void kernel_launch(void* const* d_in, const int* in_sizes, int n_in,
                              void* d_out, int out_size, void* d_ws, size_t ws_size,
                              hipStream_t stream) {
  const uint16_t* x  = (const uint16_t*)d_in[0];
  const uint16_t* ea = (const uint16_t*)d_in[1];
  const int*      src = (const int*)d_in[2];
  const int*      dst = (const int*)d_in[3];
  const uint16_t* uw = (const uint16_t*)d_in[4];
  const uint16_t* vw = (const uint16_t*)d_in[5];
  const uint16_t* gw = (const uint16_t*)d_in[6];
  const int N = in_sizes[0] / 64, E = in_sizes[2];
  char* ws = (char*)d_ws + ((size_t)100 << 20);
  (void)hipFuncSetAttribute((const void*)k_mpnn, hipFuncAttributeMaxDynamicSharedMemorySize, L_TOTAL);
  k_mpnn<<<NCH, 1024, L_TOTAL, stream>>>(src, dst, ea, x, uw, vw, gw, ws, (uint16_t*)d_out, N, E);
}
```

```cpp
#include <hip/hip_runtime.h>
#include <hip/hip_bf16.h>
#include <stdint.h>

#pragma clang diagnostic ignored "-Wint-to-pointer-cast"
#pragma clang diagnostic ignored "-Wpointer-to-int-cast"
namespace {

typedef float    f32x2  __attribute__((ext_vector_type(2)));
typedef float    f32x4  __attribute__((ext_vector_type(4)));
typedef float    f32x16 __attribute__((ext_vector_type(16)));
typedef short    bf16x8 __attribute__((ext_vector_type(8)));
typedef short    s16x4  __attribute__((ext_vector_type(4)));
typedef uint32_t u32x2  __attribute__((ext_vector_type(2)));
typedef uint32_t u32x4  __attribute__((ext_vector_type(4)));
typedef unsigned long long u64;
typedef __attribute__((address_space(1))) unsigned long long gu64;
#define LDSP(T) __attribute__((address_space(3))) T*
#define DI __device__ __forceinline__
#define RLX_AGENT __ATOMIC_RELAXED, __HIP_MEMORY_SCOPE_AGENT
#define RLX_WG    __ATOMIC_RELAXED, __HIP_MEMORY_SCOPE_WORKGROUP

constexpr int   NCH = 256;
constexpr int   NB  = 128;
constexpr int   NPB = 196;
constexpr int   CH  = 3128;
constexpr int   CHB = 880;
constexpr int   NMAX = NB * NPB;
constexpr int   NTILE = NMAX / 32;
constexpr float NLOG2E = -1.4426950408889634f;
constexpr int   AUX_SC1 = 16;
constexpr unsigned SPIN_CAP = 1u << 18;
static_assert(4 * CHB >= CH + 3 * NB && (CHB * 20) % 16 == 0, "blocks");

constexpr size_t WS_NONCE = 0;
constexpr size_t WS_SEG = 128;
constexpr size_t WS_TBG = WS_SEG + (size_t)NCH * NB * 8;
constexpr size_t WS_TRG = WS_TBG + (size_t)NCH * 16;
constexpr size_t WS_REC = WS_TRG + (size_t)NCH * 8;
constexpr size_t WS_TAB = (WS_REC + (size_t)NCH * CHB * 20 + 1024 + 127) & ~(size_t)127;
constexpr size_t WS_DBG = WS_TAB + (size_t)2 * NMAX * 128;
static_assert(WS_REC % 128 == 0 && WS_TAB % 128 == 0, "ws align");

constexpr int AGG_ROW  = 160;
constexpr int SORT_CAP = 8128;
constexpr int TAKE_MAX = SORT_CAP - 3 * NPB - 48;
constexpr int L_CNT    = 0;
constexpr int L_OFF    = L_CNT + 1024;
constexpr int L_NQ     = L_OFF + 1024;
constexpr int L_MISC   = L_NQ + 1024;
constexpr int L_QN     = L_MISC + 512;
constexpr int L_EA     = L_QN + SORT_CAP;
constexpr int L_XU     = L_EA + 197 * 128;
constexpr int L_AGG    = (L_XU + 196 * 128 + 127) & ~127;
constexpr int L_SORT   = (L_AGG + 197 * AGG_ROW + 127) & ~127;
constexpr int L_TOTAL  = L_SORT + SORT_CAP * 8;
static_assert(L_TOTAL <= 163840, "LDS");
static_assert((L_QN & 15) == 0 && (L_EA & 15) == 0 && (L_XU & 15) == 0 && TAKE_MAX >= 4096, "align");
constexpr int A_HIST = L_SORT;
constexpr int A_BASE = A_HIST + 512;
constexpr int A_ST   = A_BASE + 512;
constexpr int A_W    = (A_ST + CHB * 20 + 127) & ~127;
constexpr int A_IMG  = A_W + 16384;
constexpr int P_W    = A_IMG + 16384;
constexpr int P_X    = L_AGG;
static_assert(P_W + 8192 <= L_TOTAL && P_X + 224 * 128 <= L_SORT, "phase A images");
constexpr uint32_t QN_DUMMY = (196u * 128u) | ((196u * AGG_ROW) << 16);
constexpr uint32_t OFF_DUMMY = 0x7fffff00u;
constexpr int M_MORE = 0, M_NSTEP = 1, M_T4 = 2;
constexpr int M_CW = 3, M_CB = 4, M_CT = 5, M_PUB = 6;
constexpr int M_CL = 7, M_CE = 8, M_CP = 9;
constexpr int M_TOT = 10, M_ALL = 11, M_XAM = 12;
constexpr int M_FAST = 13, M_EXP = 14, M_GB = 15, M_CI = 24;
constexpr int M_MX = 16;

DI float    bf2f(uint32_t v16) { return __uint_as_float(v16 << 16); }
DI uint32_t cvtpk(float lo, float hi) {
  typedef __bf16 bf16x2 __attribute__((ext_vector_type(2)));
  return __builtin_bit_cast(uint32_t, __builtin_convertvector(f32x2{lo, hi}, bf16x2));
}
DI f32x16   mfma(bf16x8 a, bf16x8 b, f32x16 c) { return __builtin_amdgcn_mfma_f32_32x32x16_bf16(a, b, c, 0, 0, 0); }
DI int      crow(int r, int h) { return (r & 3) + 8 * (r >> 2) + 4 * h; }
DI uint32_t absbits(float v) { return __float_as_uint(v) & 0x7fffffffu; }

template <int CTRL, int RM> DI uint32_t dpp0(uint32_t v) { return (uint32_t)__builtin_amdgcn_update_dpp(0, (int)v, CTRL, RM, 0xf, false); }
DI uint32_t wave_scan_add(uint32_t v) {
  v += dpp0<0x111, 0xf>(v); v += dpp0<0x112, 0xf>(v); v += dpp0<0x114, 0xf>(v); v += dpp0<0x118, 0xf>(v);
  v += dpp0<0x142, 0xa>(v); v += dpp0<0x143, 0xc>(v);
  return v;
}
DI uint32_t wave_max_u32(uint32_t v) {
  v = max(v, dpp0<0x111, 0xf>(v)); v = max(v, dpp0<0x112, 0xf>(v)); v = max(v, dpp0<0x114, 0xf>(v)); v = max(v, dpp0<0x118, 0xf>(v));
  v = max(v, dpp0<0x142, 0xa>(v)); v = max(v, dpp0<0x143, 0xc>(v));
  return v;
}
DI float wave_sum_f32(float s) {
  s += __uint_as_float(dpp0<0x111, 0xf>(__float_as_uint(s))); s += __uint_as_float(dpp0<0x112, 0xf>(__float_as_uint(s)));
  s += __uint_as_float(dpp0<0x114, 0xf>(__float_as_uint(s))); s += __uint_as_float(dpp0<0x118, 0xf>(__float_as_uint(s)));
  s += __uint_as_float(dpp0<0x142, 0xa>(__float_as_uint(s))); s += __uint_as_float(dpp0<0x143, 0xc>(__float_as_uint(s)));
  return s;
}
#define LDS_BARRIER() asm volatile("s_waitcnt lgkmcnt(0)\n\ts_barrier" ::: "memory")
#define WAIT_VM(N) asm volatile("s_waitcnt vmcnt(" #N ")" ::: "memory")

DI uint32_t arrive(uint32_t* word, int lane) {
  asm volatile("s_waitcnt lgkmcnt(0)" ::: "memory");
  uint32_t v = 0;
  if (lane == 0) v = __hip_atomic_fetch_add(word, 1u, RLX_WG);
  return __builtin_amdgcn_readfirstlane(v);
}
DI void await_lds(uint32_t* word, uint32_t want) {
  for (unsigned spins = 0; spins < SPIN_CAP; spins++) {
    if (__builtin_amdgcn_readfirstlane(__hip_atomic_load(word, RLX_WG)) >= want) break;
    __builtin_amdgcn_s_sleep(1);
  }
  asm volatile("" ::: "memory");
}

DI void dma16(uint32_t ldsaddr, uint32_t voff, __amdgpu_buffer_rsrc_t srd) {
  asm volatile("s_mov_b32 m0, %0\n\ts_nop 0\n\tbuffer_load_dwordx4 %1, %2, 0 offen lds" :: "s"(ldsaddr), "v"(voff), "s"(srd) : "memory");
}
DI bf16x8 tr_frag(uint32_t addr, int four_rows) {
  const s16x4 lo = __builtin_amdgcn_ds_read_tr16_b64_v4i16((LDSP(s16x4))addr);
  const s16x4 hi = __builtin_amdgcn_ds_read_tr16_b64_v4i16((LDSP(s16x4))(addr + four_rows));
  return (bf16x8)__builtin_shufflevector(lo, hi, 0, 1, 2, 3, 4, 5, 6, 7);
}

#define TS(i) do { } while (0)
#define TS_DECL do { } while (0)
#define TS_OUT(slot) do { } while (0)
#define TS_PUB(i) do { } while (0)

DI uint32_t shape(float v, uint32_t low) {
  const uint32_t b = __float_as_uint(v);
  const uint32_t t = min(__builtin_elementwise_sub_sat((b & 0x7fffffffu) + 0x8000u, low), 0x7fffffffu);
  return (b & 0x80000000u) | (t & 0xffff0000u) | low;
}
DI void table_half(u32x4* o, const bf16x8* xf, uint32_t troff, int c, float& mb, float& mv, float& s2) {
  f32x16 zb, hv;
#pragma unroll
  for (int i = 0; i < 16; i++) { zb[i] = 0.f; hv[i] = 0.f; }
#pragma unroll
  for (int s = 0; s < 4; s++) {
    zb = mfma(tr_frag(troff + s * 2048 + c * 64, 512), xf[s], zb);
    hv = mfma(tr_frag(troff + 8192 + s * 2048 + c * 64, 512), xf[s], hv);
  }
#pragma unroll
  for (int q = 0; q < 4; q++) {
#pragma unroll
    for (int p = 0; p < 2; p++) {
      const int i = 4 * q + 2 * p;
      const float b0 = zb[i] * NLOG2E, b1 = zb[i + 1] * NLOG2E, v0 = hv[i], v1 = hv[i + 1];
      mb = fmaxf(mb, fmaxf(fabsf(b0), fabsf(b1))); mv = fmaxf(mv, fmaxf(fabsf(v0), fabsf(v1)));
      s2 = fmaf(v0, v0, s2); s2 = fmaf(v1, v1, s2);
      const uint32_t d0 = shape(b0, cvtpk(v0, 0.f));
      const uint32_t w1 = shape(v1, d0 >> 16);
      o[q][2 * p] = d0; o[q][2 * p + 1] = shape(b1, w1 >> 16);
    }
  }
}
DI void table_image(const u32x4* o, uint32_t img, int lane) {
  const int n31 = lane & 31, h = lane >> 5;
#pragma unroll
  for (int q = 0; q < 4; q++) *(LDSP(u32x4))(img + n31 * 128 + (((2 * q + h) ^ (n31 & 7)) * 16)) = o[q];
  asm volatile("s_waitcnt lgkmcnt(0)" ::: "memory");
}
DI void table_rows(u32x4* r, uint32_t img, int lane) {
#pragma unroll
  for (int i = 0; i < 4; i++) { const int row = 8 * i + (lane >> 3); r[i] = *(LDSP(const u32x4))(img + row * 128 + (((lane & 7) ^ (row & 7)) * 16)); }
  asm volatile("s_waitcnt lgkmcnt(0)" : "+v"(r[0]), "+v"(r[1]), "+v"(r[2]), "+v"(r[3]) :: "memory");
}
DI void table_store(const u32x4* r, int tile, int c, __amdgpu_buffer_rsrc_t tsrd, int lane) {
#pragma unroll
  for (int i = 0; i < 4; i++)
    __builtin_amdgcn_raw_buffer_store_b128(r[i], tsrd, (c * NMAX + tile * 32 + 8 * i + (lane >> 3)) * 128 + (lane & 7) * 16, 0, AUX_SC1);
}

DI f32x2 exp2x2(f32x2 v) { f32x2 r = {__builtin_amdgcn_exp2f(v[0]), __builtin_amdgcn_exp2f(v[1])}; return r; }
DI f32x2 rcpx2(f32x2 v) { f32x2 r = {__builtin_amdgcn_rcpf(v[0]), __builtin_amdgcn_rcpf(v[1])}; return r; }
DI f32x2 fma2(f32x2 a, f32x2 b, f32x2 c) { return __builtin_elementwise_fma(a, b, c); }
template <int MUL> DI uint32_t madlo_i(uint32_t v, uint32_t add) {
  uint32_t r; asm("v_mad_u32_u16 %0, %1, %3, %2" : "=v"(r) : "v"(v), "v"(add), "i"(MUL)); return r;
}
template <int MUL> DI uint32_t madhi_i(uint32_t v, uint32_t add) {
  uint32_t r; asm("v_mad_u32_u16 %0, %1, %3, %2 op_sel:[1,0,0,0]" : "=v"(r) : "v"(v), "v"(add), "i"(MUL)); return r;
}
template <int K> DI uint32_t byte_x4(uint32_t v) {
  uint32_t r;
  if (K == 0) asm("v_lshlrev_b32_sdwa %0, 2, %1 dst_sel:DWORD dst_unused:UNUSED_PAD src0_sel:DWORD src1_sel:BYTE_0" : "=v"(r) : "v"(v));
  if (K == 1) asm("v_lshlrev_b32_sdwa %0, 2, %1 dst_sel:DWORD dst_unused:UNUSED_PAD src0_sel:DWORD src1_sel:BYTE_1" : "=v"(r) : "v"(v));
  if (K == 2) asm("v_lshlrev_b32_sdwa %0, 2, %1 dst_sel:DWORD dst_unused:UNUSED_PAD src0_sel:DWORD src1_sel:BYTE_2" : "=v"(r) : "v"(v));
  if (K == 3) asm("v_lshlrev_b32_sdwa %0, 2, %1 dst_sel:DWORD dst_unused:UNUSED_PAD src0_sel:DWORD src1_sel:BYTE_3" : "=v"(r) : "v"(v));
  return r;
}
DI uint32_t byte_x4(uint32_t v, int k) { return k == 0 ? byte_x4<0>(v) : (k == 1 ? byte_x4<1>(v) : (k == 2 ? byte_x4<2>(v) : byte_x4<3>(v))); }
DI uint32_t lo_x128(uint32_t v) { uint32_t r; asm("v_mad_u32_u16 %0, %1, %2, 0" : "=v"(r) : "v"(v), "s"(128u)); return r; }
DI void lds_add_u32(uint32_t addr, float v) { __hip_atomic_fetch_add((LDSP(uint32_t))addr, __float_as_uint(v), RLX_WG); }
DI void lds_add_f32(uint32_t addr, float v) { __hip_atomic_fetch_add((LDSP(float))addr, v, RLX_WG); }

struct Lane {
  uint32_t rec;
  uint32_t qn;
  uint32_t c16;
  uint32_t ea, agg;
  f32x2 c01, c23;
  f32x2 magic;
};
struct Quad {
  u32x4 d[4];
  u32x4 r0, r1;
  uint32_t q;
};
template <int S> DI void fetch(Quad& Q, const Lane& L, __amdgpu_buffer_rsrc_t srd) {
  Q.r0 = *(LDSP(const u32x4))(L.rec + S * 32);
  Q.r1 = *(LDSP(const u32x4))(L.rec + S * 32 + 16);
  Q.q  = *(LDSP(const uint32_t))(L.qn + S * 4);
  Q.d[0] = __builtin_amdgcn_raw_buffer_load_b128(srd, Q.r0[0] + L.c16, 0, AUX_SC1);
  Q.d[1] = __builtin_amdgcn_raw_buffer_load_b128(srd, Q.r0[2] + L.c16, 0, AUX_SC1);
  Q.d[2] = __builtin_amdgcn_raw_buffer_load_b128(srd, Q.r1[0] + L.c16, 0, AUX_SC1);
  Q.d[3] = __builtin_amdgcn_raw_buffer_load_b128(srd, Q.r1[2] + L.c16, 0, AUX_SC1);
  asm volatile("" ::: "memory");
}
DI f32x2 fma_e(u32x2 rec, f32x2 c, u32x2 xb) {
  f32x2 r; asm("v_pk_fma_f32 %0, %1, %2, %3 op_sel:[1,0,0] op_sel_hi:[1,1,1]" : "=v"(r) : "v"(rec), "v"(c), "v"(xb)); return r;
}
DI f32x2 vpair(u32x2 d) {
  f32x2 r; asm("v_lshlrev_b64 %0, 16, %1" : "=v"(r) : "v"(d)); return r;
}
template <int MODE> DI void gate(const Quad& Q, const Lane& L) {
  const f32x4 ea = *(LDSP(const f32x4))(madlo_i<1>(Q.q, L.ea));
  const uint32_t ga = madhi_i<1>(Q.q, L.agg);
  const u32x2 rec[4] = {u32x2{Q.r0[0], Q.r0[1]}, u32x2{Q.r0[2], Q.r0[3]}, u32x2{Q.r1[0], Q.r1[1]}, u32x2{Q.r1[2], Q.r1[3]}};
  const f32x2 one = {1.f, 1.f};
#pragma unroll
  for (int p = 0; p < 2; p++) {
    const f32x2 c = p ? L.c23 : L.c01, eap = {ea[2 * p], ea[2 * p + 1]};
    f32x2 a[4], hv[4];
#pragma unroll
    for (int k = 0; k < 4; k++) {
      const u32x2 dd = {Q.d[k][2 * p], Q.d[k][2 * p + 1]};
      hv[k] = vpair(dd);
      const f32x2 y = fma_e(rec[k], c, dd);
      a[k] = MODE == 2 ? one + exp2x2(y + eap) : fma2(eap, exp2x2(y), one);
    }
    if (MODE == 0) {
      const f32x2 d01 = a[0] * a[1], n01 = fma2(hv[1], a[0], hv[0] * a[1]);
      const f32x2 d23 = a[2] * a[3], n23 = fma2(hv[3], a[2], hv[2] * a[3]);
      const f32x2 m = fma2(fma2(n23, d01, n01 * d23), rcpx2(d01 * d23), L.magic);
      lds_add_u32(ga + 64 * p, m[0]); lds_add_u32(ga + 64 * p + 32, m[1]);
    } else {
      const f32x2 m = (hv[0] * rcpx2(a[0]) + hv[1] * rcpx2(a[1])) + (hv[2] * rcpx2(a[2]) + hv[3] * rcpx2(a[3]));
      lds_add_f32(ga + 64 * p, m[0]); lds_add_f32(ga + 64 * p + 32, m[1]);
    }
  }
}
template <int MODE> DI void edge_loop(Lane& L, int n, __amdgpu_buffer_rsrc_t srd) {
  if (n <= 0) return;
  Quad A, B;
  fetch<0>(A, L, srd);
  while (true) {
    fetch<1>(B, L, srd);
    gate<MODE>(A, L);
    if (--n == 0) break;
    fetch<2>(A, L, srd);
    gate<MODE>(B, L);
    if (--n == 0) break;
    L.rec += 64; L.qn += 8;
  }
}

struct Recs { u32x4 rc[6]; uint32_t lc[6]; };
DI void load_recs(Recs& R, const uint32_t* sg, const char* ws, int g, int j, int done) {
  const __amdgpu_buffer_rsrc_t rsrd = __builtin_amdgcn_make_buffer_rsrc((void*)(ws + WS_REC), (short)0, NCH * CHB * 20, 0x00020000);
#pragma unroll
  for (int p = 0; p < 2; p++) {
#pragma unroll
    for (int r = 0; r < 3; r++) {
      uint32_t off = (uint32_t)((g + 96 * r) * (CHB * 20) + ((int)(sg[r] & 0xffff) + (done >> 2) + 8 * p + j) * 20);
      if ((int)(sg[r] >> 16) - done <= 32 * p + 4 * j) off = 0x40000000u;
      asm volatile("buffer_load_dwordx4 %0, %1, %2, 0 offen sc1" : "=&v"(R.rc[2 * r + p]) : "v"(off), "s"(rsrd) : "memory");
      asm volatile("buffer_load_dword %0, %1, %2, 0 offen offset:16 sc1" : "=&v"(R.lc[2 * r + p]) : "v"(off), "s"(rsrd) : "memory");
    }
  }
}
#define WAIT_RECS(R) asm volatile("s_waitcnt vmcnt(0)" : "+v"(R.rc[0]), "+v"(R.rc[1]), "+v"(R.rc[2]), "+v"(R.rc[3]), "+v"(R.rc[4]), "+v"(R.rc[5]), \
                                  "+v"(R.lc[0]), "+v"(R.lc[1]), "+v"(R.lc[2]), "+v"(R.lc[3]), "+v"(R.lc[4]), "+v"(R.lc[5]) :: "memory")

__global__ __launch_bounds__(1024) void k_mpnn(const int* __restrict__ src, const int* __restrict__ dst,
                                               const uint16_t* __restrict__ ea, const uint16_t* __restrict__ x,
                                               const uint16_t* __restrict__ uw, const uint16_t* __restrict__ vw,
                                               const uint16_t* __restrict__ gw, char* __restrict__ ws,
                                               uint16_t* __restrict__ out, int N, int E) {
  extern __shared__ __attribute__((aligned(128))) char smem[];
  const uint32_t lb = (uint32_t)(uintptr_t)(LDSP(char))smem;
  uint32_t* misc = (uint32_t*)(smem + L_MISC);
  const int t = threadIdx.x, lane = t & 63;
  const int w = blockIdx.x;
  const int half = (blockIdx.x & 7) >> 2, b = (blockIdx.x & 3) * 32 + (blockIdx.x >> 3);
  const int wv = __builtin_amdgcn_readfirstlane(t >> 6);
  const int r31 = lane & 31, h = lane >> 5;
  TS_DECL; TS(0);

  const int e0 = w * CH;
  const int n  = min(CH, E - e0);
  const int cnt = min(max(n - 4 * t, 0), 4);
  u32x2 xn; u32x4 d4, s4; u32x2 e2, cw;
  {
    const __amdgpu_buffer_rsrc_t dsrd = __builtin_amdgcn_make_buffer_rsrc((void*)dst, (short)0, E * 4, 0x00020000);
    const __amdgpu_buffer_rsrc_t ssrd = __builtin_amdgcn_make_buffer_rsrc((void*)src, (short)0, E * 4, 0x00020000);
    const __amdgpu_buffer_rsrc_t esrd = __builtin_amdgcn_make_buffer_rsrc((void*)ea, (short)0, (E * 2 + 3) & ~3, 0x00020000);
    const uint32_t eo = cnt > 0 ? (uint32_t)(e0 + 4 * t) : 0x20000000u;
    asm volatile("buffer_load_dwordx4 %0, %1, %2, 0 offen" : "=&v"(d4) : "v"(eo * 4), "s"(dsrd) : "memory");
    asm volatile("buffer_load_dwordx4 %0, %1, %2, 0 offen" : "=&v"(s4) : "v"(eo * 4), "s"(ssrd) : "memory");
    asm volatile("buffer_load_dwordx2 %0, %1, %2, 0 offen" : "=&v"(e2) : "v"(eo * 2), "s"(esrd) : "memory");
    asm volatile("global_load_dwordx2 %0, %1, %2 sc1" : "=&v"(xn) : "v"(0), "s"(ws + WS_NONCE) : "memory");
    asm volatile("global_load_dwordx2 %0, %1, %2" : "=&v"(cw) : "v"((128 * 64 + 32 * half + 4 * (t & 7)) * 2), "s"(gw) : "memory");
  }
  uint32_t* hist = (uint32_t*)(smem + A_HIST);
  uint32_t* base = (uint32_t*)(smem + A_BASE);
  if (t < 128) hist[t] = 0;
  if (t < 512) *(uint32_t*)(smem + (t < 256 ? L_CNT + t * 4 : L_NQ + (t - 256) * 4)) = 0;
  if (t >= 768 && t < 896) misc[t - 768] = 0;
  if (t >= 896 && t < 928) *(float*)(smem + L_EA + 196 * 128 + (t - 896) * 4) = 0.f;
  LDS_BARRIER();
  asm volatile("s_waitcnt vmcnt(0)" : "+v"(xn), "+v"(d4), "+v"(s4), "+v"(e2), "+v"(cw) :: "memory");
  const uint32_t tag32 = xn[0] + 1u;
  const u64 tag40 = ((((u64)xn[1] << 32) | xn[0]) + 1ull) & 0xffffffffffull;
  const uint32_t ev[4] = {e2[0] & 0xffffu, e2[0] >> 16, e2[1] & 0xffffu, e2[1] >> 16};
  uint32_t bk[4], rk0[4];
#pragma unroll
  for (int k = 0; k < 4; k++) {
    bk[k] = (d4[k] * 21400u) >> 22;
    rk0[k] = 0;
    if (k < cnt) rk0[k] = __hip_atomic_fetch_add(&hist[bk[k]], 1u, RLX_WG);
  }
  const __amdgpu_buffer_rsrc_t xsrd = __builtin_amdgcn_make_buffer_rsrc((void*)x, (short)0, N * 128, 0x00020000);
  const uint32_t img = lb + A_IMG + (wv & 3) * 4096;
  const uint32_t cA = (uint32_t)(((lane & 7) ^ (lane >> 4)) * 16);
  auto tile_rows = [&](uint32_t to, int tile) {
#pragma unroll
    for (int i = 0; i < 4; i++) dma16(to + i * 1024, (tile * 32 + 8 * i + (lane >> 3)) * 128 + ((i & 1) ? cA ^ 64u : cA), xsrd);
  };
  if (wv >= 13) {
    const __amdgpu_buffer_rsrc_t bsrd = __builtin_amdgcn_make_buffer_rsrc((void*)(gw + 64 * 64), (short)0, 8192, 0x00020000);
    const __amdgpu_buffer_rsrc_t vsrd = __builtin_amdgcn_make_buffer_rsrc((void*)vw, (short)0, 8192, 0x00020000);
    tile_rows(img, w + 256 * (wv - 13));
    if (wv == 13 && w + 768 < NTILE) tile_rows(lb + A_IMG, w + 768);
#pragma unroll
    for (int i = 0; i < 6; i++) {
      const int p = wv - 13 + 3 * i;
      if (p < 8) dma16(lb + A_W + p * 1024, p * 1024 + lane * 16, bsrd);
      else if (p < 16) dma16(lb + A_W + p * 1024, (p - 8) * 1024 + lane * 16, vsrd);
    }
  } else if (wv < 12) {
    const __amdgpu_buffer_rsrc_t asrd = __builtin_amdgcn_make_buffer_rsrc((void*)gw, (short)0, 64 * 128, 0x00020000);
    const __amdgpu_buffer_rsrc_t usrd = __builtin_amdgcn_make_buffer_rsrc((void*)uw, (short)0, 64 * 128, 0x00020000);
    const uint32_t woff = (lane >> 2) * 128 + half * 64 + (lane & 3) * 16;
#pragma unroll
    for (int i = 0; i < 3; i++) {
      const int p = wv + 12 * i;
      if (p < 4) dma16(lb + P_W + p * 1024, p * 2048 + woff, asrd);
      else if (p < 8) dma16(lb + P_W + p * 1024, (p - 4) * 2048 + woff, usrd);
      else if (p < 33) { const int q = p - 8; dma16(lb + P_X + q * 1024, (b * NPB + 8 * q + (lane >> 3)) * 128 + ((q & 1) ? cA ^ 64u : cA), xsrd); }
    }
  }
  TS(1);
  LDS_BARRIER();
  if (t < 64) {
    const u32x2 c = *(const u32x2*)(hist + 2 * t);
    const uint32_t p0 = (c[0] + 3) >> 2, tot = p0 + ((c[1] + 3) >> 2);
    const uint32_t ex = wave_scan_add(tot) - tot;
    *(u32x2*)(base + 2 * t) = u32x2{ex, ex + p0};
  }
  LDS_BARRIER();
  if (wv <= 12) {
#pragma unroll
    for (int k = 0; k < 4; k++) {
      if (k < cnt) {
        const uint32_t p = base[bk[k]] * 4 + rk0[k];
        const uint32_t o = (p >> 2) * 20 + (p & 3);
        *(uint32_t*)(smem + A_ST + o + (p & 3) * 3) = (s4[k] & 0xffffu) | (ev[k] << 16);
        *(uint8_t*)(smem + A_ST + o + 16) = (uint8_t)(d4[k] - bk[k] * NPB);
      }
    }
    const uint32_t m2 = wave_max_u32(max(max(ev[0] & 0x7fffu, ev[1] & 0x7fffu), max(ev[2] & 0x7fffu, ev[3] & 0x7fffu)) << 16);
    if (lane == 63) __hip_atomic_fetch_max(&misc[M_MX + 2], m2, RLX_WG);
  }
  LDS_BARRIER();
  TS(2);
  Lane L;
  if (wv <= 12) {
    const __amdgpu_buffer_rsrc_t rsrd = __builtin_amdgcn_make_buffer_rsrc((void*)(ws + WS_REC + (size_t)w * CHB * 20), (short)0, CHB * 20, 0x00020000);
    const uint32_t nb = base[127] + ((hist[127] + 3) >> 2);
#pragma unroll
    for (int i = 0; i < 2; i++) {
      const int c = t + 832 * i;
      if (c * 16 < (int)nb * 20) __builtin_amdgcn_raw_buffer_store_b128(*(const u32x4*)(smem + A_ST + c * 16), rsrd, c * 16, 0, AUX_SC1);
    }
    WAIT_VM(0);
    if (arrive(&misc[M_CW], lane) == 12) {
      const u32x2 c = *(const u32x2*)(hist + 2 * lane), o = *(const u32x2*)(base + 2 * lane);
      gu64* g = (gu64*)(ws + WS_SEG) + w * NB + 2 * lane;
      __hip_atomic_store(g,     (u64)(o[0] | (c[0] << 12)) | (tag40 << 24), RLX_AGENT);
      __hip_atomic_store(g + 1, (u64)(o[1] | (c[1] << 12)) | (tag40 << 24), RLX_AGENT);
      if (lane == 0) { __hip_atomic_store(&misc[M_PUB], 1u, RLX_WG); TS_PUB(0); }
    }
    if (wv >= 5 && wv < 12) {
      await_lds(&misc[M_CW], 13u);
      const int rt = wv - 5, row = 32 * rt + r31;
      const uint32_t xa = lb + P_X + row * 128 + ((h ^ ((row >> 1) & 7)) * 16);
      const uint32_t troff = lb + P_W + (8 * h + ((lane & 15) >> 2)) * 64 + (16 * ((lane >> 4) & 1) + 4 * (lane & 3)) * 2;
      bf16x8 a[4];
#pragma unroll
      for (int s = 0; s < 4; s++) a[s] = *(LDSP(const bf16x8))(xa ^ (32u * s));
      uint32_t xam = 0;
#pragma unroll
      for (int part = 0; part < 2; part++) {
        f32x16 acc;
#pragma unroll
        for (int i = 0; i < 16; i++) acc[i] = 0.f;
#pragma unroll
        for (int s = 0; s < 4; s++) acc = mfma(a[s], tr_frag(troff + part * 4096 + s * 1024, 256), acc);
        const uint32_t o = lb + (part ? L_XU : L_EA) + (32 * rt + 4 * h) * 128 + r31 * 4;
#pragma unroll
        for (int i = 0; i < 16; i++) {
          const float v = part ? acc[i] : acc[i] * NLOG2E;
          if (rt < 6 || crow(i, 0) + 4 * h < 4) { *(LDSP(float))(o + crow(i, 0) * 128) = v; if (!part) xam = max(xam, absbits(v)); }
        }
      }
      xam = wave_max_u32(xam);
      if (lane == 63) __hip_atomic_fetch_max(&misc[M_XAM], xam, RLX_WG);
      arrive(&misc[M_CE], lane);
    }
  } else {
    WAIT_VM(0);
    arrive(&misc[M_CL], lane);
    await_lds(&misc[M_CL], 3u);
    const __amdgpu_buffer_rsrc_t wsrd = __builtin_amdgcn_make_buffer_rsrc((void*)(ws + WS_TAB), (short)0, 2 * NMAX * 128, 0x00020000);
    const uint32_t troff = lb + A_W + (8 * h + ((lane & 15) >> 2)) * 128 + (16 * ((lane >> 4) & 1) + 4 * (lane & 3)) * 2;
    const uint32_t xa = img + r31 * 128 + ((h ^ ((r31 >> 1) & 7)) * 16);
    float mb = 0.f, mv = 0.f, s2 = 0.f;
    const int tile = w + 256 * (wv - 13);
    const bool extra = wv == 13 && w + 768 < NTILE;
    const uint32_t xe = xa - img + lb + A_IMG;
    u32x4 ra[4], rb[4], rc[4], rd[4];
    auto tile_halves = [&](uint32_t x0, u32x4* r0, u32x4* r1) {
      bf16x8 xf[4]; u32x4 o[4];
#pragma unroll
      for (int s = 0; s < 4; s++) xf[s] = *(LDSP(const bf16x8))(x0 ^ (32u * s));
      table_half(o, xf, troff, 0, mb, mv, s2);
      table_image(o, img, lane);
      table_rows(r0, img, lane);
      table_half(o, xf, troff, 1, mb, mv, s2);
      table_image(o, img, lane);
      table_rows(r1, img, lane);
    };
    tile_halves(xa, ra, rb);
    if (extra) tile_halves(xe, rc, rd);
    arrive(&misc[M_CI], lane);
    const uint32_t m0 = wave_max_u32(__float_as_uint(mb)), m1 = wave_max_u32(__float_as_uint(mv));
    s2 = wave_sum_f32(s2);
    if (lane == 63) {
      __hip_atomic_fetch_max(&misc[M_MX], m0, RLX_WG); __hip_atomic_fetch_max(&misc[M_MX + 1], m1, RLX_WG);
      misc[M_MX + 4 + (wv & 3)] = __float_as_uint(s2);
    }
    if (arrive(&misc[M_CB], lane) == 2 && lane == 0) {
      const u32x4 m = *(const u32x4*)(misc + M_MX), q = *(const u32x4*)(misc + M_MX + 4);
      const float sq = (__uint_as_float(q[1]) + __uint_as_float(q[2])) + __uint_as_float(q[3]);
      const uint32_t g0 = min((m[0] + 0xffffu) >> 16, 0x7fffu) | (min((m[1] + 0xffffu) >> 16, 0x7fffu) << 16);
      const uint32_t g1 = (m[2] >> 16) | (__float_as_uint(sq) & 0xffff0000u);
      gu64* g = (gu64*)(ws + WS_TBG) + 2 * w;
      __hip_atomic_store(g,     (u64)g0 | ((u64)tag32 << 32), RLX_AGENT);
      __hip_atomic_store(g + 1, (u64)g1 | ((u64)tag32 << 32), RLX_AGENT);
    }
    await_lds(&misc[M_PUB], 1u);
    table_store(ra, tile, 0, wsrd, lane);
    table_store(rb, tile, 1, wsrd, lane);
    if (extra) { table_store(rc, w + 768, 0, wsrd, lane); table_store(rd, w + 768, 1, wsrd, lane); }
    WAIT_VM(0);
    if (arrive(&misc[M_CT], lane) == 2 && lane == 0) { __hip_atomic_store((gu64*)(ws + WS_TRG) + w, (u64)tag32 << 32, RLX_AGENT); TS_PUB(1); }
  }
  L.c01 = f32x2{bf2f(cw[0] & 0xffffu) * NLOG2E, __uint_as_float(cw[0] & 0xffff0000u) * NLOG2E};
  L.c23 = f32x2{bf2f(cw[1] & 0xffffu) * NLOG2E, __uint_as_float(cw[1] & 0xffff0000u) * NLOG2E};
  TS(3);

  auto thread_id = [&]() {
    int v = wv * 64 + (int)__builtin_amdgcn_mbcnt_hi(~0u, __builtin_amdgcn_mbcnt_lo(~0u, 0u));
    asm volatile("" : "+v"(v));
    return v;
  };
  const int tb = thread_id(), lb_ = tb & 63;
  const __amdgpu_buffer_rsrc_t tsrd = __builtin_amdgcn_make_buffer_rsrc((void*)(ws + WS_TAB + (size_t)half * NMAX * 128), (short)0, N * 128, 0x00020000);
  uint32_t sg[3] = {0, 0, 0};
  auto poll_segments = [&](int g) {
    gu64* gs = (gu64*)(ws + WS_SEG) + g * NB + b;
    const bool third = g + 192 < NCH;
    for (unsigned spins = 0;; spins++) {
      const u64 v0 = __hip_atomic_load(gs, RLX_AGENT), v1 = __hip_atomic_load(gs + 96 * NB, RLX_AGENT);
      const u64 v2 = __hip_atomic_load(gs + (third ? 192 * NB : 0), RLX_AGENT);
      const bool ok = (v0 >> 24) == tag40 && (v1 >> 24) == tag40 && (v2 >> 24) == tag40;
      sg[0] = ((uint32_t)v0 & 0xfffu) | ((((uint32_t)v0 >> 12) & 0xfffu) << 16);
      sg[1] = ((uint32_t)v1 & 0xfffu) | ((((uint32_t)v1 >> 12) & 0xfffu) << 16);
      sg[2] = third ? ((uint32_t)v2 & 0xfffu) | ((((uint32_t)v2 >> 12) & 0xfffu) << 16) : 0u;
      if (__all(ok)) break;
      if (spins > SPIN_CAP) { sg[0] = sg[1] = sg[2] = 0; break; }
      __builtin_amdgcn_s_sleep(12);
    }
  };
  Recs R;
  int done = 0;
  uint32_t rk[6][2]; bool part1[3] = {false, false, false};
  auto post_total = [&](int dn, int j, int l) {
    uint32_t v = 0;
    if (j == 0) v = min(max((int)(sg[0] >> 16) - dn, 0), 64) + min(max((int)(sg[1] >> 16) - dn, 0), 64) + min(max((int)(sg[2] >> 16) - dn, 0), 64);
    v = wave_scan_add(v);
    if (l == 63) __hip_atomic_fetch_add(&misc[M_TOT], v, RLX_WG);
  };
  auto rank = [&](int tt, int dn, int take) {
    const int j = tt & 7;
    const uint32_t spare = (196u + (uint32_t)(tt & 63) % 60u) * 0x01010101u;
    bool more = false;
#pragma unroll
    for (int i = 0; i < 6; i++) {
      const int len = (int)(sg[i >> 1] >> 16) - dn;
      more |= len > take;
      const int v = min(max(min(len, take) - 32 * (i & 1) - 4 * j, 0), 4);
      const uint32_t drop = (uint32_t)(0xffffffffull << (8 * v));
      R.lc[i] = (R.lc[i] & ~drop) | (spare & drop);
      if (i & 1) part1[i >> 1] = __any(v > 0);
    }
    uint32_t r[3][4];
#pragma unroll
    for (int st = 0; st < 3; st++)
#pragma unroll
      for (int k = 0; k < 4; k++) r[st][k] = __hip_atomic_fetch_add((LDSP(uint32_t))(L_CNT + byte_x4(R.lc[2 * st], k)), 1u, RLX_WG);
#pragma unroll
    for (int st = 0; st < 3; st++) {
      rk[2 * st][0] = r[st][0] | (r[st][1] << 16); rk[2 * st][1] = r[st][2] | (r[st][3] << 16);
      asm volatile("" : "+v"(rk[2 * st][0]), "+v"(rk[2 * st][1]));
      rk[2 * st + 1][0] = rk[2 * st + 1][1] = 0;
      if (part1[st]) {
        uint32_t q[4];
#pragma unroll
        for (int k = 0; k < 4; k++) q[k] = __hip_atomic_fetch_add((LDSP(uint32_t))(L_CNT + byte_x4(R.lc[2 * st + 1], k)), 1u, RLX_WG);
        rk[2 * st + 1][0] = q[0] | (q[1] << 16); rk[2 * st + 1][1] = q[2] | (q[3] << 16);
      }
    }
    if (__any(more) && (tt & 63) == 0) misc[M_MORE] = 1;
  };
  auto scan = [&](int ln_) {
    u32x4 c = *(const u32x4*)(smem + L_CNT + ln_ * 16);
    if (ln_ >= NPB / 4) c = u32x4{0, 0, 0, 0};
    const u32x4 p = (c + 3) & ~3u;
    const uint32_t s1 = p[0] + p[1], s2 = s1 + p[2], tot = s2 + p[3];
    const uint32_t inc = wave_scan_add(tot), ex = inc - tot;
    *(u32x4*)(smem + L_OFF + ln_ * 16) = ln_ < NPB / 4 ? u32x4{ex, ex + p[0], ex + s1, ex + s2} : u32x4{SORT_CAP, SORT_CAP, SORT_CAP, SORT_CAP};
    const uint32_t T4 = (uint32_t)__builtin_amdgcn_readlane((int)inc, 63);
    const uint32_t T32 = (T4 + 31) & ~31u;
    if ((T4 >> 2) + ln_ < (T32 >> 2) + 2) *(uint32_t*)(smem + L_QN + ((T4 >> 2) + ln_) * 4) = QN_DUMMY;
    if (ln_ == 0) { misc[M_NSTEP] = T32 >> 5; misc[M_T4] = T4; }
  };
  auto scatter = [&](int tt) {
    if (wv < 12) {
#pragma unroll
      for (int i = 0; i < 6; i++) {
        if ((i & 1) == 0 || part1[i >> 1]) {
#pragma unroll
          for (int k = 0; k < 4; k++) {
            const uint32_t pos = min(*(LDSP(const uint32_t))(L_OFF + byte_x4(R.lc[i], k)) + ((rk[i][k >> 1] >> (16 * (k & 1))) & 0xffffu), (uint32_t)(SORT_CAP - 1));
            const uint32_t rec = R.rc[i][k];
            *(LDSP(u32x2))(lb + L_SORT + pos * 8) = u32x2{lo_x128(rec), rec & 0xffff0000u};
          }
        }
      }
    }
    u32x2 dummy = {(uint32_t)tt, 0u};
    asm volatile("" : "+v"(dummy));
    dummy = u32x2{dummy[0] * 0u + OFF_DUMMY, 0u};
    const int u = 831 - tt;
    if (u >= 0 && u < NPB * 4) {
      const uint32_t nd = u >> 2, k = u & 3;
      const uint32_t c = *(const uint32_t*)(smem + L_CNT + nd * 4), o = *(const uint32_t*)(smem + L_OFF + nd * 4);
      const uint32_t val = nd * (128u + (AGG_ROW << 16));
      for (uint32_t q = k; 4 * q < c; q += 4) *(uint32_t*)(smem + L_QN + o + q * 4) = val;
      if (k == 3) *(uint32_t*)(smem + L_NQ + nd * 4) += (c + 3) >> 2;
      if (k < 3 && c + k < ((c + 3) & ~3u)) *(u32x2*)(smem + L_SORT + (o + c + k) * 8) = dummy;
    }
    if (wv == 12) {
      const uint32_t T4 = misc[M_T4], T32 = (T4 + 31) & ~31u, i = T4 + (tt & 63);
      if (i < T32 + 8) *(u32x2*)(smem + L_SORT + i * 8) = dummy;
    }
  };
  auto lane_consts = [&](int tt, int nstep) {
    const int n_ = (nstep + 15 - wv) >> 4, first = 8 * (wv * (nstep >> 4) + min(wv, nstep & 15));
    const int g = (tt & 63) >> 3, j = tt & 7;
    L.rec = lb + L_SORT + (first + g * n_) * 32;
    L.qn  = lb + L_QN + (first + g * n_) * 4;
    L.c16 = j * 16;
    L.ea  = lb + L_EA + j * 16;
    L.agg = lb + L_AGG + j * 4;
    asm volatile("" : "+v"(L.rec), "+v"(L.qn), "+v"(L.ea), "+v"(L.agg));
    return n_;
  };

  int take = 64;
  bool fast = false; float delta = 0.f;
  uint32_t gb = 0;
  auto group_barrier = [&]() { gb += 13; arrive(&misc[M_GB], lb_); await_lds(&misc[M_GB], gb); };
  if (wv <= 12) {
    if (wv < 12) {
      await_lds(&misc[M_CE], 7u);
      poll_segments(tb >> 3);
      TS(4);
      load_recs(R, sg, ws, tb >> 3, tb & 7, 0);
      post_total(0, tb & 7, lb_);
      uint32_t v = (tb & 7) == 0 ? (sg[0] >> 16) + (sg[1] >> 16) + (sg[2] >> 16) : 0u;
      v = wave_scan_add(v);
      if (lb_ == 63) __hip_atomic_fetch_add(&misc[M_ALL], v, RLX_WG);
      arrive(&misc[M_CP], lb_);
      WAIT_RECS(R);
      TS(5);
      rank(tb, 0, take);
    } else {
      const uint32_t cm = wave_max_u32(max(max(absbits(L.c01[0]), absbits(L.c01[1])), max(absbits(L.c23[0]), absbits(L.c23[1]))));
      await_lds(&misc[M_CP], 12u);
      gu64* gt = (gu64*)(ws + WS_TBG) + 2 * lb_;
      uint32_t mb, mv_, me; float sq;
      for (unsigned spins = 0;; spins++) {
        bool ok = true; mb = mv_ = me = 0; sq = 0.f;
#pragma unroll
        for (int i = 0; i < 4; i++) {
          const u64 v0 = __hip_atomic_load(gt + 128 * i, RLX_AGENT), v1 = __hip_atomic_load(gt + 128 * i + 1, RLX_AGENT);
          ok &= (uint32_t)(v0 >> 32) == tag32 && (uint32_t)(v1 >> 32) == tag32;
          mb = max(mb, (uint32_t)v0 << 16); mv_ = max(mv_, (uint32_t)v0 & 0xffff0000u);
          me = max(me, (uint32_t)v1 << 16); sq += __uint_as_float((uint32_t)v1 & 0xffff0000u);
        }
        if (__all(ok)) break;
        if (spins > SPIN_CAP) break;
        __builtin_amdgcn_s_sleep(16);
      }
      mb = wave_max_u32(mb); mv_ = wave_max_u32(mv_); me = wave_max_u32(me); sq = wave_sum_f32(sq);
      await_lds(&misc[M_CE], 7u);
      const float mv = __uint_as_float(mv_);
      const float yb = (__uint_as_float(misc[M_XAM]) + __uint_as_float(mb) + __uint_as_float(me) * __uint_as_float(cm)) * 1.01f;
      const float all = (float)(misc[M_ALL] + 4u);
      const int p1 = (int)(__float_as_uint(4.1f * mv) >> 23) - 127 + 2, p2 = (int)(__float_as_uint(all * mv * 1.01f) >> 23) - 127 + 1 - 8;
      const int p = max(max(p1, p2), -80);
      const float dl = __uint_as_float((uint32_t)(p - 23 + 127) << 23);
      const bool fine = dl * 4096.f <= __builtin_amdgcn_sqrtf(sq * __builtin_amdgcn_rcpf(64.f * (float)N)) * 0.99f || mv == 0.f;
      if (lb_ == 63) {
        misc[M_FAST] = (yb <= 28.f) && (mv <= 1.0e12f) && fine;
        misc[M_EXP] = (uint32_t)(p + 127);
      }
  }
    group_barrier();
    if (w == 0 && tb == 0) __hip_atomic_store((gu64*)(ws + WS_NONCE), (((u64)xn[1] << 32) | xn[0]) + 1ull, RLX_AGENT);
    if (__builtin_amdgcn_readfirstlane(misc[M_TOT]) > (uint32_t)TAKE_MAX) {
      group_barrier();
      if (tb < 256) *(uint32_t*)(smem + L_CNT + tb * 4) = 0;
      group_barrier();
      take = 16;
      if (wv < 12) rank(tb, 0, take);
      group_barrier();
    }
    if (wv == 12) scan(lb_);
    else {
      for (int i = tb; i < 197 * AGG_ROW / 16; i += 768) *(u32x4*)(smem + L_AGG + i * 16) = u32x4{0, 0, 0, 0};
      if (__builtin_amdgcn_readfirstlane(misc[M_FAST])) {
        for (int i = tb; i < NPB * 8; i += 768) {
          f32x4 v = *(const f32x4*)(smem + L_EA + i * 16);
          v[0] = __builtin_amdgcn_exp2f(v[0]); v[1] = __builtin_amdgcn_exp2f(v[1]); v[2] = __builtin_amdgcn_exp2f(v[2]); v[3] = __builtin_amdgcn_exp2f(v[3]);
          *(f32x4*)(smem + L_EA + i * 16) = v;
        }
      }
    }
    group_barrier();
    TS(6);
    scatter(tb);
    if (wv == 12) {
      gu64* gt = (gu64*)(ws + WS_TRG) + lb_;
      for (unsigned spins = 0; spins < SPIN_CAP; spins++) {
        bool ok = true;
#pragma unroll
        for (int i = 0; i < 4; i++) ok &= (uint32_t)(__hip_atomic_load(gt + 64 * i, RLX_AGENT) >> 32) == tag32;
        if (__all(ok)) break;
        __builtin_amdgcn_s_sleep(16);
      }
    }
  }
  LDS_BARRIER();
  fast = __builtin_amdgcn_readfirstlane(misc[M_FAST]);
  take = __builtin_amdgcn_readfirstlane(misc[M_TOT]) > (uint32_t)TAKE_MAX ? 16 : 64;
  {
    const uint32_t pexp = __builtin_amdgcn_readfirstlane(misc[M_EXP]);
    delta = __uint_as_float((pexp - 23u) << 23);
    const float mg = __uint_as_float((pexp << 23) | 0x400000u);
    L.magic = f32x2{mg, mg};
  }
  int again = __builtin_amdgcn_readfirstlane(misc[M_MORE]);
  int nstep = __builtin_amdgcn_readfirstlane(misc[M_NSTEP]);
  TS(7);
  {
    const int n_ = lane_consts(tb, nstep);
    if (fast) edge_loop<0>(L, n_, tsrd);
    else      edge_loop<2>(L, n_, tsrd);
  }
  while (again) {
    done += take;
    const int t2 = thread_id();
    if (wv < 12) { poll_segments(t2 >> 3); load_recs(R, sg, ws, t2 >> 3, t2 & 7, done); WAIT_RECS(R); }
    LDS_BARRIER();
    if (t2 < 256) *(uint32_t*)(smem + L_CNT + t2 * 4) = 0;
    if (t2 == 0) { misc[M_MORE] = 0; misc[M_TOT] = 0; }
    LDS_BARRIER();
    if (wv < 12) post_total(done, t2 & 7, t2 & 63);
    LDS_BARRIER();
    take = __builtin_amdgcn_readfirstlane(misc[M_TOT]) <= (uint32_t)TAKE_MAX ? 64 : 16;
    if (wv < 12) rank(t2, done, take);
    LDS_BARRIER();
    if (wv == 12) scan(t2 & 63);
    LDS_BARRIER();
    scatter(t2);
    again = __builtin_amdgcn_readfirstlane(misc[M_MORE]);
    LDS_BARRIER();
    nstep = __builtin_amdgcn_readfirstlane(misc[M_NSTEP]);
    const int n_ = lane_consts(t2, nstep);
    if (fast) edge_loop<0>(L, n_, tsrd);
    else      edge_loop<2>(L, n_, tsrd);
  }
  LDS_BARRIER();

  const int te = thread_id();
  if (te < NPB * 4) {
    const int nl = te >> 2, u = te & 3, node = b * NPB + nl;
    if (node < N) {
      float y[8];
      const uint32_t bias = *(const uint32_t*)(smem + L_NQ + nl * 4) * __float_as_uint(L.magic[0]);
      const f32x4 u0 = *(const f32x4*)(smem + L_XU + nl * 128 + u * 32), u1 = *(const f32x4*)(smem + L_XU + nl * 128 + u * 32 + 16);
#pragma unroll
      for (int i = 0; i < 4; i++) {
        const u32x2 v2 = *(const u32x2*)(smem + L_AGG + nl * AGG_ROW + (8 * i + 2 * u) * 4);
#pragma unroll
        for (int j = 0; j < 2; j++) {
          const float agg = fast ? (float)(int)(v2[j] - bias) * delta : __uint_as_float(v2[j]);
          const float v = (j ? u1[i] : u0[i]) + agg;
          y[4 * j + i] = v * __builtin_amdgcn_rcpf(1.f + __builtin_amdgcn_exp2f(v * NLOG2E));
        }
      }
      const u32x4 o = {cvtpk(y[0], y[1]), cvtpk(y[2], y[3]), cvtpk(y[4], y[5]), cvtpk(y[6], y[7])};
      *(u32x4*)(out + (size_t)node * 64 + 32 * half + 8 * u) = o;
    }
  }
  TS_OUT(256 + blockIdx.x);
}

}

extern "C" void kernel_launch(void* const* d_in, const int* in_sizes, int n_in,
                              void* d_out, int out_size, void* d_ws, size_t ws_size,
                              hipStream_t stream) {
  const uint16_t* x  = (const uint16_t*)d_in[0];
  const uint16_t* ea = (const uint16_t*)d_in[1];
  const int*      src = (const int*)d_in[2];
  const int*      dst = (const int*)d_in[3];
  const uint16_t* uw = (const uint16_t*)d_in[4];
  const uint16_t* vw = (const uint16_t*)d_in[5];
  const uint16_t* gw = (const uint16_t*)d_in[6];
  const int N = in_sizes[0] / 64, E = in_sizes[2];
  char* ws = (char*)d_ws;
  (void)hipFuncSetAttribute((const void*)k_mpnn, hipFuncAttributeMaxDynamicSharedMemorySize, L_TOTAL);
  k_mpnn<<<NCH, 1024, L_TOTAL, stream>>>(src, dst, ea, x, uw, vw, gw, ws, (uint16_t*)d_out, N, E);
}
```

```cpp
#include <hip/hip_runtime.h>
#include <hip/hip_bf16.h>
#include <stdint.h>

#pragma clang diagnostic ignored "-Wint-to-pointer-cast"
#pragma clang diagnostic ignored "-Wpointer-to-int-cast"
namespace {

typedef float    f32x2  __attribute__((ext_vector_type(2)));
typedef float    f32x4  __attribute__((ext_vector_type(4)));
typedef float    f32x16 __attribute__((ext_vector_type(16)));
typedef short    bf16x8 __attribute__((ext_vector_type(8)));
typedef short    s16x4  __attribute__((ext_vector_type(4)));
typedef uint32_t u32x2  __attribute__((ext_vector_type(2)));
typedef uint32_t u32x4  __attribute__((ext_vector_type(4)));
typedef unsigned long long u64;
typedef __attribute__((address_space(1))) unsigned long long gu64;
#define LDSP(T) __attribute__((address_space(3))) T*
#define DI __device__ __forceinline__
#define RLX_AGENT __ATOMIC_RELAXED, __HIP_MEMORY_SCOPE_AGENT
#define RLX_WG    __ATOMIC_RELAXED, __HIP_MEMORY_SCOPE_WORKGROUP

constexpr int   NCH = 256;
constexpr int   NB  = 128;
constexpr int   NPB = 196;
constexpr int   CH  = 3128;
constexpr int   CHB = 880;
constexpr int   NMAX = NB * NPB;
constexpr int   NTILE = NMAX / 32;
constexpr float NLOG2E = -1.4426950408889634f;
constexpr int   AUX_SC1 = 16;
constexpr unsigned SPIN_CAP = 1u << 18;
static_assert(4 * CHB >= CH + 3 * NB && (CHB * 20) % 16 == 0, "blocks");

constexpr size_t WS_NONCE = 0;
constexpr size_t WS_SEG = 128;
constexpr size_t WS_TBG = WS_SEG + (size_t)NCH * NB * 8;
constexpr size_t WS_EMG = WS_TBG + (size_t)NCH * 16;
constexpr size_t WS_TRG = WS_EMG + (size_t)NCH * 8;
constexpr size_t WS_REC = WS_TRG + (size_t)NCH * 8;
constexpr size_t WS_TAB = (WS_REC + (size_t)NCH * CHB * 20 + 1024 + 127) & ~(size_t)127;
constexpr size_t WS_DBG = WS_TAB + (size_t)2 * NMAX * 128;
static_assert(WS_REC % 128 == 0 && WS_TAB % 128 == 0, "ws align");

constexpr int AGG_ROW  = 160;
constexpr int SORT_CAP = 8128;
constexpr int TAKE_MAX = SORT_CAP - 3 * NPB - 48;
constexpr int L_CNT    = 0;
constexpr int L_OFF    = L_CNT + 1024;
constexpr int L_NQ     = L_OFF + 1024;
constexpr int L_MISC   = L_NQ + 1024;
constexpr int L_QN     = L_MISC + 512;
constexpr int L_EA     = L_QN + SORT_CAP;
constexpr int L_XU     = L_EA + 197 * 128;
constexpr int L_AGG    = (L_XU + 196 * 128 + 127) & ~127;
constexpr int L_SORT   = (L_AGG + 197 * AGG_ROW + 127) & ~127;
constexpr int L_TOTAL  = L_SORT + SORT_CAP * 8;
static_assert(L_TOTAL <= 163840, "LDS");
static_assert((L_QN & 15) == 0 && (L_EA & 15) == 0 && (L_XU & 15) == 0 && TAKE_MAX >= 4096, "align");
constexpr int A_HIST = L_SORT;
constexpr int A_BASE = A_HIST + 512;
constexpr int A_ST   = A_BASE + 512;
constexpr int A_W    = (A_ST + CHB * 20 + 127) & ~127;
constexpr int A_IMG  = A_W + 16384;
constexpr int P_W    = A_IMG + 16384;
constexpr int P_X    = L_AGG;
static_assert(P_W + 8192 <= L_TOTAL && P_X + 224 * 128 <= L_SORT, "phase A images");
constexpr uint32_t QN_DUMMY = (196u * 128u) | ((196u * AGG_ROW) << 16);
constexpr uint32_t OFF_DUMMY = 0x7fffff00u;
constexpr int M_MORE = 0, M_NSTEP = 1, M_T4 = 2;
constexpr int M_CW = 3, M_CB = 4, M_CT = 5, M_PUB = 6;
constexpr int M_CL = 7, M_CE = 8, M_CP = 9;
constexpr int M_TOT = 10, M_ALL = 11, M_XAM = 12;
constexpr int M_FAST = 13, M_EXP = 14, M_GB = 15, M_TAG0 = 24, M_TAG1 = 25, M_TGF = 26;
constexpr int M_MX = 16;

DI float    bf2f(uint32_t v16) { return __uint_as_float(v16 << 16); }
DI uint32_t cvtpk(float lo, float hi) {
  typedef __bf16 bf16x2 __attribute__((ext_vector_type(2)));
  return __builtin_bit_cast(uint32_t, __builtin_convertvector(f32x2{lo, hi}, bf16x2));
}
DI f32x16   mfma(bf16x8 a, bf16x8 b, f32x16 c) { return __builtin_amdgcn_mfma_f32_32x32x16_bf16(a, b, c, 0, 0, 0); }
DI int      crow(int r, int h) { return (r & 3) + 8 * (r >> 2) + 4 * h; }
DI uint32_t absbits(float v) { return __float_as_uint(v) & 0x7fffffffu; }

template <int CTRL, int RM> DI uint32_t dpp0(uint32_t v) { return (uint32_t)__builtin_amdgcn_update_dpp(0, (int)v, CTRL, RM, 0xf, false); }
DI uint32_t wave_scan_add(uint32_t v) {
  v += dpp0<0x111, 0xf>(v); v += dpp0<0x112, 0xf>(v); v += dpp0<0x114, 0xf>(v); v += dpp0<0x118, 0xf>(v);
  v += dpp0<0x142, 0xa>(v); v += dpp0<0x143, 0xc>(v);
  return v;
}
DI uint32_t wave_max_u32(uint32_t v) {
  v = max(v, dpp0<0x111, 0xf>(v)); v = max(v, dpp0<0x112, 0xf>(v)); v = max(v, dpp0<0x114, 0xf>(v)); v = max(v, dpp0<0x118, 0xf>(v));
  v = max(v, dpp0<0x142, 0xa>(v)); v = max(v, dpp0<0x143, 0xc>(v));
  return v;
}
DI float wave_sum_f32(float s) {
  s += __uint_as_float(dpp0<0x111, 0xf>(__float_as_uint(s))); s += __uint_as_float(dpp0<0x112, 0xf>(__float_as_uint(s)));
  s += __uint_as_float(dpp0<0x114, 0xf>(__float_as_uint(s))); s += __uint_as_float(dpp0<0x118, 0xf>(__float_as_uint(s)));
  s += __uint_as_float(dpp0<0x142, 0xa>(__float_as_uint(s))); s += __uint_as_float(dpp0<0x143, 0xc>(__float_as_uint(s)));
  return s;
}
#define LDS_BARRIER() asm volatile("s_waitcnt lgkmcnt(0)\n\ts_barrier" ::: "memory")
#define WAIT_VM(N) asm volatile("s_waitcnt vmcnt(" #N ")" ::: "memory")

DI uint32_t arrive(uint32_t* word, int lane) {
  asm volatile("s_waitcnt lgkmcnt(0)" ::: "memory");
  uint32_t v = 0;
  if (lane == 0) v = __hip_atomic_fetch_add(word, 1u, RLX_WG);
  return __builtin_amdgcn_readfirstlane(v);
}
DI void await_lds(uint32_t* word, uint32_t want) {
  for (unsigned spins = 0; spins < SPIN_CAP; spins++) {
    if (__builtin_amdgcn_readfirstlane(__hip_atomic_load(word, RLX_WG)) >= want) break;
    __builtin_amdgcn_s_sleep(1);
  }
  asm volatile("" ::: "memory");
}

DI void dma16(uint32_t ldsaddr, uint32_t voff, __amdgpu_buffer_rsrc_t srd) {
  asm volatile("s_mov_b32 m0, %0\n\ts_nop 0\n\tbuffer_load_dwordx4 %1, %2, 0 offen lds" :: "s"(ldsaddr), "v"(voff), "s"(srd) : "memory");
}
DI bf16x8 tr_frag(uint32_t addr, int four_rows) {
  const s16x4 lo = __builtin_amdgcn_ds_read_tr16_b64_v4i16((LDSP(s16x4))addr);
  const s16x4 hi = __builtin_amdgcn_ds_read_tr16_b64_v4i16((LDSP(s16x4))(addr + four_rows));
  return (bf16x8)__builtin_shufflevector(lo, hi, 0, 1, 2, 3, 4, 5, 6, 7);
}

#define TS(i) do { } while (0)
#define TS_DECL do { } while (0)
#define TS_OUT(slot) do { } while (0)
#define TS_PUB(i) do { } while (0)

DI uint32_t shape(float v, uint32_t low) {
  const uint32_t b = __float_as_uint(v);
  const uint32_t t = min(__builtin_elementwise_sub_sat((b & 0x7fffffffu) + 0x8000u, low), 0x7fffffffu);
  return (b & 0x80000000u) | (t & 0xffff0000u) | low;
}
DI void table_half(u32x4* o, const bf16x8* xf, uint32_t troff, int c, float& mb, float& mv, float& s2) {
  f32x16 zb, hv;
#pragma unroll
  for (int i = 0; i < 16; i++) { zb[i] = 0.f; hv[i] = 0.f; }
#pragma unroll
  for (int s = 0; s < 4; s++) {
    zb = mfma(tr_frag(troff + s * 2048 + c * 64, 512), xf[s], zb);
    hv = mfma(tr_frag(troff + 8192 + s * 2048 + c * 64, 512), xf[s], hv);
  }
#pragma unroll
  for (int q = 0; q < 4; q++) {
#pragma unroll
    for (int p = 0; p < 2; p++) {
      const int i = 4 * q + 2 * p;
      const float b0 = zb[i] * NLOG2E, b1 = zb[i + 1] * NLOG2E, v0 = hv[i], v1 = hv[i + 1];
      mb = fmaxf(mb, fmaxf(fabsf(b0), fabsf(b1))); mv = fmaxf(mv, fmaxf(fabsf(v0), fabsf(v1)));
      s2 = fmaf(v0, v0, s2); s2 = fmaf(v1, v1, s2);
      const uint32_t d0 = shape(b0, cvtpk(v0, 0.f));
      const uint32_t w1 = shape(v1, d0 >> 16);
      o[q][2 * p] = d0; o[q][2 * p + 1] = shape(b1, w1 >> 16);
    }
  }
}
DI void table_image(const u32x4* o, uint32_t img, int lane) {
  const int n31 = lane & 31, h = lane >> 5;
#pragma unroll
  for (int q = 0; q < 4; q++) *(LDSP(u32x4))(img + n31 * 128 + (((2 * q + h) ^ (n31 & 7)) * 16)) = o[q];
  asm volatile("s_waitcnt lgkmcnt(0)" ::: "memory");
}
DI void table_rows(u32x4* r, uint32_t img, int lane) {
#pragma unroll
  for (int i = 0; i < 4; i++) { const int row = 8 * i + (lane >> 3); r[i] = *(LDSP(const u32x4))(img + row * 128 + (((lane & 7) ^ (row & 7)) * 16)); }
  asm volatile("s_waitcnt lgkmcnt(0)" : "+v"(r[0]), "+v"(r[1]), "+v"(r[2]), "+v"(r[3]) :: "memory");
}
DI void table_store(const u32x4* r, int tile, int c, __amdgpu_buffer_rsrc_t tsrd, int lane) {
#pragma unroll
  for (int i = 0; i < 4; i++)
    __builtin_amdgcn_raw_buffer_store_b128(r[i], tsrd, (c * NMAX + tile * 32 + 8 * i + (lane >> 3)) * 128 + (lane & 7) * 16, 0, AUX_SC1);
}

DI f32x2 exp2x2(f32x2 v) { f32x2 r = {__builtin_amdgcn_exp2f(v[0]), __builtin_amdgcn_exp2f(v[1])}; return r; }
DI f32x2 rcpx2(f32x2 v) { f32x2 r = {__builtin_amdgcn_rcpf(v[0]), __builtin_amdgcn_rcpf(v[1])}; return r; }
DI f32x2 fma2(f32x2 a, f32x2 b, f32x2 c) { return __builtin_elementwise_fma(a, b, c); }
template <int MUL> DI uint32_t madlo_i(uint32_t v, uint32_t add) {
  uint32_t r; asm("v_mad_u32_u16 %0, %1, %3, %2" : "=v"(r) : "v"(v), "v"(add), "i"(MUL)); return r;
}
template <int MUL> DI uint32_t madhi_i(uint32_t v, uint32_t add) {
  uint32_t r; asm("v_mad_u32_u16 %0, %1, %3, %2 op_sel:[1,0,0,0]" : "=v"(r) : "v"(v), "v"(add), "i"(MUL)); return r;
}
template <int K> DI uint32_t byte_x4(uint32_t v) {
  uint32_t r;
  if (K == 0) asm("v_lshlrev_b32_sdwa %0, 2, %1 dst_sel:DWORD dst_unused:UNUSED_PAD src0_sel:DWORD src1_sel:BYTE_0" : "=v"(r) : "v"(v));
  if (K == 1) asm("v_lshlrev_b32_sdwa %0, 2, %1 dst_sel:DWORD dst_unused:UNUSED_PAD src0_sel:DWORD src1_sel:BYTE_1" : "=v"(r) : "v"(v));
  if (K == 2) asm("v_lshlrev_b32_sdwa %0, 2, %1 dst_sel:DWORD dst_unused:UNUSED_PAD src0_sel:DWORD src1_sel:BYTE_2" : "=v"(r) : "v"(v));
  if (K == 3) asm("v_lshlrev_b32_sdwa %0, 2, %1 dst_sel:DWORD dst_unused:UNUSED_PAD src0_sel:DWORD src1_sel:BYTE_3" : "=v"(r) : "v"(v));
  return r;
}
DI uint32_t byte_x4(uint32_t v, int k) { return k == 0 ? byte_x4<0>(v) : (k == 1 ? byte_x4<1>(v) : (k == 2 ? byte_x4<2>(v) : byte_x4<3>(v))); }
DI uint32_t lo_x128(uint32_t v) { uint32_t r; asm("v_mad_u32_u16 %0, %1, %2, 0" : "=v"(r) : "v"(v), "s"(128u)); return r; }
DI void lds_add_u32(uint32_t addr, float v) { __hip_atomic_fetch_add((LDSP(uint32_t))addr, __float_as_uint(v), RLX_WG); }
DI void lds_add_f32(uint32_t addr, float v) { __hip_atomic_fetch_add((LDSP(float))addr, v, RLX_WG); }

struct Lane {
  uint32_t rec;
  uint32_t qn;
  uint32_t c16;
  uint32_t ea, agg;
  f32x2 c01, c23;
  f32x2 magic;
};
struct Quad {
  u32x4 d[4];
  u32x4 r0, r1;
  uint32_t q;
};
template <int S> DI void fetch(Quad& Q, const Lane& L, __amdgpu_buffer_rsrc_t srd) {
  Q.r0 = *(LDSP(const u32x4))(L.rec + S * 32);
  Q.r1 = *(LDSP(const u32x4))(L.rec + S * 32 + 16);
  Q.q  = *(LDSP(const uint32_t))(L.qn + S * 4);
  Q.d[0] = __builtin_amdgcn_raw_buffer_load_b128(srd, Q.r0[0] + L.c16, 0, AUX_SC1);
  Q.d[1] = __builtin_amdgcn_raw_buffer_load_b128(srd, Q.r0[2] + L.c16, 0, AUX_SC1);
  Q.d[2] = __builtin_amdgcn_raw_buffer_load_b128(srd, Q.r1[0] + L.c16, 0, AUX_SC1);
  Q.d[3] = __builtin_amdgcn_raw_buffer_load_b128(srd, Q.r1[2] + L.c16, 0, AUX_SC1);
  asm volatile("" ::: "memory");
}
DI f32x2 fma_e(u32x2 rec, f32x2 c, u32x2 xb) {
  f32x2 r; asm("v_pk_fma_f32 %0, %1, %2, %3 op_sel:[1,0,0] op_sel_hi:[1,1,1]" : "=v"(r) : "v"(rec), "v"(c), "v"(xb)); return r;
}
DI f32x2 vpair(u32x2 d) {
  f32x2 r; asm("v_lshlrev_b64 %0, 16, %1" : "=v"(r) : "v"(d)); return r;
}
template <int MODE> DI void gate(const Quad& Q, const Lane& L) {
  const f32x4 ea = *(LDSP(const f32x4))(madlo_i<1>(Q.q, L.ea));
  const uint32_t ga = madhi_i<1>(Q.q, L.agg);
  const u32x2 rec[4] = {u32x2{Q.r0[0], Q.r0[1]}, u32x2{Q.r0[2], Q.r0[3]}, u32x2{Q.r1[0], Q.r1[1]}, u32x2{Q.r1[2], Q.r1[3]}};
  const f32x2 one = {1.f, 1.f};
#pragma unroll
  for (int p = 0; p < 2; p++) {
    const f32x2 c = p ? L.c23 : L.c01, eap = {ea[2 * p], ea[2 * p + 1]};
    f32x2 a[4], hv[4];
#pragma unroll
    for (int k = 0; k < 4; k++) {
      const u32x2 dd = {Q.d[k][2 * p], Q.d[k][2 * p + 1]};
      hv[k] = vpair(dd);
      const f32x2 y = fma_e(rec[k], c, dd);
      a[k] = MODE == 2 ? one + exp2x2(y + eap) : fma2(eap, exp2x2(y), one);
    }
    if (MODE == 0) {
      const f32x2 d01 = a[0] * a[1], n01 = fma2(hv[1], a[0], hv[0] * a[1]);
      const f32x2 d23 = a[2] * a[3], n23 = fma2(hv[3], a[2], hv[2] * a[3]);
      const f32x2 m = fma2(fma2(n23, d01, n01 * d23), rcpx2(d01 * d23), L.magic);
      lds_add_u32(ga + 64 * p, m[0]); lds_add_u32(ga + 64 * p + 32, m[1]);
    } else {
      const f32x2 m = (hv[0] * rcpx2(a[0]) + hv[1] * rcpx2(a[1])) + (hv[2] * rcpx2(a[2]) + hv[3] * rcpx2(a[3]));
      lds_add_f32(ga + 64 * p, m[0]); lds_add_f32(ga + 64 * p + 32, m[1]);
    }
  }
}
template <int MODE> DI void edge_loop(Lane& L, int n, __amdgpu_buffer_rsrc_t srd) {
  if (n <= 0) return;
  Quad A, B;
  fetch<0>(A, L, srd);
  while (true) {
    fetch<1>(B, L, srd);
    gate<MODE>(A, L);
    if (--n == 0) break;
    fetch<2>(A, L, srd);
    gate<MODE>(B, L);
    if (--n == 0) break;
    L.rec += 64; L.qn += 8;
  }
}

struct Recs { u32x4 rc[6]; uint32_t lc[6]; };
DI void load_recs(Recs& R, const uint32_t* sg, const char* ws, int g, int j, int done) {
  const __amdgpu_buffer_rsrc_t rsrd = __builtin_amdgcn_make_buffer_rsrc((void*)(ws + WS_REC), (short)0, NCH * CHB * 20, 0x00020000);
#pragma unroll
  for (int p = 0; p < 2; p++) {
#pragma unroll
    for (int r = 0; r < 3; r++) {
      uint32_t off = (uint32_t)((g + 96 * r) * (CHB * 20) + ((int)(sg[r] & 0xffff) + (done >> 2) + 8 * p + j) * 20);
      if ((int)(sg[r] >> 16) - done <= 32 * p + 4 * j) off = 0x40000000u;
      asm volatile("buffer_load_dwordx4 %0, %1, %2, 0 offen sc1" : "=&v"(R.rc[2 * r + p]) : "v"(off), "s"(rsrd) : "memory");
      asm volatile("buffer_load_dword %0, %1, %2, 0 offen offset:16 sc1" : "=&v"(R.lc[2 * r + p]) : "v"(off), "s"(rsrd) : "memory");
    }
  }
}
#define WAIT_RECS(R) asm volatile("s_waitcnt vmcnt(0)" : "+v"(R.rc[0]), "+v"(R.rc[1]), "+v"(R.rc[2]), "+v"(R.rc[3]), "+v"(R.rc[4]), "+v"(R.rc[5]), \
                                  "+v"(R.lc[0]), "+v"(R.lc[1]), "+v"(R.lc[2]), "+v"(R.lc[3]), "+v"(R.lc[4]), "+v"(R.lc[5]) :: "memory")

__global__ __launch_bounds__(1024) void k_mpnn(const int* __restrict__ src, const int* __restrict__ dst,
                                               const uint16_t* __restrict__ ea, const uint16_t* __restrict__ x,
                                               const uint16_t* __restrict__ uw, const uint16_t* __restrict__ vw,
                                               const uint16_t* __restrict__ gw, char* __restrict__ ws,
                                               uint16_t* __restrict__ out, int N, int E) {
  extern __shared__ __attribute__((aligned(128))) char smem[];
  const uint32_t lb = (uint32_t)(uintptr_t)(LDSP(char))smem;
  uint32_t* misc = (uint32_t*)(smem + L_MISC);
  const int t = threadIdx.x, lane = t & 63;
  const int w = blockIdx.x;
  const int half = (blockIdx.x & 7) >> 2, b = (blockIdx.x & 3) * 32 + (blockIdx.x >> 3);
  const int wv = __builtin_amdgcn_readfirstlane(t >> 6);
  const int r31 = lane & 31, h = lane >> 5;
  TS_DECL; TS(0);

  uint32_t gb = 0;
  auto group_barrier = [&](int l) { gb += 13; arrive(&misc[M_GB], l); await_lds(&misc[M_GB], gb); };
  const __amdgpu_buffer_rsrc_t xsrd = __builtin_amdgcn_make_buffer_rsrc((void*)x, (short)0, N * 128, 0x00020000);
  const uint32_t cA = (uint32_t)(((lane & 7) ^ (lane >> 4)) * 16);
  u32x2 xn, cw;
#define LOAD_XN_CW(a, b) do { \
    asm volatile("global_load_dwordx2 %0, %1, %2 sc1" : "=&v"(a) : "v"(0), "s"(ws + WS_NONCE) : "memory"); \
    asm volatile("global_load_dwordx2 %0, %1, %2" : "=&v"(b) : "v"((128 * 64 + 32 * half + 4 * (t & 7)) * 2), "s"(gw) : "memory"); } while (0)
  Lane L;
  if (wv >= 13) {
    u32x2 xn1, cw1;
    LOAD_XN_CW(xn1, cw1);
    const uint32_t img = lb + A_IMG + (wv & 3) * 4096;
    auto tile_rows = [&](uint32_t to, int tile) {
#pragma unroll
      for (int i = 0; i < 4; i++) dma16(to + i * 1024, (tile * 32 + 8 * i + (lane >> 3)) * 128 + ((i & 1) ? cA ^ 64u : cA), xsrd);
    };
    const __amdgpu_buffer_rsrc_t bsrd = __builtin_amdgcn_make_buffer_rsrc((void*)(gw + 64 * 64), (short)0, 8192, 0x00020000);
    const __amdgpu_buffer_rsrc_t vsrd = __builtin_amdgcn_make_buffer_rsrc((void*)vw, (short)0, 8192, 0x00020000);
    const int tile = w + 256 * (wv - 13);
    const bool extra = wv == 13 && w + 768 < NTILE;
    tile_rows(img, tile);
    if (extra) tile_rows(lb + A_IMG, w + 768);
#pragma unroll
    for (int i = 0; i < 6; i++) {
      const int p = wv - 13 + 3 * i;
      if (p < 8) dma16(lb + A_W + p * 1024, p * 1024 + lane * 16, bsrd);
      else if (p < 16) dma16(lb + A_W + p * 1024, (p - 8) * 1024 + lane * 16, vsrd);
    }
    LDS_BARRIER();
    asm volatile("s_waitcnt vmcnt(0)" : "+v"(xn1), "+v"(cw1) :: "memory");
    xn = xn1; cw = cw1;
    const uint32_t tag = xn[0] + 1u;
    arrive(&misc[M_CL], lane);
    await_lds(&misc[M_CL], 3u);
    const __amdgpu_buffer_rsrc_t wsrd = __builtin_amdgcn_make_buffer_rsrc((void*)(ws + WS_TAB), (short)0, 2 * NMAX * 128, 0x00020000);
    const uint32_t troff = lb + A_W + (8 * h + ((lane & 15) >> 2)) * 128 + (16 * ((lane >> 4) & 1) + 4 * (lane & 3)) * 2;
    const uint32_t xa = img + r31 * 128 + ((h ^ ((r31 >> 1) & 7)) * 16), xe = xa - img + lb + A_IMG;
    float mb = 0.f, mv = 0.f, s2 = 0.f;
    u32x4 ra[4], rb[4], rc[4], rd[4];
    auto tile_halves = [&](uint32_t x0, u32x4* r0, u32x4* r1) {
      bf16x8 xf[4]; u32x4 o[4];
#pragma unroll
      for (int s = 0; s < 4; s++) xf[s] = *(LDSP(const bf16x8))(x0 ^ (32u * s));
      table_half(o, xf, troff, 0, mb, mv, s2);
      table_image(o, img, lane);
      table_rows(r0, img, lane);
      table_half(o, xf, troff, 1, mb, mv, s2);
      table_image(o, img, lane);
      table_rows(r1, img, lane);
    };
    tile_halves(xa, ra, rb);
    if (extra) tile_halves(xe, rc, rd);
    const uint32_t m0 = wave_max_u32(__float_as_uint(mb)), m1 = wave_max_u32(__float_as_uint(mv));
    s2 = wave_sum_f32(s2);
    if (lane == 63) {
      __hip_atomic_fetch_max(&misc[M_MX], m0, RLX_WG); __hip_atomic_fetch_max(&misc[M_MX + 1], m1, RLX_WG);
      misc[M_MX + 4 + (wv & 3)] = __float_as_uint(s2);
    }
    arrive(&misc[M_CB], lane);
    await_lds(&misc[M_PUB], 1u);
    table_store(ra, tile, 0, wsrd, lane);
    table_store(rb, tile, 1, wsrd, lane);
    if (extra) { table_store(rc, w + 768, 0, wsrd, lane); table_store(rd, w + 768, 1, wsrd, lane); }
    WAIT_VM(0);
    if (arrive(&misc[M_CT], lane) == 2 && lane == 0) { __hip_atomic_store((gu64*)(ws + WS_TRG) + w, (u64)tag << 32, RLX_AGENT); TS_PUB(1); }
  } else {
    const int e0 = w * CH;
    const int n  = min(CH, E - e0);
    const int cnt = min(max(n - 4 * t, 0), 4);
    u32x4 d4, s4; u32x2 e2, xn2, cw2;
    LOAD_XN_CW(xn2, cw2);
    {
      const __amdgpu_buffer_rsrc_t dsrd = __builtin_amdgcn_make_buffer_rsrc((void*)dst, (short)0, E * 4, 0x00020000);
      const __amdgpu_buffer_rsrc_t ssrd = __builtin_amdgcn_make_buffer_rsrc((void*)src, (short)0, E * 4, 0x00020000);
      const __amdgpu_buffer_rsrc_t esrd = __builtin_amdgcn_make_buffer_rsrc((void*)ea, (short)0, (E * 2 + 3) & ~3, 0x00020000);
      const uint32_t eo = cnt > 0 ? (uint32_t)(e0 + 4 * t) : 0x20000000u;
      asm volatile("buffer_load_dwordx4 %0, %1, %2, 0 offen" : "=&v"(d4) : "v"(eo * 4), "s"(dsrd) : "memory");
      asm volatile("buffer_load_dwordx4 %0, %1, %2, 0 offen" : "=&v"(s4) : "v"(eo * 4), "s"(ssrd) : "memory");
      asm volatile("buffer_load_dwordx2 %0, %1, %2, 0 offen" : "=&v"(e2) : "v"(eo * 2), "s"(esrd) : "memory");
    }
    uint32_t* hist = (uint32_t*)(smem + A_HIST);
    uint32_t* base = (uint32_t*)(smem + A_BASE);
    if (t < 128) { hist[t] = 0; misc[t] = 0; }
    if (t >= 128 && t < 640) *(uint32_t*)(smem + (t < 384 ? L_CNT + (t - 128) * 4 : L_NQ + (t - 384) * 4)) = 0;
    if (t >= 640 && t < 672) *(float*)(smem + L_EA + 196 * 128 + (t - 640) * 4) = 0.f;
    LDS_BARRIER();
    asm volatile("s_waitcnt vmcnt(0)" : "+v"(xn2), "+v"(cw2), "+v"(d4), "+v"(s4), "+v"(e2) :: "memory");
    xn = xn2; cw = cw2;
    const u64 tag40 = ((((u64)xn[1] << 32) | xn[0]) + 1ull) & 0xffffffffffull;
    const uint32_t ev[4] = {e2[0] & 0xffffu, e2[0] >> 16, e2[1] & 0xffffu, e2[1] >> 16};
    uint32_t bk[4], rk0[4];
#pragma unroll
    for (int k = 0; k < 4; k++) {
      bk[k] = (d4[k] * 21400u) >> 22;
      rk0[k] = 0;
      if (k < cnt) rk0[k] = __hip_atomic_fetch_add(&hist[bk[k]], 1u, RLX_WG);
    }
    TS(1);
    group_barrier(lane);
    if (wv == 0) {
      const u32x2 c = *(const u32x2*)(hist + 2 * t);
      const uint32_t p0 = (c[0] + 3) >> 2, tot = p0 + ((c[1] + 3) >> 2);
      const uint32_t ex = wave_scan_add(tot) - tot;
      *(u32x2*)(base + 2 * t) = u32x2{ex, ex + p0};
      if (t == 0) { misc[M_TAG0] = xn[0]; misc[M_TAG1] = xn[1]; }
    } else if (wv < 12) {
      const __amdgpu_buffer_rsrc_t asrd = __builtin_amdgcn_make_buffer_rsrc((void*)gw, (short)0, 64 * 128, 0x00020000);
      const __amdgpu_buffer_rsrc_t usrd = __builtin_amdgcn_make_buffer_rsrc((void*)uw, (short)0, 64 * 128, 0x00020000);
      const uint32_t woff = (lane >> 2) * 128 + half * 64 + (lane & 3) * 16;
#pragma unroll
      for (int i = 0; i < 3; i++) {
        const int p = wv - 1 + 11 * i;
        if (p < 4) dma16(lb + P_W + p * 1024, p * 2048 + woff, asrd);
        else if (p < 8) dma16(lb + P_W + p * 1024, (p - 4) * 2048 + woff, usrd);
        else { const int q = p - 8; dma16(lb + P_X + q * 1024, (b * NPB + 8 * q + (lane >> 3)) * 128 + ((q & 1) ? cA ^ 64u : cA), xsrd); }
      }
    }
    const uint32_t m2 = wave_max_u32(max(max(ev[0] & 0x7fffu, ev[1] & 0x7fffu), max(ev[2] & 0x7fffu, ev[3] & 0x7fffu)) << 16);
    if (lane == 63) __hip_atomic_fetch_max(&misc[M_MX + 2], m2, RLX_WG);
    group_barrier(lane);
#pragma unroll
    for (int k = 0; k < 4; k++) {
      if (k < cnt) {
        const uint32_t p = base[bk[k]] * 4 + rk0[k];
        const uint32_t o = (p >> 2) * 20 + (p & 3);
        *(uint32_t*)(smem + A_ST + o + (p & 3) * 3) = (s4[k] & 0xffffu) | (ev[k] << 16);
        *(uint8_t*)(smem + A_ST + o + 16) = (uint8_t)(d4[k] - bk[k] * NPB);
      }
    }
    group_barrier(lane);
    TS(2);
    const __amdgpu_buffer_rsrc_t rsrd = __builtin_amdgcn_make_buffer_rsrc((void*)(ws + WS_REC + (size_t)w * CHB * 20), (short)0, CHB * 20, 0x00020000);
    const uint32_t nb = base[127] + ((hist[127] + 3) >> 2);
#pragma unroll
    for (int i = 0; i < 2; i++) {
      const int c = t + 832 * i;
      if (c * 16 < (int)nb * 20) __builtin_amdgcn_raw_buffer_store_b128(*(const u32x4*)(smem + A_ST + c * 16), rsrd, c * 16, 0, AUX_SC1);
    }
    WAIT_VM(0);
    if (arrive(&misc[M_CW], lane) == 12) {
      const u32x2 c = *(const u32x2*)(hist + 2 * lane), o = *(const u32x2*)(base + 2 * lane);
      gu64* g = (gu64*)(ws + WS_SEG) + w * NB + 2 * lane;
      __hip_atomic_store(g,     (u64)(o[0] | (c[0] << 12)) | (tag40 << 24), RLX_AGENT);
      __hip_atomic_store(g + 1, (u64)(o[1] | (c[1] << 12)) | (tag40 << 24), RLX_AGENT);
      if (lane == 0) { __hip_atomic_store((gu64*)(ws + WS_EMG) + w, (u64)misc[M_MX + 2] | ((u64)(xn[0] + 1u) << 32), RLX_AGENT); __hip_atomic_store(&misc[M_PUB], 1u, RLX_WG); TS_PUB(0); }
    }
  }
  const uint32_t tag32 = xn[0] + 1u;
  const u64 tag40 = ((((u64)xn[1] << 32) | xn[0]) + 1ull) & 0xffffffffffull;
  if (wv < 12) {
    if (wv >= 5 && wv < 12) {
      await_lds(&misc[M_CW], 13u);
      const int rt = wv - 5, r31_ = lane & 31, h_ = lane >> 5, row = 32 * rt + r31_;
      const uint32_t xa = lb + P_X + row * 128 + ((h_ ^ ((row >> 1) & 7)) * 16);
      const uint32_t troff = lb + P_W + (8 * h_ + ((lane & 15) >> 2)) * 64 + (16 * ((lane >> 4) & 1) + 4 * (lane & 3)) * 2;
      uint32_t xam = 0;
#pragma unroll
      for (int part = 0; part < 2; part++) {
        f32x16 acc;
#pragma unroll
        for (int i = 0; i < 16; i++) acc[i] = 0.f;
#pragma unroll
        for (int s = 0; s < 4; s++) acc = mfma(*(LDSP(const bf16x8))(xa ^ (32u * s)), tr_frag(troff + part * 4096 + s * 1024, 256), acc);
        const uint32_t o = lb + (part ? L_XU : L_EA) + (32 * rt + 4 * h_) * 128 + r31_ * 4;
#pragma unroll
        for (int i = 0; i < 16; i++) {
          const float v = part ? acc[i] : acc[i] * NLOG2E;
          if (rt < 6 || crow(i, 0) + 4 * h_ < 4) { *(LDSP(float))(o + crow(i, 0) * 128) = v; if (!part) xam = max(xam, absbits(v)); }
        }
      }
      xam = wave_max_u32(xam);
      if (lane == 63) __hip_atomic_fetch_max(&misc[M_XAM], xam, RLX_WG);
      arrive(&misc[M_CE], lane);
    }
  }
  L.c01 = f32x2{bf2f(cw[0] & 0xffffu) * NLOG2E, __uint_as_float(cw[0] & 0xffff0000u) * NLOG2E};
  L.c23 = f32x2{bf2f(cw[1] & 0xffffu) * NLOG2E, __uint_as_float(cw[1] & 0xffff0000u) * NLOG2E};
  TS(3);

  auto thread_id = [&]() {
    int v = wv * 64 + (int)__builtin_amdgcn_mbcnt_hi(~0u, __builtin_amdgcn_mbcnt_lo(~0u, 0u));
    asm volatile("" : "+v"(v));
    return v;
  };
  const int tb = thread_id(), lb_ = tb & 63;
  const __amdgpu_buffer_rsrc_t tsrd = __builtin_amdgcn_make_buffer_rsrc((void*)(ws + WS_TAB + (size_t)half * NMAX * 128), (short)0, N * 128, 0x00020000);
  uint32_t sg[3] = {0, 0, 0};
  auto poll_segments = [&](int g) {
    gu64* gs = (gu64*)(ws + WS_SEG) + g * NB + b;
    const bool third = g + 192 < NCH;
    for (unsigned spins = 0;; spins++) {
      const u64 v0 = __hip_atomic_load(gs, RLX_AGENT), v1 = __hip_atomic_load(gs + 96 * NB, RLX_AGENT);
      const u64 v2 = __hip_atomic_load(gs + (third ? 192 * NB : 0), RLX_AGENT);
      const bool ok = (v0 >> 24) == tag40 && (v1 >> 24) == tag40 && (v2 >> 24) == tag40;
      sg[0] = ((uint32_t)v0 & 0xfffu) | ((((uint32_t)v0 >> 12) & 0xfffu) << 16);
      sg[1] = ((uint32_t)v1 & 0xfffu) | ((((uint32_t)v1 >> 12) & 0xfffu) << 16);
      sg[2] = third ? ((uint32_t)v2 & 0xfffu) | ((((uint32_t)v2 >> 12) & 0xfffu) << 16) : 0u;
      if (__all(ok)) break;
      if (spins > SPIN_CAP) { sg[0] = sg[1] = sg[2] = 0; break; }
      __builtin_amdgcn_s_sleep(12);
    }
  };
  Recs R;
  int done = 0;
  uint32_t rk[6][2]; bool part1[3] = {false, false, false};
  auto post_total = [&](int dn, int j, int l) {
    uint32_t v = 0;
    if (j == 0) v = min(max((int)(sg[0] >> 16) - dn, 0), 64) + min(max((int)(sg[1] >> 16) - dn, 0), 64) + min(max((int)(sg[2] >> 16) - dn, 0), 64);
    v = wave_scan_add(v);
    if (l == 63) __hip_atomic_fetch_add(&misc[M_TOT], v, RLX_WG);
  };
  auto rank = [&](int tt, int dn, int take) {
    const int j = tt & 7;
    const uint32_t spare = (196u + (uint32_t)(tt & 63) % 60u) * 0x01010101u;
    bool more = false;
#pragma unroll
    for (int i = 0; i < 6; i++) {
      const int len = (int)(sg[i >> 1] >> 16) - dn;
      more |= len > take;
      const int v = min(max(min(len, take) - 32 * (i & 1) - 4 * j, 0), 4);
      const uint32_t drop = (uint32_t)(0xffffffffull << (8 * v));
      R.lc[i] = (R.lc[i] & ~drop) | (spare & drop);
      if (i & 1) part1[i >> 1] = __any(v > 0);
    }
    uint32_t r[3][4];
#pragma unroll
    for (int st = 0; st < 3; st++)
#pragma unroll
      for (int k = 0; k < 4; k++) r[st][k] = __hip_atomic_fetch_add((LDSP(uint32_t))(L_CNT + byte_x4(R.lc[2 * st], k)), 1u, RLX_WG);
#pragma unroll
    for (int st = 0; st < 3; st++) {
      rk[2 * st][0] = r[st][0] | (r[st][1] << 16); rk[2 * st][1] = r[st][2] | (r[st][3] << 16);
      asm volatile("" : "+v"(rk[2 * st][0]), "+v"(rk[2 * st][1]));
      rk[2 * st + 1][0] = rk[2 * st + 1][1] = 0;
      if (part1[st]) {
        uint32_t q[4];
#pragma unroll
        for (int k = 0; k < 4; k++) q[k] = __hip_atomic_fetch_add((LDSP(uint32_t))(L_CNT + byte_x4(R.lc[2 * st + 1], k)), 1u, RLX_WG);
        rk[2 * st + 1][0] = q[0] | (q[1] << 16); rk[2 * st + 1][1] = q[2] | (q[3] << 16);
      }
    }
    if (__any(more) && (tt & 63) == 0) misc[M_MORE] = 1;
  };
  auto scan = [&](int ln_) {
    u32x4 c = *(const u32x4*)(smem + L_CNT + ln_ * 16);
    if (ln_ >= NPB / 4) c = u32x4{0, 0, 0, 0};
    const u32x4 p = (c + 3) & ~3u;
    const uint32_t s1 = p[0] + p[1], s2 = s1 + p[2], tot = s2 + p[3];
    const uint32_t inc = wave_scan_add(tot), ex = inc - tot;
    *(u32x4*)(smem + L_OFF + ln_ * 16) = ln_ < NPB / 4 ? u32x4{ex, ex + p[0], ex + s1, ex + s2} : u32x4{SORT_CAP, SORT_CAP, SORT_CAP, SORT_CAP};
    const uint32_t T4 = (uint32_t)__builtin_amdgcn_readlane((int)inc, 63);
    const uint32_t T32 = (T4 + 31) & ~31u;
    if ((T4 >> 2) + ln_ < (T32 >> 2) + 2) *(uint32_t*)(smem + L_QN + ((T4 >> 2) + ln_) * 4) = QN_DUMMY;
    if (ln_ == 0) { misc[M_NSTEP] = T32 >> 5; misc[M_T4] = T4; }
  };
  auto scatter = [&](int tt) {
    if (wv < 12) {
#pragma unroll
      for (int i = 0; i < 6; i++) {
        if ((i & 1) == 0 || part1[i >> 1]) {
#pragma unroll
          for (int k = 0; k < 4; k++) {
            const uint32_t pos = min(*(LDSP(const uint32_t))(L_OFF + byte_x4(R.lc[i], k)) + ((rk[i][k >> 1] >> (16 * (k & 1))) & 0xffffu), (uint32_t)(SORT_CAP - 1));
            const uint32_t rec = R.rc[i][k];
            *(LDSP(u32x2))(lb + L_SORT + pos * 8) = u32x2{lo_x128(rec), rec & 0xffff0000u};
          }
        }
      }
    }
    u32x2 dummy = {(uint32_t)tt, 0u};
    asm volatile("" : "+v"(dummy));
    dummy = u32x2{dummy[0] * 0u + OFF_DUMMY, 0u};
    const int u = 831 - tt;
    if (u >= 0 && u < NPB * 4) {
      const uint32_t nd = u >> 2, k = u & 3;
      const uint32_t c = *(const uint32_t*)(smem + L_CNT + nd * 4), o = *(const uint32_t*)(smem + L_OFF + nd * 4);
      const uint32_t val = nd * (128u + (AGG_ROW << 16));
      for (uint32_t q = k; 4 * q < c; q += 4) *(uint32_t*)(smem + L_QN + o + q * 4) = val;
      if (k == 3) *(uint32_t*)(smem + L_NQ + nd * 4) += (c + 3) >> 2;
      if (k < 3 && c + k < ((c + 3) & ~3u)) *(u32x2*)(smem + L_SORT + (o + c + k) * 8) = dummy;
    }
    if (wv == 12) {
      const uint32_t T4 = misc[M_T4], T32 = (T4 + 31) & ~31u, i = T4 + (tt & 63);
      if (i < T32 + 8) *(u32x2*)(smem + L_SORT + i * 8) = dummy;
    }
  };
  auto lane_consts = [&](int tt, int nstep) {
    const int n_ = (nstep + 15 - wv) >> 4, first = 8 * (wv * (nstep >> 4) + min(wv, nstep & 15));
    const int g = (tt & 63) >> 3, j = tt & 7;
    L.rec = lb + L_SORT + (first + g * n_) * 32;
    L.qn  = lb + L_QN + (first + g * n_) * 4;
    L.c16 = j * 16;
    L.ea  = lb + L_EA + j * 16;
    L.agg = lb + L_AGG + j * 4;
    asm volatile("" : "+v"(L.rec), "+v"(L.qn), "+v"(L.ea), "+v"(L.agg));
    return n_;
  };

  int take = 64;
  bool fast = false; float delta = 0.f;
  if (wv <= 12) {
    if (wv < 12) {
      await_lds(&misc[M_CE], 7u);
      poll_segments(tb >> 3);
      TS(4);
      load_recs(R, sg, ws, tb >> 3, tb & 7, 0);
      post_total(0, tb & 7, lb_);
      uint32_t v = (tb & 7) == 0 ? (sg[0] >> 16) + (sg[1] >> 16) + (sg[2] >> 16) : 0u;
      v = wave_scan_add(v);
      if (lb_ == 63) __hip_atomic_fetch_add(&misc[M_ALL], v, RLX_WG);
      arrive(&misc[M_CP], lb_);
      WAIT_RECS(R);
      TS(5);
      rank(tb, 0, take);
    } else {
      const uint32_t cm = wave_max_u32(max(max(absbits(L.c01[0]), absbits(L.c01[1])), max(absbits(L.c23[0]), absbits(L.c23[1]))));
      await_lds(&misc[M_CB], 3u);
      if (lb_ == 0) {
        const u32x4 m = *(const u32x4*)(misc + M_MX), q = *(const u32x4*)(misc + M_MX + 4);
        const float sq = (__uint_as_float(q[1]) + __uint_as_float(q[2])) + __uint_as_float(q[3]);
        gu64* g = (gu64*)(ws + WS_TBG) + 2 * w;
        __hip_atomic_store(g,     (u64)(min((m[0] + 0xffffu) >> 16, 0x7fffu) | (min((m[1] + 0xffffu) >> 16, 0x7fffu) << 16)) | ((u64)tag32 << 32), RLX_AGENT);
        __hip_atomic_store(g + 1, (u64)__float_as_uint(sq) | ((u64)tag32 << 32), RLX_AGENT);
      }
      await_lds(&misc[M_CP], 12u);
      gu64* gt = (gu64*)(ws + WS_TBG) + 2 * lb_;
      gu64* ge = (gu64*)(ws + WS_EMG) + lb_;
      uint32_t mb, mv_, me; float sq;
      for (unsigned spins = 0;; spins++) {
        bool ok = true; mb = mv_ = me = 0; sq = 0.f;
#pragma unroll
        for (int i = 0; i < 4; i++) {
          const u64 v0 = __hip_atomic_load(gt + 128 * i, RLX_AGENT), v1 = __hip_atomic_load(gt + 128 * i + 1, RLX_AGENT), v2 = __hip_atomic_load(ge + 64 * i, RLX_AGENT);
          ok &= (uint32_t)(v0 >> 32) == tag32 && (uint32_t)(v1 >> 32) == tag32 && (uint32_t)(v2 >> 32) == tag32;
          mb = max(mb, (uint32_t)v0 << 16); mv_ = max(mv_, (uint32_t)v0 & 0xffff0000u);
          me = max(me, (uint32_t)v2); sq += __uint_as_float((uint32_t)v1);
        }
        if (__all(ok)) break;
        if (spins > SPIN_CAP) break;
        __builtin_amdgcn_s_sleep(16);
      }
      mb = wave_max_u32(mb); mv_ = wave_max_u32(mv_); me = wave_max_u32(me); sq = wave_sum_f32(sq);
      await_lds(&misc[M_CE], 7u);
      const float mv = __uint_as_float(mv_);
      const float yb = (__uint_as_float(misc[M_XAM]) + __uint_as_float(mb) + __uint_as_float(me) * __uint_as_float(cm)) * 1.01f;
      const float all = (float)(misc[M_ALL] + 4u);
      const int p1 = (int)(__float_as_uint(4.1f * mv) >> 23) - 127 + 2, p2 = (int)(__float_as_uint(all * mv * 1.01f) >> 23) - 127 + 1 - 8;
      const int p = max(max(p1, p2), -80);
      const float dl = __uint_as_float((uint32_t)(p - 23 + 127) << 23);
      const bool fine = dl * 4096.f <= __builtin_amdgcn_sqrtf(sq * __builtin_amdgcn_rcpf(64.f * (float)N)) * 0.99f || mv == 0.f;
      if (lb_ == 63) {
        misc[M_FAST] = (yb <= 28.f) && (mv <= 1.0e12f) && fine;
        misc[M_EXP] = (uint32_t)(p + 127);
      }
  }
    group_barrier(lb_);
    if (w == 0 && tb == 0) __hip_atomic_store((gu64*)(ws + WS_NONCE), (((u64)xn[1] << 32) | xn[0]) + 1ull, RLX_AGENT);
    if (__builtin_amdgcn_readfirstlane(misc[M_TOT]) > (uint32_t)TAKE_MAX) {
      group_barrier(lb_);
      if (tb < 256) *(uint32_t*)(smem + L_CNT + tb * 4) = 0;
      group_barrier(lb_);
      take = 16;
      if (wv < 12) rank(tb, 0, take);
      group_barrier(lb_);
    }
    if (wv == 12) scan(lb_);
    else {
      for (int i = tb; i < 197 * AGG_ROW / 16; i += 768) *(u32x4*)(smem + L_AGG + i * 16) = u32x4{0, 0, 0, 0};
      if (__builtin_amdgcn_readfirstlane(misc[M_FAST])) {
        for (int i = tb; i < NPB * 8; i += 768) {
          f32x4 v = *(const f32x4*)(smem + L_EA + i * 16);
          v[0] = __builtin_amdgcn_exp2f(v[0]); v[1] = __builtin_amdgcn_exp2f(v[1]); v[2] = __builtin_amdgcn_exp2f(v[2]); v[3] = __builtin_amdgcn_exp2f(v[3]);
          *(f32x4*)(smem + L_EA + i * 16) = v;
        }
      }
    }
    await_lds(&misc[M_CB], 3u);
    group_barrier(lb_);
    TS(6);
    scatter(tb);
    if (wv == 12) {
      gu64* gt = (gu64*)(ws + WS_TRG) + lb_;
      for (unsigned spins = 0; spins < SPIN_CAP; spins++) {
        bool ok = true;
#pragma unroll
        for (int i = 0; i < 4; i++) ok &= (uint32_t)(__hip_atomic_load(gt + 64 * i, RLX_AGENT) >> 32) == tag32;
        if (__all(ok)) break;
        __builtin_amdgcn_s_sleep(16);
      }
    }
  }
  LDS_BARRIER();
  fast = __builtin_amdgcn_readfirstlane(misc[M_FAST]);
  take = __builtin_amdgcn_readfirstlane(misc[M_TOT]) > (uint32_t)TAKE_MAX ? 16 : 64;
  {
    const uint32_t pexp = __builtin_amdgcn_readfirstlane(misc[M_EXP]);
    delta = __uint_as_float((pexp - 23u) << 23);
    const float mg = __uint_as_float((pexp << 23) | 0x400000u);
    L.magic = f32x2{mg, mg};
  }
  int again = __builtin_amdgcn_readfirstlane(misc[M_MORE]);
  int nstep = __builtin_amdgcn_readfirstlane(misc[M_NSTEP]);
  TS(7);
  {
    const int n_ = lane_consts(tb, nstep);
    if (fast) edge_loop<0>(L, n_, tsrd);
    else      edge_loop<2>(L, n_, tsrd);
  }
  while (again) {
    done += take;
    const int t2 = thread_id();
    if (wv < 12) { poll_segments(t2 >> 3); load_recs(R, sg, ws, t2 >> 3, t2 & 7, done); WAIT_RECS(R); }
    LDS_BARRIER();
    if (t2 < 256) *(uint32_t*)(smem + L_CNT + t2 * 4) = 0;
    if (t2 == 0) { misc[M_MORE] = 0; misc[M_TOT] = 0; }
    LDS_BARRIER();
    if (wv < 12) post_total(done, t2 & 7, t2 & 63);
    LDS_BARRIER();
    take = __builtin_amdgcn_readfirstlane(misc[M_TOT]) <= (uint32_t)TAKE_MAX ? 64 : 16;
    if (wv < 12) rank(t2, done, take);
    LDS_BARRIER();
    if (wv == 12) scan(t2 & 63);
    LDS_BARRIER();
    scatter(t2);
    again = __builtin_amdgcn_readfirstlane(misc[M_MORE]);
    LDS_BARRIER();
    nstep = __builtin_amdgcn_readfirstlane(misc[M_NSTEP]);
    const int n_ = lane_consts(t2, nstep);
    if (fast) edge_loop<0>(L, n_, tsrd);
    else      edge_loop<2>(L, n_, tsrd);
  }
  LDS_BARRIER();

  const int te = thread_id();
  if (te < NPB * 4) {
    const int nl = te >> 2, u = te & 3, node = b * NPB + nl;
    if (node < N) {
      float y[8];
      const uint32_t bias = *(const uint32_t*)(smem + L_NQ + nl * 4) * __float_as_uint(L.magic[0]);
      const f32x4 u0 = *(const f32x4*)(smem + L_XU + nl * 128 + u * 32), u1 = *(const f32x4*)(smem + L_XU + nl * 128 + u * 32 + 16);
#pragma unroll
      for (int i = 0; i < 4; i++) {
        const u32x2 v2 = *(const u32x2*)(smem + L_AGG + nl * AGG_ROW + (8 * i + 2 * u) * 4);
#pragma unroll
        for (int j = 0; j < 2; j++) {
          const float agg = fast ? (float)(int)(v2[j] - bias) * delta : __uint_as_float(v2[j]);
          const float v = (j ? u1[i] : u0[i]) + agg;
          y[4 * j + i] = v * __builtin_amdgcn_rcpf(1.f + __builtin_amdgcn_exp2f(v * NLOG2E));
        }
      }
      const u32x4 o = {cvtpk(y[0], y[1]), cvtpk(y[2], y[3]), cvtpk(y[4], y[5]), cvtpk(y[6], y[7])};
      *(u32x4*)(out + (size_t)node * 64 + 32 * half + 8 * u) = o;
    }
  }
  TS_OUT(256 + blockIdx.x);
}

}

extern "C" void kernel_launch(void* const* d_in, const int* in_sizes, int n_in,
                              void* d_out, int out_size, void* d_ws, size_t ws_size,
                              hipStream_t stream) {
  const uint16_t* x  = (const uint16_t*)d_in[0];
  const uint16_t* ea = (const uint16_t*)d_in[1];
  const int*      src = (const int*)d_in[2];
  const int*      dst = (const int*)d_in[3];
  const uint16_t* uw = (const uint16_t*)d_in[4];
  const uint16_t* vw = (const uint16_t*)d_in[5];
  const uint16_t* gw = (const uint16_t*)d_in[6];
  const int N = in_sizes[0] / 64, E = in_sizes[2];
  char* ws = (char*)d_ws;
  (void)hipFuncSetAttribute((const void*)k_mpnn, hipFuncAttributeMaxDynamicSharedMemorySize, L_TOTAL);
  k_mpnn<<<NCH, 1024, L_TOTAL, stream>>>(src, dst, ea, x, uw, vw, gw, ws, (uint16_t*)d_out, N, E);
}
```

```cpp
#include <hip/hip_runtime.h>
#include <hip/hip_bf16.h>
#include <stdint.h>

#pragma clang diagnostic ignored "-Wint-to-pointer-cast"
#pragma clang diagnostic ignored "-Wpointer-to-int-cast"
namespace {

typedef float    f32x2  __attribute__((ext_vector_type(2)));
typedef float    f32x4  __attribute__((ext_vector_type(4)));
typedef float    f32x16 __attribute__((ext_vector_type(16)));
typedef short    bf16x8 __attribute__((ext_vector_type(8)));
typedef short    s16x4  __attribute__((ext_vector_type(4)));
typedef uint32_t u32x2  __attribute__((ext_vector_type(2)));
typedef uint32_t u32x4  __attribute__((ext_vector_type(4)));
typedef unsigned long long u64;
typedef __attribute__((address_space(1))) unsigned long long gu64;
#define LDSP(T) __attribute__((address_space(3))) T*
#define DI __device__ __forceinline__
#define RLX_AGENT __ATOMIC_RELAXED, __HIP_MEMORY_SCOPE_AGENT
#define RLX_WG    __ATOMIC_RELAXED, __HIP_MEMORY_SCOPE_WORKGROUP

constexpr int   NCH = 256;
constexpr int   NB  = 128;
constexpr int   NPB = 196;
constexpr int   CH  = 3128;
constexpr int   CHB = 880;
constexpr int   NMAX = NB * NPB;
constexpr int   NTILE = NMAX / 32;
constexpr float NLOG2E = -1.4426950408889634f;
constexpr int   AUX_SC1 = 16;
constexpr unsigned SPIN_CAP = 1u << 18;
static_assert(4 * CHB >= CH + 3 * NB && (CHB * 20) % 16 == 0, "blocks");

constexpr size_t WS_NONCE = 0;
constexpr size_t WS_SEG = 128;
constexpr size_t WS_TBG = WS_SEG + (size_t)NCH * NB * 8;
constexpr size_t WS_EMG = WS_TBG + (size_t)NCH * 16;
constexpr size_t WS_TRG = WS_EMG + (size_t)NCH * 8;
constexpr size_t WS_REC = WS_TRG + (size_t)NCH * 8;
constexpr size_t WS_TAB = (WS_REC + (size_t)NCH * CHB * 20 + 1024 + 127) & ~(size_t)127;
constexpr size_t WS_DBG = WS_TAB + (size_t)2 * NMAX * 128;
static_assert(WS_REC % 128 == 0 && WS_TAB % 128 == 0, "ws align");

constexpr int AGG_ROW  = 160;
constexpr int SORT_CAP = 8128;
constexpr int TAKE_MAX = SORT_CAP - 3 * NPB - 48;
constexpr int L_CNT    = 0;
constexpr int L_OFF    = L_CNT + 1024;
constexpr int L_NQ     = L_OFF + 1024;
constexpr int L_MISC   = L_NQ + 1024;
constexpr int L_QN     = L_MISC + 512;
constexpr int L_EA     = L_QN + SORT_CAP;
constexpr int L_XU     = L_EA + 197 * 128;
constexpr int L_AGG    = (L_XU + 196 * 128 + 127) & ~127;
constexpr int L_SORT   = (L_AGG + 197 * AGG_ROW + 127) & ~127;
constexpr int L_TOTAL  = L_SORT + SORT_CAP * 8;
static_assert(L_TOTAL <= 163840, "LDS");
static_assert((L_QN & 15) == 0 && (L_EA & 15) == 0 && (L_XU & 15) == 0 && TAKE_MAX >= 4096, "align");
constexpr int A_HIST = L_SORT;
constexpr int A_BASE = A_HIST + 512;
constexpr int A_ST   = A_BASE + 512;
constexpr int A_W    = (A_ST + CHB * 20 + 127) & ~127;
constexpr int A_IMG  = A_W + 16384;
constexpr int P_W    = A_IMG + 16384;
constexpr int P_X    = L_AGG;
static_assert(P_W + 8192 <= L_TOTAL && P_X + 224 * 128 <= L_SORT, "phase A images");
constexpr uint32_t QN_DUMMY = (196u * 128u) | ((196u * AGG_ROW) << 16);
constexpr uint32_t OFF_DUMMY = 0x7fffff00u;
constexpr int M_MORE = 0, M_NSTEP = 1, M_T4 = 2;
constexpr int M_CW = 3, M_CB = 4, M_CT = 5, M_PUB = 6;
constexpr int M_CL = 7, M_CE = 8, M_CP = 9;
constexpr int M_TOT = 10, M_ALL = 11, M_XAM = 12;
constexpr int M_FAST = 13, M_EXP = 14, M_GB = 15, M_CR = 27, M_TAG0 = 24, M_TAG1 = 25, M_TGF = 26;
constexpr int M_MX = 16;

DI float    bf2f(uint32_t v16) { return __uint_as_float(v16 << 16); }
DI uint32_t cvtpk(float lo, float hi) {
  typedef __bf16 bf16x2 __attribute__((ext_vector_type(2)));
  return __builtin_bit_cast(uint32_t, __builtin_convertvector(f32x2{lo, hi}, bf16x2));
}
DI f32x16   mfma(bf16x8 a, bf16x8 b, f32x16 c) { return __builtin_amdgcn_mfma_f32_32x32x16_bf16(a, b, c, 0, 0, 0); }
DI int      crow(int r, int h) { return (r & 3) + 8 * (r >> 2) + 4 * h; }
DI uint32_t absbits(float v) { return __float_as_uint(v) & 0x7fffffffu; }

template <int CTRL, int RM> DI uint32_t dpp0(uint32_t v) { return (uint32_t)__builtin_amdgcn_update_dpp(0, (int)v, CTRL, RM, 0xf, false); }
DI uint32_t wave_scan_add(uint32_t v) {
  v += dpp0<0x111, 0xf>(v); v += dpp0<0x112, 0xf>(v); v += dpp0<0x114, 0xf>(v); v += dpp0<0x118, 0xf>(v);
  v += dpp0<0x142, 0xa>(v); v += dpp0<0x143, 0xc>(v);
  return v;
}
DI uint32_t wave_max_u32(uint32_t v) {
  v = max(v, dpp0<0x111, 0xf>(v)); v = max(v, dpp0<0x112, 0xf>(v)); v = max(v, dpp0<0x114, 0xf>(v)); v = max(v, dpp0<0x118, 0xf>(v));
  v = max(v, dpp0<0x142, 0xa>(v)); v = max(v, dpp0<0x143, 0xc>(v));
  return v;
}
DI float wave_sum_f32(float s) {
  s += __uint_as_float(dpp0<0x111, 0xf>(__float_as_uint(s))); s += __uint_as_float(dpp0<0x112, 0xf>(__float_as_uint(s)));
  s += __uint_as_float(dpp0<0x114, 0xf>(__float_as_uint(s))); s += __uint_as_float(dpp0<0x118, 0xf>(__float_as_uint(s)));
  s += __uint_as_float(dpp0<0x142, 0xa>(__float_as_uint(s))); s += __uint_as_float(dpp0<0x143, 0xc>(__float_as_uint(s)));
  return s;
}
#define LDS_BARRIER() asm volatile("s_waitcnt lgkmcnt(0)\n\ts_barrier" ::: "memory")
#define WAIT_VM(N) asm volatile("s_waitcnt vmcnt(" #N ")" ::: "memory")

DI uint32_t arrive(uint32_t* word, int lane) {
  asm volatile("s_waitcnt lgkmcnt(0)" ::: "memory");
  uint32_t v = 0;
  if (lane == 0) v = __hip_atomic_fetch_add(word, 1u, RLX_WG);
  return __builtin_amdgcn_readfirstlane(v);
}
DI void await_lds(uint32_t* word, uint32_t want) {
  for (unsigned spins = 0; spins < SPIN_CAP; spins++) {
    if (__builtin_amdgcn_readfirstlane(__hip_atomic_load(word, RLX_WG)) >= want) break;
    __builtin_amdgcn_s_sleep(1);
  }
  asm volatile("" ::: "memory");
}

DI void dma16(uint32_t ldsaddr, uint32_t voff, __amdgpu_buffer_rsrc_t srd) {
  asm volatile("s_mov_b32 m0, %0\n\ts_nop 0\n\tbuffer_load_dwordx4 %1, %2, 0 offen lds" :: "s"(ldsaddr), "v"(voff), "s"(srd) : "memory");
}
DI bf16x8 tr_frag(uint32_t addr, int four_rows) {
  const s16x4 lo = __builtin_amdgcn_ds_read_tr16_b64_v4i16((LDSP(s16x4))addr);
  const s16x4 hi = __builtin_amdgcn_ds_read_tr16_b64_v4i16((LDSP(s16x4))(addr + four_rows));
  return (bf16x8)__builtin_shufflevector(lo, hi, 0, 1, 2, 3, 4, 5, 6, 7);
}

#define TS(i) do { } while (0)
#define TS_DECL do { } while (0)
#define TS_OUT(slot) do { } while (0)
#define TS_PUB(i) do { } while (0)

DI uint32_t shape(float v, uint32_t low) {
  const uint32_t b = __float_as_uint(v);
  const uint32_t t = min(__builtin_elementwise_sub_sat((b & 0x7fffffffu) + 0x8000u, low), 0x7fffffffu);
  return (b & 0x80000000u) | (t & 0xffff0000u) | low;
}
DI void table_half(u32x4* o, const bf16x8* xf, uint32_t troff, int c, float& mb, float& mv, float& s2) {
  f32x16 zb, hv;
#pragma unroll
  for (int i = 0; i < 16; i++) { zb[i] = 0.f; hv[i] = 0.f; }
#pragma unroll
  for (int s = 0; s < 4; s++) {
    zb = mfma(tr_frag(troff + s * 2048 + c * 64, 512), xf[s], zb);
    hv = mfma(tr_frag(troff + 8192 + s * 2048 + c * 64, 512), xf[s], hv);
  }
#pragma unroll
  for (int q = 0; q < 4; q++) {
#pragma unroll
    for (int p = 0; p < 2; p++) {
      const int i = 4 * q + 2 * p;
      const float b0 = zb[i] * NLOG2E, b1 = zb[i + 1] * NLOG2E, v0 = hv[i], v1 = hv[i + 1];
      mb = fmaxf(mb, fmaxf(fabsf(b0), fabsf(b1))); mv = fmaxf(mv, fmaxf(fabsf(v0), fabsf(v1)));
      s2 = fmaf(v0, v0, s2); s2 = fmaf(v1, v1, s2);
      const uint32_t d0 = shape(b0, cvtpk(v0, 0.f));
      const uint32_t w1 = shape(v1, d0 >> 16);
      o[q][2 * p] = d0; o[q][2 * p + 1] = shape(b1, w1 >> 16);
    }
  }
}
DI void table_image(const u32x4* o, uint32_t img, int lane) {
  const int n31 = lane & 31, h = lane >> 5;
#pragma unroll
  for (int q = 0; q < 4; q++) *(LDSP(u32x4))(img + n31 * 128 + (((2 * q + h) ^ (n31 & 7)) * 16)) = o[q];
  asm volatile("s_waitcnt lgkmcnt(0)" ::: "memory");
}
DI void table_rows(u32x4* r, uint32_t img, int lane) {
#pragma unroll
  for (int i = 0; i < 4; i++) { const int row = 8 * i + (lane >> 3); r[i] = *(LDSP(const u32x4))(img + row * 128 + (((lane & 7) ^ (row & 7)) * 16)); }
  asm volatile("s_waitcnt lgkmcnt(0)" : "+v"(r[0]), "+v"(r[1]), "+v"(r[2]), "+v"(r[3]) :: "memory");
}
DI void table_store(const u32x4* r, int tile, int c, __amdgpu_buffer_rsrc_t tsrd, int lane) {
#pragma unroll
  for (int i = 0; i < 4; i++)
    __builtin_amdgcn_raw_buffer_store_b128(r[i], tsrd, (c * NMAX + tile * 32 + 8 * i + (lane >> 3)) * 128 + (lane & 7) * 16, 0, AUX_SC1);
}

DI f32x2 exp2x2(f32x2 v) { f32x2 r = {__builtin_amdgcn_exp2f(v[0]), __builtin_amdgcn_exp2f(v[1])}; return r; }
DI f32x2 rcpx2(f32x2 v) { f32x2 r = {__builtin_amdgcn_rcpf(v[0]), __builtin_amdgcn_rcpf(v[1])}; return r; }
DI f32x2 fma2(f32x2 a, f32x2 b, f32x2 c) { return __builtin_elementwise_fma(a, b, c); }
template <int MUL> DI uint32_t madlo_i(uint32_t v, uint32_t add) {
  uint32_t r; asm("v_mad_u32_u16 %0, %1, %3, %2" : "=v"(r) : "v"(v), "v"(add), "i"(MUL)); return r;
}
template <int MUL> DI uint32_t madhi_i(uint32_t v, uint32_t add) {
  uint32_t r; asm("v_mad_u32_u16 %0, %1, %3, %2 op_sel:[1,0,0,0]" : "=v"(r) : "v"(v), "v"(add), "i"(MUL)); return r;
}
template <int K> DI uint32_t byte_x4(uint32_t v) {
  uint32_t r;
  if (K == 0) asm("v_lshlrev_b32_sdwa %0, 2, %1 dst_sel:DWORD dst_unused:UNUSED_PAD src0_sel:DWORD src1_sel:BYTE_0" : "=v"(r) : "v"(v));
  if (K == 1) asm("v_lshlrev_b32_sdwa %0, 2, %1 dst_sel:DWORD dst_unused:UNUSED_PAD src0_sel:DWORD src1_sel:BYTE_1" : "=v"(r) : "v"(v));
  if (K == 2) asm("v_lshlrev_b32_sdwa %0, 2, %1 dst_sel:DWORD dst_unused:UNUSED_PAD src0_sel:DWORD src1_sel:BYTE_2" : "=v"(r) : "v"(v));
  if (K == 3) asm("v_lshlrev_b32_sdwa %0, 2, %1 dst_sel:DWORD dst_unused:UNUSED_PAD src0_sel:DWORD src1_sel:BYTE_3" : "=v"(r) : "v"(v));
  return r;
}
DI uint32_t byte_x4(uint32_t v, int k) { return k == 0 ? byte_x4<0>(v) : (k == 1 ? byte_x4<1>(v) : (k == 2 ? byte_x4<2>(v) : byte_x4<3>(v))); }
DI uint32_t lo_x128(uint32_t v) { uint32_t r; asm("v_mad_u32_u16 %0, %1, %2, 0" : "=v"(r) : "v"(v), "s"(128u)); return r; }
DI void lds_add_u32(uint32_t addr, float v) { __hip_atomic_fetch_add((LDSP(uint32_t))addr, __float_as_uint(v), RLX_WG); }
DI void lds_add_f32(uint32_t addr, float v) { __hip_atomic_fetch_add((LDSP(float))addr, v, RLX_WG); }

struct Lane {
  uint32_t rec;
  uint32_t qn;
  uint32_t c16;
  uint32_t ea, agg;
  f32x2 c01, c23;
  f32x2 magic;
};
struct Quad {
  u32x4 d[4];
  u32x4 r0, r1;
  uint32_t q;
};
template <int S> DI void fetch(Quad& Q, const Lane& L, __amdgpu_buffer_rsrc_t srd) {
  Q.r0 = *(LDSP(const u32x4))(L.rec + S * 32);
  Q.r1 = *(LDSP(const u32x4))(L.rec + S * 32 + 16);
  Q.q  = *(LDSP(const uint32_t))(L.qn + S * 4);
  Q.d[0] = __builtin_amdgcn_raw_buffer_load_b128(srd, Q.r0[0] + L.c16, 0, AUX_SC1);
  Q.d[1] = __builtin_amdgcn_raw_buffer_load_b128(srd, Q.r0[2] + L.c16, 0, AUX_SC1);
  Q.d[2] = __builtin_amdgcn_raw_buffer_load_b128(srd, Q.r1[0] + L.c16, 0, AUX_SC1);
  Q.d[3] = __builtin_amdgcn_raw_buffer_load_b128(srd, Q.r1[2] + L.c16, 0, AUX_SC1);
  asm volatile("" ::: "memory");
}
DI f32x2 fma_e(u32x2 rec, f32x2 c, u32x2 xb) {
  f32x2 r; asm("v_pk_fma_f32 %0, %1, %2, %3 op_sel:[1,0,0] op_sel_hi:[1,1,1]" : "=v"(r) : "v"(rec), "v"(c), "v"(xb)); return r;
}
DI f32x2 vpair(u32x2 d) {
  f32x2 r; asm("v_lshlrev_b64 %0, 16, %1" : "=v"(r) : "v"(d)); return r;
}
template <int MODE> DI void gate(const Quad& Q, const Lane& L) {
  const f32x4 ea = *(LDSP(const f32x4))(madlo_i<1>(Q.q, L.ea));
  const uint32_t ga = madhi_i<1>(Q.q, L.agg);
  const u32x2 rec[4] = {u32x2{Q.r0[0], Q.r0[1]}, u32x2{Q.r0[2], Q.r0[3]}, u32x2{Q.r1[0], Q.r1[1]}, u32x2{Q.r1[2], Q.r1[3]}};
  const f32x2 one = {1.f, 1.f};
#pragma unroll
  for (int p = 0; p < 2; p++) {
    const f32x2 c = p ? L.c23 : L.c01, eap = {ea[2 * p], ea[2 * p + 1]};
    f32x2 a[4], hv[4];
#pragma unroll
    for (int k = 0; k < 4; k++) {
      const u32x2 dd = {Q.d[k][2 * p], Q.d[k][2 * p + 1]};
      hv[k] = vpair(dd);
      const f32x2 y = fma_e(rec[k], c, dd);
      a[k] = MODE == 2 ? one + exp2x2(y + eap) : fma2(eap, exp2x2(y), one);
    }
    if (MODE == 0) {
      const f32x2 d01 = a[0] * a[1], n01 = fma2(hv[1], a[0], hv[0] * a[1]);
      const f32x2 d23 = a[2] * a[3], n23 = fma2(hv[3], a[2], hv[2] * a[3]);
      const f32x2 m = fma2(fma2(n23, d01, n01 * d23), rcpx2(d01 * d23), L.magic);
      lds_add_u32(ga + 64 * p, m[0]); lds_add_u32(ga + 64 * p + 32, m[1]);
    } else {
      const f32x2 m = (hv[0] * rcpx2(a[0]) + hv[1] * rcpx2(a[1])) + (hv[2] * rcpx2(a[2]) + hv[3] * rcpx2(a[3]));
      lds_add_f32(ga + 64 * p, m[0]); lds_add_f32(ga + 64 * p + 32, m[1]);
    }
  }
}
template <int MODE> DI void edge_loop(Lane& L, int n, __amdgpu_buffer_rsrc_t srd) {
  if (n <= 0) return;
  Quad A, B;
  fetch<0>(A, L, srd);
  while (true) {
    fetch<1>(B, L, srd);
    gate<MODE>(A, L);
    if (--n == 0) break;
    fetch<2>(A, L, srd);
    gate<MODE>(B, L);
    if (--n == 0) break;
    L.rec += 64; L.qn += 8;
  }
}

struct Recs { u32x4 rc[6]; uint32_t lc[6]; };
DI void load_recs(Recs& R, const uint32_t* sg, const char* ws, int g, int j, int done) {
  const __amdgpu_buffer_rsrc_t rsrd = __builtin_amdgcn_make_buffer_rsrc((void*)(ws + WS_REC), (short)0, NCH * CHB * 20, 0x00020000);
#pragma unroll
  for (int p = 0; p < 2; p++) {
#pragma unroll
    for (int r = 0; r < 3; r++) {
      uint32_t off = (uint32_t)((g + 96 * r) * (CHB * 20) + ((int)(sg[r] & 0xffff) + (done >> 2) + 8 * p + j) * 20);
      if ((int)(sg[r] >> 16) - done <= 32 * p + 4 * j) off = 0x40000000u;
      asm volatile("buffer_load_dwordx4 %0, %1, %2, 0 offen sc1" : "=&v"(R.rc[2 * r + p]) : "v"(off), "s"(rsrd) : "memory");
      asm volatile("buffer_load_dword %0, %1, %2, 0 offen offset:16 sc1" : "=&v"(R.lc[2 * r + p]) : "v"(off), "s"(rsrd) : "memory");
    }
  }
}
#define WAIT_RECS(R) asm volatile("s_waitcnt vmcnt(0)" : "+v"(R.rc[0]), "+v"(R.rc[1]), "+v"(R.rc[2]), "+v"(R.rc[3]), "+v"(R.rc[4]), "+v"(R.rc[5]), \
                                  "+v"(R.lc[0]), "+v"(R.lc[1]), "+v"(R.lc[2]), "+v"(R.lc[3]), "+v"(R.lc[4]), "+v"(R.lc[5]) :: "memory")

__global__ __launch_bounds__(1024) void k_mpnn(const int* __restrict__ src, const int* __restrict__ dst,
                                               const uint16_t* __restrict__ ea, const uint16_t* __restrict__ x,
                                               const uint16_t* __restrict__ uw, const uint16_t* __restrict__ vw,
                                               const uint16_t* __restrict__ gw, char* __restrict__ ws,
                                               uint16_t* __restrict__ out, int N, int E) {
  extern __shared__ __attribute__((aligned(128))) char smem[];
  const uint32_t lb = (uint32_t)(uintptr_t)(LDSP(char))smem;
  uint32_t* misc = (uint32_t*)(smem + L_MISC);
  const int t = threadIdx.x, lane = t & 63;
  const int w = blockIdx.x;
  const int half = (blockIdx.x & 7) >> 2, b = (blockIdx.x & 3) * 32 + (blockIdx.x >> 3);
  const int wv = __builtin_amdgcn_readfirstlane(t >> 6);
  const int r31 = lane & 31, h = lane >> 5;
  TS_DECL; TS(0);

  uint32_t gb = 0;
  auto group_barrier = [&](int l) { gb += 13; arrive(&misc[M_GB], l); await_lds(&misc[M_GB], gb); };
  const __amdgpu_buffer_rsrc_t xsrd = __builtin_amdgcn_make_buffer_rsrc((void*)x, (short)0, N * 128, 0x00020000);
  const uint32_t cA = (uint32_t)(((lane & 7) ^ (lane >> 4)) * 16);
  u32x2 xn, cw;
#define LOAD_XN_CW(a, b) do { \
    asm volatile("global_load_dwordx2 %0, %1, %2 sc1" : "=&v"(a) : "v"(0), "s"(ws + WS_NONCE) : "memory"); \
    asm volatile("global_load_dwordx2 %0, %1, %2" : "=&v"(b) : "v"((128 * 64 + 32 * half + 4 * (t & 7)) * 2), "s"(gw) : "memory"); } while (0)
  Lane L;
  if (wv >= 13) {
    u32x2 xn1, cw1;
    LOAD_XN_CW(xn1, cw1);
    const uint32_t img = lb + A_IMG + (wv & 3) * 4096;
    auto tile_rows = [&](uint32_t to, int tile) {
#pragma unroll
      for (int i = 0; i < 4; i++) dma16(to + i * 1024, (tile * 32 + 8 * i + (lane >> 3)) * 128 + ((i & 1) ? cA ^ 64u : cA), xsrd);
    };
    const __amdgpu_buffer_rsrc_t bsrd = __builtin_amdgcn_make_buffer_rsrc((void*)(gw + 64 * 64), (short)0, 8192, 0x00020000);
    const __amdgpu_buffer_rsrc_t vsrd = __builtin_amdgcn_make_buffer_rsrc((void*)vw, (short)0, 8192, 0x00020000);
    const int tile = w + 256 * (wv - 13);
    const bool extra = wv == 13 && w + 768 < NTILE;
    LDS_BARRIER();
    asm volatile("s_waitcnt vmcnt(0)" : "+v"(xn1), "+v"(cw1) :: "memory");
    xn = xn1; cw = cw1;
    tile_rows(img, tile);
    if (extra) tile_rows(lb + A_IMG, w + 768);
#pragma unroll
    for (int i = 0; i < 6; i++) {
      const int p = wv - 13 + 3 * i;
      if (p < 8) dma16(lb + A_W + p * 1024, p * 1024 + lane * 16, bsrd);
      else if (p < 16) dma16(lb + A_W + p * 1024, (p - 8) * 1024 + lane * 16, vsrd);
    }
    WAIT_VM(0);
    const uint32_t tag = xn[0] + 1u;
    arrive(&misc[M_CL], lane);
    await_lds(&misc[M_CL], 3u);
    const __amdgpu_buffer_rsrc_t wsrd = __builtin_amdgcn_make_buffer_rsrc((void*)(ws + WS_TAB), (short)0, 2 * NMAX * 128, 0x00020000);
    const uint32_t troff = lb + A_W + (8 * h + ((lane & 15) >> 2)) * 128 + (16 * ((lane >> 4) & 1) + 4 * (lane & 3)) * 2;
    const uint32_t xa = img + r31 * 128 + ((h ^ ((r31 >> 1) & 7)) * 16), xe = xa - img + lb + A_IMG;
    float mb = 0.f, mv = 0.f, s2 = 0.f;
    u32x4 ra[4], rb[4], rc[4], rd[4];
    auto tile_halves = [&](uint32_t x0, u32x4* r0, u32x4* r1) {
      bf16x8 xf[4]; u32x4 o[4];
#pragma unroll
      for (int s = 0; s < 4; s++) xf[s] = *(LDSP(const bf16x8))(x0 ^ (32u * s));
      table_half(o, xf, troff, 0, mb, mv, s2);
      table_image(o, img, lane);
      table_rows(r0, img, lane);
      table_half(o, xf, troff, 1, mb, mv, s2);
      table_image(o, img, lane);
      table_rows(r1, img, lane);
    };
    tile_halves(xa, ra, rb);
    if (extra) tile_halves(xe, rc, rd);
    const uint32_t m0 = wave_max_u32(__float_as_uint(mb)), m1 = wave_max_u32(__float_as_uint(mv));
    s2 = wave_sum_f32(s2);
    if (lane == 63) {
      __hip_atomic_fetch_max(&misc[M_MX], m0, RLX_WG); __hip_atomic_fetch_max(&misc[M_MX + 1], m1, RLX_WG);
      misc[M_MX + 4 + (wv & 3)] = __float_as_uint(s2);
    }
    arrive(&misc[M_CB], lane);
    await_lds(&misc[M_CP], 12u);
    table_store(ra, tile, 0, wsrd, lane);
    table_store(rb, tile, 1, wsrd, lane);
    if (extra) { table_store(rc, w + 768, 0, wsrd, lane); table_store(rd, w + 768, 1, wsrd, lane); }
    WAIT_VM(0);
    if (arrive(&misc[M_CT], lane) == 2 && lane == 0) { __hip_atomic_store((gu64*)(ws + WS_TRG) + w, (u64)tag << 32, RLX_AGENT); TS_PUB(1); }
  } else {
    const int e0 = w * CH;
    const int n  = min(CH, E - e0);
    const int cnt = min(max(n - 4 * t, 0), 4);
    u32x4 d4, s4; u32x2 e2, xn2, cw2;
    LOAD_XN_CW(xn2, cw2);
    {
      const __amdgpu_buffer_rsrc_t dsrd = __builtin_amdgcn_make_buffer_rsrc((void*)dst, (short)0, E * 4, 0x00020000);
      const __amdgpu_buffer_rsrc_t ssrd = __builtin_amdgcn_make_buffer_rsrc((void*)src, (short)0, E * 4, 0x00020000);
      const __amdgpu_buffer_rsrc_t esrd = __builtin_amdgcn_make_buffer_rsrc((void*)ea, (short)0, (E * 2 + 3) & ~3, 0x00020000);
      const uint32_t eo = cnt > 0 ? (uint32_t)(e0 + 4 * t) : 0x20000000u;
      asm volatile("buffer_load_dwordx4 %0, %1, %2, 0 offen" : "=&v"(d4) : "v"(eo * 4), "s"(dsrd) : "memory");
      asm volatile("buffer_load_dwordx4 %0, %1, %2, 0 offen" : "=&v"(s4) : "v"(eo * 4), "s"(ssrd) : "memory");
      asm volatile("buffer_load_dwordx2 %0, %1, %2, 0 offen" : "=&v"(e2) : "v"(eo * 2), "s"(esrd) : "memory");
    }
    uint32_t* hist = (uint32_t*)(smem + A_HIST);
    uint32_t* base = (uint32_t*)(smem + A_BASE);
    if (t < 128) { hist[t] = 0; misc[t] = 0; }
    if (t >= 128 && t < 640) *(uint32_t*)(smem + (t < 384 ? L_CNT + (t - 128) * 4 : L_NQ + (t - 384) * 4)) = 0;
    if (t >= 640 && t < 672) *(float*)(smem + L_EA + 196 * 128 + (t - 640) * 4) = 0.f;
    LDS_BARRIER();
    asm volatile("s_waitcnt vmcnt(0)" : "+v"(xn2), "+v"(cw2), "+v"(d4), "+v"(s4), "+v"(e2) :: "memory");
    xn = xn2; cw = cw2;
    const u64 tag40 = ((((u64)xn[1] << 32) | xn[0]) + 1ull) & 0xffffffffffull;
    const uint32_t ev[4] = {e2[0] & 0xffffu, e2[0] >> 16, e2[1] & 0xffffu, e2[1] >> 16};
    uint32_t bk[4], rk0[4];
#pragma unroll
    for (int k = 0; k < 4; k++) {
      bk[k] = (d4[k] * 21400u) >> 22;
      rk0[k] = 0;
      if (k < cnt) rk0[k] = __hip_atomic_fetch_add(&hist[bk[k]], 1u, RLX_WG);
    }
    TS(1);
    group_barrier(lane);
    if (wv == 0) {
      const u32x2 c = *(const u32x2*)(hist + 2 * t);
      const uint32_t p0 = (c[0] + 3) >> 2, tot = p0 + ((c[1] + 3) >> 2);
      const uint32_t ex = wave_scan_add(tot) - tot;
      *(u32x2*)(base + 2 * t) = u32x2{ex, ex + p0};
      if (t == 0) { misc[M_TAG0] = xn[0]; misc[M_TAG1] = xn[1]; }
    } else if (wv < 12) {
      const __amdgpu_buffer_rsrc_t asrd = __builtin_amdgcn_make_buffer_rsrc((void*)gw, (short)0, 64 * 128, 0x00020000);
      const __amdgpu_buffer_rsrc_t usrd = __builtin_amdgcn_make_buffer_rsrc((void*)uw, (short)0, 64 * 128, 0x00020000);
      const uint32_t woff = (lane >> 2) * 128 + half * 64 + (lane & 3) * 16;
#pragma unroll
      for (int i = 0; i < 3; i++) {
        const int p = wv - 1 + 11 * i;
        if (p < 4) dma16(lb + P_W + p * 1024, p * 2048 + woff, asrd);
        else if (p < 8) dma16(lb + P_W + p * 1024, (p - 4) * 2048 + woff, usrd);
        else { const int q = p - 8; dma16(lb + P_X + q * 1024, (b * NPB + 8 * q + (lane >> 3)) * 128 + ((q & 1) ? cA ^ 64u : cA), xsrd); }
      }
    }
    const uint32_t m2 = wave_max_u32(max(max(ev[0] & 0x7fffu, ev[1] & 0x7fffu), max(ev[2] & 0x7fffu, ev[3] & 0x7fffu)) << 16);
    if (lane == 63) __hip_atomic_fetch_max(&misc[M_MX + 2], m2, RLX_WG);
    group_barrier(lane);
#pragma unroll
    for (int k = 0; k < 4; k++) {
      if (k < cnt) {
        const uint32_t p = base[bk[k]] * 4 + rk0[k];
        const uint32_t o = (p >> 2) * 20 + (p & 3);
        *(uint32_t*)(smem + A_ST + o + (p & 3) * 3) = (s4[k] & 0xffffu) | (ev[k] << 16);
        *(uint8_t*)(smem + A_ST + o + 16) = (uint8_t)(d4[k] - bk[k] * NPB);
      }
    }
    group_barrier(lane);
    TS(2);
    const __amdgpu_buffer_rsrc_t rsrd = __builtin_amdgcn_make_buffer_rsrc((void*)(ws + WS_REC + (size_t)w * CHB * 20), (short)0, CHB * 20, 0x00020000);
    const uint32_t nb = base[127] + ((hist[127] + 3) >> 2);
#pragma unroll
    for (int i = 0; i < 2; i++) {
      const int c = t + 832 * i;
      if (c * 16 < (int)nb * 20) __builtin_amdgcn_raw_buffer_store_b128(*(const u32x4*)(smem + A_ST + c * 16), rsrd, c * 16, 0, AUX_SC1);
    }
    WAIT_VM(0);
    if (arrive(&misc[M_CW], lane) == 12) {
      const u32x2 c = *(const u32x2*)(hist + 2 * lane), o = *(const u32x2*)(base + 2 * lane);
      gu64* g = (gu64*)(ws + WS_SEG) + w * NB + 2 * lane;
      __hip_atomic_store(g,     (u64)(o[0] | (c[0] << 12)) | (tag40 << 24), RLX_AGENT);
      __hip_atomic_store(g + 1, (u64)(o[1] | (c[1] << 12)) | (tag40 << 24), RLX_AGENT);
      if (lane == 0) { __hip_atomic_store((gu64*)(ws + WS_EMG) + w, (u64)misc[M_MX + 2] | ((u64)(xn[0] + 1u) << 32), RLX_AGENT); __hip_atomic_store(&misc[M_PUB], 1u, RLX_WG); TS_PUB(0); }
    }
  }
  const uint32_t tag32 = xn[0] + 1u;
  const u64 tag40 = ((((u64)xn[1] << 32) | xn[0]) + 1ull) & 0xffffffffffull;
  if (wv < 12) {
    if (wv >= 5 && wv < 12) {
      await_lds(&misc[M_CW], 13u);
      const int rt = wv - 5, r31_ = lane & 31, h_ = lane >> 5, row = 32 * rt + r31_;
      const uint32_t xa = lb + P_X + row * 128 + ((h_ ^ ((row >> 1) & 7)) * 16);
      const uint32_t troff = lb + P_W + (8 * h_ + ((lane & 15) >> 2)) * 64 + (16 * ((lane >> 4) & 1) + 4 * (lane & 3)) * 2;
      uint32_t xam = 0;
#pragma unroll
      for (int part = 0; part < 2; part++) {
        f32x16 acc;
#pragma unroll
        for (int i = 0; i < 16; i++) acc[i] = 0.f;
#pragma unroll
        for (int s = 0; s < 4; s++) acc = mfma(*(LDSP(const bf16x8))(xa ^ (32u * s)), tr_frag(troff + part * 4096 + s * 1024, 256), acc);
        const uint32_t o = lb + (part ? L_XU : L_EA) + (32 * rt + 4 * h_) * 128 + r31_ * 4;
#pragma unroll
        for (int i = 0; i < 16; i++) {
          const float v = part ? acc[i] : acc[i] * NLOG2E;
          if (rt < 6 || crow(i, 0) + 4 * h_ < 4) { *(LDSP(float))(o + crow(i, 0) * 128) = v; if (!part) xam = max(xam, absbits(v)); }
        }
      }
      xam = wave_max_u32(xam);
      if (lane == 63) __hip_atomic_fetch_max(&misc[M_XAM], xam, RLX_WG);
      arrive(&misc[M_CE], lane);
    }
  }
  L.c01 = f32x2{bf2f(cw[0] & 0xffffu) * NLOG2E, __uint_as_float(cw[0] & 0xffff0000u) * NLOG2E};
  L.c23 = f32x2{bf2f(cw[1] & 0xffffu) * NLOG2E, __uint_as_float(cw[1] & 0xffff0000u) * NLOG2E};
  TS(3);

  auto thread_id = [&]() {
    int v = wv * 64 + (int)__builtin_amdgcn_mbcnt_hi(~0u, __builtin_amdgcn_mbcnt_lo(~0u, 0u));
    asm volatile("" : "+v"(v));
    return v;
  };
  const int tb = thread_id(), lb_ = tb & 63;
  const __amdgpu_buffer_rsrc_t tsrd = __builtin_amdgcn_make_buffer_rsrc((void*)(ws + WS_TAB + (size_t)half * NMAX * 128), (short)0, N * 128, 0x00020000);
  uint32_t sg[3] = {0, 0, 0};
  auto poll_segments = [&](int g) {
    gu64* gs = (gu64*)(ws + WS_SEG) + g * NB + b;
    const bool third = g + 192 < NCH;
    for (unsigned spins = 0;; spins++) {
      const u64 v0 = __hip_atomic_load(gs, RLX_AGENT), v1 = __hip_atomic_load(gs + 96 * NB, RLX_AGENT);
      const u64 v2 = __hip_atomic_load(gs + (third ? 192 * NB : 0), RLX_AGENT);
      const bool ok = (v0 >> 24) == tag40 && (v1 >> 24) == tag40 && (v2 >> 24) == tag40;
      sg[0] = ((uint32_t)v0 & 0xfffu) | ((((uint32_t)v0 >> 12) & 0xfffu) << 16);
      sg[1] = ((uint32_t)v1 & 0xfffu) | ((((uint32_t)v1 >> 12) & 0xfffu) << 16);
      sg[2] = third ? ((uint32_t)v2 & 0xfffu) | ((((uint32_t)v2 >> 12) & 0xfffu) << 16) : 0u;
      if (__all(ok)) break;
      if (spins > SPIN_CAP) { sg[0] = sg[1] = sg[2] = 0; break; }
      __builtin_amdgcn_s_sleep(12);
    }
  };
  Recs R;
  int done = 0;
  uint32_t rk[6][2]; bool part1[3] = {false, false, false};
  auto post_total = [&](int dn, int j, int l) {
    uint32_t v = 0;
    if (j == 0) v = min(max((int)(sg[0] >> 16) - dn, 0), 64) + min(max((int)(sg[1] >> 16) - dn, 0), 64) + min(max((int)(sg[2] >> 16) - dn, 0), 64);
    v = wave_scan_add(v);
    if (l == 63) __hip_atomic_fetch_add(&misc[M_TOT], v, RLX_WG);
  };
  auto rank = [&](int tt, int dn, int take) {
    const int j = tt & 7;
    const uint32_t spare = (196u + (uint32_t)(tt & 63) % 60u) * 0x01010101u;
    bool more = false;
#pragma unroll
    for (int i = 0; i < 6; i++) {
      const int len = (int)(sg[i >> 1] >> 16) - dn;
      more |= len > take;
      const int v = min(max(min(len, take) - 32 * (i & 1) - 4 * j, 0), 4);
      const uint32_t drop = (uint32_t)(0xffffffffull << (8 * v));
      R.lc[i] = (R.lc[i] & ~drop) | (spare & drop);
      if (i & 1) part1[i >> 1] = __any(v > 0);
    }
    uint32_t r[3][4];
#pragma unroll
    for (int st = 0; st < 3; st++)
#pragma unroll
      for (int k = 0; k < 4; k++) r[st][k] = __hip_atomic_fetch_add((LDSP(uint32_t))(L_CNT + byte_x4(R.lc[2 * st], k)), 1u, RLX_WG);
#pragma unroll
    for (int st = 0; st < 3; st++) {
      rk[2 * st][0] = r[st][0] | (r[st][1] << 16); rk[2 * st][1] = r[st][2] | (r[st][3] << 16);
      asm volatile("" : "+v"(rk[2 * st][0]), "+v"(rk[2 * st][1]));
      rk[2 * st + 1][0] = rk[2 * st + 1][1] = 0;
      if (part1[st]) {
        uint32_t q[4];
#pragma unroll
        for (int k = 0; k < 4; k++) q[k] = __hip_atomic_fetch_add((LDSP(uint32_t))(L_CNT + byte_x4(R.lc[2 * st + 1], k)), 1u, RLX_WG);
        rk[2 * st + 1][0] = q[0] | (q[1] << 16); rk[2 * st + 1][1] = q[2] | (q[3] << 16);
      }
    }
    if (__any(more) && (tt & 63) == 0) misc[M_MORE] = 1;
  };
  auto scan = [&](int ln_) {
    u32x4 c = *(const u32x4*)(smem + L_CNT + ln_ * 16);
    if (ln_ >= NPB / 4) c = u32x4{0, 0, 0, 0};
    const u32x4 p = (c + 3) & ~3u;
    const uint32_t s1 = p[0] + p[1], s2 = s1 + p[2], tot = s2 + p[3];
    const uint32_t inc = wave_scan_add(tot), ex = inc - tot;
    *(u32x4*)(smem + L_OFF + ln_ * 16) = ln_ < NPB / 4 ? u32x4{ex, ex + p[0], ex + s1, ex + s2} : u32x4{SORT_CAP, SORT_CAP, SORT_CAP, SORT_CAP};
    const uint32_t T4 = (uint32_t)__builtin_amdgcn_readlane((int)inc, 63);
    const uint32_t T32 = (T4 + 31) & ~31u;
    if ((T4 >> 2) + ln_ < (T32 >> 2) + 2) *(uint32_t*)(smem + L_QN + ((T4 >> 2) + ln_) * 4) = QN_DUMMY;
    if (ln_ == 0) { misc[M_NSTEP] = T32 >> 5; misc[M_T4] = T4; }
  };
  auto scatter = [&](int tt) {
    if (wv < 12) {
#pragma unroll
      for (int i = 0; i < 6; i++) {
        if ((i & 1) == 0 || part1[i >> 1]) {
#pragma unroll
          for (int k = 0; k < 4; k++) {
            const uint32_t pos = min(*(LDSP(const uint32_t))(L_OFF + byte_x4(R.lc[i], k)) + ((rk[i][k >> 1] >> (16 * (k & 1))) & 0xffffu), (uint32_t)(SORT_CAP - 1));
            const uint32_t rec = R.rc[i][k];
            *(LDSP(u32x2))(lb + L_SORT + pos * 8) = u32x2{lo_x128(rec), rec & 0xffff0000u};
          }
        }
      }
    }
    u32x2 dummy = {(uint32_t)tt, 0u};
    asm volatile("" : "+v"(dummy));
    dummy = u32x2{dummy[0] * 0u + OFF_DUMMY, 0u};
    const int u = 831 - tt;
    if (u >= 0 && u < NPB * 4) {
      const uint32_t nd = u >> 2, k = u & 3;
      const uint32_t c = *(const uint32_t*)(smem + L_CNT + nd * 4), o = *(const uint32_t*)(smem + L_OFF + nd * 4);
      const uint32_t val = nd * (128u + (AGG_ROW << 16));
      for (uint32_t q = k; 4 * q < c; q += 4) *(uint32_t*)(smem + L_QN + o + q * 4) = val;
      if (k == 3) *(uint32_t*)(smem + L_NQ + nd * 4) += (c + 3) >> 2;
      if (k < 3 && c + k < ((c + 3) & ~3u)) *(u32x2*)(smem + L_SORT + (o + c + k) * 8) = dummy;
    }
    if (wv == 12) {
      const uint32_t T4 = misc[M_T4], T32 = (T4 + 31) & ~31u, i = T4 + (tt & 63);
      if (i < T32 + 8) *(u32x2*)(smem + L_SORT + i * 8) = dummy;
    }
  };
  auto lane_consts = [&](int tt, int nstep) {
    const int n_ = (nstep + 15 - wv) >> 4, first = 8 * (wv * (nstep >> 4) + min(wv, nstep & 15));
    const int g = (tt & 63) >> 3, j = tt & 7;
    L.rec = lb + L_SORT + (first + g * n_) * 32;
    L.qn  = lb + L_QN + (first + g * n_) * 4;
    L.c16 = j * 16;
    L.ea  = lb + L_EA + j * 16;
    L.agg = lb + L_AGG + j * 4;
    asm volatile("" : "+v"(L.rec), "+v"(L.qn), "+v"(L.ea), "+v"(L.agg));
    return n_;
  };

  int take = 64;
  bool fast = false; float delta = 0.f;
  if (wv <= 12) {
    if (wv < 12) {
      await_lds(&misc[M_CE], 7u);
      poll_segments(tb >> 3);
      TS(4);
      load_recs(R, sg, ws, tb >> 3, tb & 7, 0);
      post_total(0, tb & 7, lb_);
      uint32_t v = (tb & 7) == 0 ? (sg[0] >> 16) + (sg[1] >> 16) + (sg[2] >> 16) : 0u;
      v = wave_scan_add(v);
      if (lb_ == 63) __hip_atomic_fetch_add(&misc[M_ALL], v, RLX_WG);
      arrive(&misc[M_CP], lb_);
      WAIT_RECS(R);
      arrive(&misc[M_CR], lb_);
      TS(5);
      rank(tb, 0, take);
    } else {
      await_lds(&misc[M_CB], 3u);
      if (lb_ == 0) {
        const u32x4 m = *(const u32x4*)(misc + M_MX), q = *(const u32x4*)(misc + M_MX + 4);
        const float sq = (__uint_as_float(q[1]) + __uint_as_float(q[2])) + __uint_as_float(q[3]);
        gu64* g = (gu64*)(ws + WS_TBG) + 2 * w;
        __hip_atomic_store(g,     (u64)(min((m[0] + 0xffffu) >> 16, 0x7fffu) | (min((m[1] + 0xffffu) >> 16, 0x7fffu) << 16)) | ((u64)tag32 << 32), RLX_AGENT);
        __hip_atomic_store(g + 1, (u64)__float_as_uint(sq) | ((u64)tag32 << 32), RLX_AGENT);
      }
    }
    group_barrier(lb_);
    if (w == 0 && tb == 0) __hip_atomic_store((gu64*)(ws + WS_NONCE), (((u64)xn[1] << 32) | xn[0]) + 1ull, RLX_AGENT);
    if (__builtin_amdgcn_readfirstlane(misc[M_TOT]) > (uint32_t)TAKE_MAX) {
      group_barrier(lb_);
      if (tb < 256) *(uint32_t*)(smem + L_CNT + tb * 4) = 0;
      group_barrier(lb_);
      take = 16;
      if (wv < 12) rank(tb, 0, take);
      group_barrier(lb_);
    }
    if (wv == 12) scan(lb_);
    else {
      for (int i = tb; i < 197 * AGG_ROW / 16; i += 768) *(u32x4*)(smem + L_AGG + i * 16) = u32x4{0, 0, 0, 0};
    }
    await_lds(&misc[M_CB], 3u);
    group_barrier(lb_);
    TS(6);
    scatter(tb);
    if (wv == 12) {
      gu64* gr = (gu64*)(ws + WS_TRG) + lb_;
      for (unsigned spins = 0; spins < SPIN_CAP; spins++) {
        bool ok = true;
#pragma unroll
        for (int i = 0; i < 4; i++) ok &= (uint32_t)(__hip_atomic_load(gr + 64 * i, RLX_AGENT) >> 32) == tag32;
        if (__all(ok)) break;
        __builtin_amdgcn_s_sleep(16);
      }
      const uint32_t cm = wave_max_u32(max(max(absbits(L.c01[0]), absbits(L.c01[1])), max(absbits(L.c23[0]), absbits(L.c23[1]))));
      gu64* gt = (gu64*)(ws + WS_TBG) + 2 * lb_;
      gu64* ge = (gu64*)(ws + WS_EMG) + lb_;
      uint32_t mb, mv_, me; float sq;
      for (unsigned spins = 0;; spins++) {
        bool ok = true; mb = mv_ = me = 0; sq = 0.f;
#pragma unroll
        for (int i = 0; i < 4; i++) {
          const u64 v0 = __hip_atomic_load(gt + 128 * i, RLX_AGENT), v1 = __hip_atomic_load(gt + 128 * i + 1, RLX_AGENT), v2 = __hip_atomic_load(ge + 64 * i, RLX_AGENT);
          ok &= (uint32_t)(v0 >> 32) == tag32 && (uint32_t)(v1 >> 32) == tag32 && (uint32_t)(v2 >> 32) == tag32;
          mb = max(mb, (uint32_t)v0 << 16); mv_ = max(mv_, (uint32_t)v0 & 0xffff0000u);
          me = max(me, (uint32_t)v2); sq += __uint_as_float((uint32_t)v1);
        }
        if (__all(ok)) break;
        if (spins > SPIN_CAP) break;
        __builtin_amdgcn_s_sleep(16);
      }
      mb = wave_max_u32(mb); mv_ = wave_max_u32(mv_); me = wave_max_u32(me); sq = wave_sum_f32(sq);
      const float mv = __uint_as_float(mv_);
      const float yb = (__uint_as_float(misc[M_XAM]) + __uint_as_float(mb) + __uint_as_float(me) * __uint_as_float(cm)) * 1.01f;
      const float all = (float)(misc[M_ALL] + 4u);
      const int p1 = (int)(__float_as_uint(4.1f * mv) >> 23) - 127 + 2, p2 = (int)(__float_as_uint(all * mv * 1.01f) >> 23) - 127 + 1 - 8;
      const int p = max(max(p1, p2), -80);
      const float dl = __uint_as_float((uint32_t)(p - 23 + 127) << 23);
      const bool fine = dl * 4096.f <= __builtin_amdgcn_sqrtf(sq * __builtin_amdgcn_rcpf(64.f * (float)N)) * 0.99f || mv == 0.f;
      if (lb_ == 63) {
        misc[M_FAST] = (yb <= 28.f) && (mv <= 1.0e12f) && fine;
        misc[M_EXP] = (uint32_t)(p + 127);
      }
    }
  }
  LDS_BARRIER();
  if (__builtin_amdgcn_readfirstlane(misc[M_FAST])) {
    for (int i = tb; i < NPB * 8; i += 1024) {
      f32x4 v = *(const f32x4*)(smem + L_EA + i * 16);
      v[0] = __builtin_amdgcn_exp2f(v[0]); v[1] = __builtin_amdgcn_exp2f(v[1]); v[2] = __builtin_amdgcn_exp2f(v[2]); v[3] = __builtin_amdgcn_exp2f(v[3]);
      *(f32x4*)(smem + L_EA + i * 16) = v;
    }
  }
  LDS_BARRIER();
  fast = __builtin_amdgcn_readfirstlane(misc[M_FAST]);
  take = __builtin_amdgcn_readfirstlane(misc[M_TOT]) > (uint32_t)TAKE_MAX ? 16 : 64;
  {
    const uint32_t pexp = __builtin_amdgcn_readfirstlane(misc[M_EXP]);
    delta = __uint_as_float((pexp - 23u) << 23);
    const float mg = __uint_as_float((pexp << 23) | 0x400000u);
    L.magic = f32x2{mg, mg};
  }
  int again = __builtin_amdgcn_readfirstlane(misc[M_MORE]);
  int nstep = __builtin_amdgcn_readfirstlane(misc[M_NSTEP]);
  TS(7);
  {
    const int n_ = lane_consts(tb, nstep);
    if (fast) edge_loop<0>(L, n_, tsrd);
    else      edge_loop<2>(L, n_, tsrd);
  }
  while (again) {
    done += take;
    const int t2 = thread_id();
    if (wv < 12) { poll_segments(t2 >> 3); load_recs(R, sg, ws, t2 >> 3, t2 & 7, done); WAIT_RECS(R); }
    LDS_BARRIER();
    if (t2 < 256) *(uint32_t*)(smem + L_CNT + t2 * 4) = 0;
    if (t2 == 0) { misc[M_MORE] = 0; misc[M_TOT] = 0; }
    LDS_BARRIER();
    if (wv < 12) post_total(done, t2 & 7, t2 & 63);
    LDS_BARRIER();
    take = __builtin_amdgcn_readfirstlane(misc[M_TOT]) <= (uint32_t)TAKE_MAX ? 64 : 16;
    if (wv < 12) rank(t2, done, take);
    LDS_BARRIER();
    if (wv == 12) scan(t2 & 63);
    LDS_BARRIER();
    scatter(t2);
    again = __builtin_amdgcn_readfirstlane(misc[M_MORE]);
    LDS_BARRIER();
    nstep = __builtin_amdgcn_readfirstlane(misc[M_NSTEP]);
    const int n_ = lane_consts(t2, nstep);
    if (fast) edge_loop<0>(L, n_, tsrd);
    else      edge_loop<2>(L, n_, tsrd);
  }
  LDS_BARRIER();

  const int te = thread_id();
  if (te < NPB * 4) {
    const int nl = te >> 2, u = te & 3, node = b * NPB + nl;
    if (node < N) {
      float y[8];
      const uint32_t bias = *(const uint32_t*)(smem + L_NQ + nl * 4) * __float_as_uint(L.magic[0]);
      const f32x4 u0 = *(const f32x4*)(smem + L_XU + nl * 128 + u * 32), u1 = *(const f32x4*)(smem + L_XU + nl * 128 + u * 32 + 16);
#pragma unroll
      for (int i = 0; i < 4; i++) {
        const u32x2 v2 = *(const u32x2*)(smem + L_AGG + nl * AGG_ROW + (8 * i + 2 * u) * 4);
#pragma unroll
        for (int j = 0; j < 2; j++) {
          const float agg = fast ? (float)(int)(v2[j] - bias) * delta : __uint_as_float(v2[j]);
          const float v = (j ? u1[i] : u0[i]) + agg;
          y[4 * j + i] = v * __builtin_amdgcn_rcpf(1.f + __builtin_amdgcn_exp2f(v * NLOG2E));
        }
      }
      const u32x4 o = {cvtpk(y[0], y[1]), cvtpk(y[2], y[3]), cvtpk(y[4], y[5]), cvtpk(y[6], y[7])};
      *(u32x4*)(out + (size_t)node * 64 + 32 * half + 8 * u) = o;
    }
  }
  TS_OUT(256 + blockIdx.x);
}

}

extern "C" void kernel_launch(void* const* d_in, const int* in_sizes, int n_in,
                              void* d_out, int out_size, void* d_ws, size_t ws_size,
                              hipStream_t stream) {
  const uint16_t* x  = (const uint16_t*)d_in[0];
  const uint16_t* ea = (const uint16_t*)d_in[1];
  const int*      src = (const int*)d_in[2];
  const int*      dst = (const int*)d_in[3];
  const uint16_t* uw = (const uint16_t*)d_in[4];
  const uint16_t* vw = (const uint16_t*)d_in[5];
  const uint16_t* gw = (const uint16_t*)d_in[6];
  const int N = in_sizes[0] / 64, E = in_sizes[2];
  char* ws = (char*)d_ws;
  (void)hipFuncSetAttribute((const void*)k_mpnn, hipFuncAttributeMaxDynamicSharedMemorySize, L_TOTAL);
  k_mpnn<<<NCH, 1024, L_TOTAL, stream>>>(src, dst, ea, x, uw, vw, gw, ws, (uint16_t*)d_out, N, E);
}
```

```cpp
#include <hip/hip_runtime.h>
#include <hip/hip_bf16.h>
#include <stdint.h>

#pragma clang diagnostic ignored "-Wint-to-pointer-cast"
#pragma clang diagnostic ignored "-Wpointer-to-int-cast"
namespace {

typedef float    f32x2  __attribute__((ext_vector_type(2)));
typedef float    f32x4  __attribute__((ext_vector_type(4)));
typedef float    f32x16 __attribute__((ext_vector_type(16)));
typedef short    bf16x8 __attribute__((ext_vector_type(8)));
typedef short    s16x4  __attribute__((ext_vector_type(4)));
typedef uint32_t u32x2  __attribute__((ext_vector_type(2)));
typedef uint32_t u32x4  __attribute__((ext_vector_type(4)));
typedef unsigned long long u64;
typedef __attribute__((address_space(1))) unsigned long long gu64;
#define LDSP(T) __attribute__((address_space(3))) T*
#define DI __device__ __forceinline__
#define RLX_AGENT __ATOMIC_RELAXED, __HIP_MEMORY_SCOPE_AGENT
#define RLX_WG    __ATOMIC_RELAXED, __HIP_MEMORY_SCOPE_WORKGROUP

constexpr int   NCH = 256;
constexpr int   NB  = 128;
constexpr int   NPB = 196;
constexpr int   CH  = 3128;
constexpr int   CHB = 880;
constexpr int   NMAX = NB * NPB;
constexpr int   NTILE = NMAX / 32;
constexpr float NLOG2E = -1.4426950408889634f;
constexpr int   AUX_SC1 = 16;
constexpr unsigned SPIN_CAP = 1u << 18;
#define POLL_DELAY 16
static_assert(4 * CHB >= CH + 3 * NB && (CHB * 20) % 16 == 0, "blocks");

constexpr size_t WS_NONCE = 0;
constexpr size_t WS_SEG = 128;
constexpr size_t WS_TBG = WS_SEG + (size_t)NCH * NB * 8;
constexpr size_t WS_EMG = WS_TBG + (size_t)NCH * 16;
constexpr size_t WS_TRG = WS_EMG + (size_t)NCH * 8;
constexpr size_t WS_REC = WS_TRG + (size_t)NCH * 8;
constexpr size_t WS_TAB = (WS_REC + (size_t)NCH * CHB * 20 + 1024 + 127) & ~(size_t)127;
constexpr size_t WS_DBG = WS_TAB + (size_t)2 * NMAX * 128;
static_assert(WS_REC % 128 == 0 && WS_TAB % 128 == 0, "ws align");

constexpr int AGG_ROW  = 160;
constexpr int SORT_CAP = 8128;
constexpr int TAKE_MAX = SORT_CAP - 3 * NPB - 48;
constexpr int L_CNT    = 0;
constexpr int L_OFF    = L_CNT + 1024;
constexpr int L_NQ     = L_OFF + 1024;
constexpr int L_MISC   = L_NQ + 1024;
constexpr int L_QN     = L_MISC + 512;
constexpr int L_EA     = L_QN + SORT_CAP;
constexpr int L_XU     = L_EA + 197 * 128;
constexpr int L_AGG    = (L_XU + 196 * 128 + 127) & ~127;
constexpr int L_SORT   = (L_AGG + 197 * AGG_ROW + 127) & ~127;
constexpr int L_TOTAL  = L_SORT + SORT_CAP * 8;
static_assert(L_TOTAL <= 163840, "LDS");
static_assert((L_QN & 15) == 0 && (L_EA & 15) == 0 && (L_XU & 15) == 0 && TAKE_MAX >= 4096, "align");
constexpr int A_HIST = L_SORT;
constexpr int A_BASE = A_HIST + 512;
constexpr int A_ST   = A_BASE + 512;
constexpr int A_W    = (A_ST + CHB * 20 + 127) & ~127;
constexpr int A_IMG  = A_W + 16384;
constexpr int P_W    = A_IMG + 16384;
constexpr int P_X    = L_AGG;
static_assert(P_W + 8192 <= L_TOTAL && P_X + 224 * 128 <= L_SORT, "phase A images");
constexpr uint32_t QN_DUMMY = (196u * 128u) | ((196u * AGG_ROW) << 16);
constexpr uint32_t OFF_DUMMY = 0x7fffff00u;
constexpr int M_MORE = 0, M_NSTEP = 1, M_T4 = 2;
constexpr int M_CW = 3, M_CB = 4, M_CT = 5, M_PUB = 6;
constexpr int M_CL = 7, M_CE = 8, M_CP = 9;
constexpr int M_TOT = 10, M_ALL = 11, M_XAM = 12;
constexpr int M_FAST = 13, M_EXP = 14, M_GB = 15, M_TAG0 = 24, M_TAG1 = 25, M_TGF = 26;
constexpr int M_MX = 16;

DI float    bf2f(uint32_t v16) { return __uint_as_float(v16 << 16); }
DI uint32_t cvtpk(float lo, float hi) {
  typedef __bf16 bf16x2 __attribute__((ext_vector_type(2)));
  return __builtin_bit_cast(uint32_t, __builtin_convertvector(f32x2{lo, hi}, bf16x2));
}
DI f32x16   mfma(bf16x8 a, bf16x8 b, f32x16 c) { return __builtin_amdgcn_mfma_f32_32x32x16_bf16(a, b, c, 0, 0, 0); }
DI int      crow(int r, int h) { return (r & 3) + 8 * (r >> 2) + 4 * h; }
DI uint32_t absbits(float v) { return __float_as_uint(v) & 0x7fffffffu; }

template <int CTRL, int RM> DI uint32_t dpp0(uint32_t v) { return (uint32_t)__builtin_amdgcn_update_dpp(0, (int)v, CTRL, RM, 0xf, false); }
DI uint32_t wave_scan_add(uint32_t v) {
  v += dpp0<0x111, 0xf>(v); v += dpp0<0x112, 0xf>(v); v += dpp0<0x114, 0xf>(v); v += dpp0<0x118, 0xf>(v);
  v += dpp0<0x142, 0xa>(v); v += dpp0<0x143, 0xc>(v);
  return v;
}
DI uint32_t wave_max_u32(uint32_t v) {
  v = max(v, dpp0<0x111, 0xf>(v)); v = max(v, dpp0<0x112, 0xf>(v)); v = max(v, dpp0<0x114, 0xf>(v)); v = max(v, dpp0<0x118, 0xf>(v));
  v = max(v, dpp0<0x142, 0xa>(v)); v = max(v, dpp0<0x143, 0xc>(v));
  return v;
}
DI float wave_sum_f32(float s) {
  s += __uint_as_float(dpp0<0x111, 0xf>(__float_as_uint(s))); s += __uint_as_float(dpp0<0x112, 0xf>(__float_as_uint(s)));
  s += __uint_as_float(dpp0<0x114, 0xf>(__float_as_uint(s))); s += __uint_as_float(dpp0<0x118, 0xf>(__float_as_uint(s)));
  s += __uint_as_float(dpp0<0x142, 0xa>(__float_as_uint(s))); s += __uint_as_float(dpp0<0x143, 0xc>(__float_as_uint(s)));
  return s;
}
#define LDS_BARRIER() asm volatile("s_waitcnt lgkmcnt(0)\n\ts_barrier" ::: "memory")
#define WAIT_VM(N) asm volatile("s_waitcnt vmcnt(" #N ")" ::: "memory")

DI uint32_t arrive(uint32_t* word, int lane) {
  asm volatile("s_waitcnt lgkmcnt(0)" ::: "memory");
  uint32_t v = 0;
  if (lane == 0) v = __hip_atomic_fetch_add(word, 1u, RLX_WG);
  return __builtin_amdgcn_readfirstlane(v);
}
DI void await_lds(uint32_t* word, uint32_t want) {
  for (unsigned spins = 0; spins < SPIN_CAP; spins++) {
    if (__builtin_amdgcn_readfirstlane(__hip_atomic_load(word, RLX_WG)) >= want) break;
    __builtin_amdgcn_s_sleep(1);
  }
  asm volatile("" ::: "memory");
}

DI void dma16(uint32_t ldsaddr, uint32_t voff, __amdgpu_buffer_rsrc_t srd) {
  asm volatile("s_mov_b32 m0, %0\n\ts_nop 0\n\tbuffer_load_dwordx4 %1, %2, 0 offen lds" :: "s"(ldsaddr), "v"(voff), "s"(srd) : "memory");
}
DI bf16x8 tr_frag(uint32_t addr, int four_rows) {
  const s16x4 lo = __builtin_amdgcn_ds_read_tr16_b64_v4i16((LDSP(s16x4))addr);
  const s16x4 hi = __builtin_amdgcn_ds_read_tr16_b64_v4i16((LDSP(s16x4))(addr + four_rows));
  return (bf16x8)__builtin_shufflevector(lo, hi, 0, 1, 2, 3, 4, 5, 6, 7);
}

#define TS(i) do { } while (0)
#define TS_DECL do { } while (0)
#define TS_OUT(slot) do { } while (0)
#define TS_PUB(i) do { } while (0)

DI uint32_t shape(float v, uint32_t low) {
  const uint32_t b = __float_as_uint(v);
  const uint32_t t = min(__builtin_elementwise_sub_sat((b & 0x7fffffffu) + 0x8000u, low), 0x7fffffffu);
  return (b & 0x80000000u) | (t & 0xffff0000u) | low;
}
DI void table_half(u32x4* o, const bf16x8* xf, uint32_t troff, int c, float& mb, float& mv, float& s2) {
  f32x16 zb, hv;
#pragma unroll
  for (int i = 0; i < 16; i++) { zb[i] = 0.f; hv[i] = 0.f; }
#pragma unroll
  for (int s = 0; s < 4; s++) {
    zb = mfma(tr_frag(troff + s * 2048 + c * 64, 512), xf[s], zb);
    hv = mfma(tr_frag(troff + 8192 + s * 2048 + c * 64, 512), xf[s], hv);
  }
#pragma unroll
  for (int q = 0; q < 4; q++) {
#pragma unroll
    for (int p = 0; p < 2; p++) {
      const int i = 4 * q + 2 * p;
      const float b0 = zb[i] * NLOG2E, b1 = zb[i + 1] * NLOG2E, v0 = hv[i], v1 = hv[i + 1];
      mb = fmaxf(mb, fmaxf(fabsf(b0), fabsf(b1))); mv = fmaxf(mv, fmaxf(fabsf(v0), fabsf(v1)));
      s2 = fmaf(v0, v0, s2); s2 = fmaf(v1, v1, s2);
      const uint32_t d0 = shape(b0, cvtpk(v0, 0.f));
      const uint32_t w1 = shape(v1, d0 >> 16);
      o[q][2 * p] = d0; o[q][2 * p + 1] = shape(b1, w1 >> 16);
    }
  }
}
DI void table_image(const u32x4* o, uint32_t img, int lane) {
  const int n31 = lane & 31, h = lane >> 5;
#pragma unroll
  for (int q = 0; q < 4; q++) *(LDSP(u32x4))(img + n31 * 128 + (((2 * q + h) ^ (n31 & 7)) * 16)) = o[q];
  asm volatile("s_waitcnt lgkmcnt(0)" ::: "memory");
}
DI void table_rows(u32x4* r, uint32_t img, int lane) {
#pragma unroll
  for (int i = 0; i < 4; i++) { const int row = 8 * i + (lane >> 3); r[i] = *(LDSP(const u32x4))(img + row * 128 + (((lane & 7) ^ (row & 7)) * 16)); }
  asm volatile("s_waitcnt lgkmcnt(0)" : "+v"(r[0]), "+v"(r[1]), "+v"(r[2]), "+v"(r[3]) :: "memory");
}
DI void table_store(const u32x4* r, int tile, int c, __amdgpu_buffer_rsrc_t tsrd, int lane) {
#pragma unroll
  for (int i = 0; i < 4; i++)
    __builtin_amdgcn_raw_buffer_store_b128(r[i], tsrd, (c * NMAX + tile * 32 + 8 * i + (lane >> 3)) * 128 + (lane & 7) * 16, 0, AUX_SC1);
}

DI f32x2 exp2x2(f32x2 v) { f32x2 r = {__builtin_amdgcn_exp2f(v[0]), __builtin_amdgcn_exp2f(v[1])}; return r; }
DI f32x2 rcpx2(f32x2 v) { f32x2 r = {__builtin_amdgcn_rcpf(v[0]), __builtin_amdgcn_rcpf(v[1])}; return r; }
DI f32x2 fma2(f32x2 a, f32x2 b, f32x2 c) { return __builtin_elementwise_fma(a, b, c); }
template <int MUL> DI uint32_t madlo_i(uint32_t v, uint32_t add) {
  uint32_t r; asm("v_mad_u32_u16 %0, %1, %3, %2" : "=v"(r) : "v"(v), "v"(add), "i"(MUL)); return r;
}
template <int MUL> DI uint32_t madhi_i(uint32_t v, uint32_t add) {
  uint32_t r; asm("v_mad_u32_u16 %0, %1, %3, %2 op_sel:[1,0,0,0]" : "=v"(r) : "v"(v), "v"(add), "i"(MUL)); return r;
}
template <int K> DI uint32_t byte_x4(uint32_t v) {
  uint32_t r;
  if (K == 0) asm("v_lshlrev_b32_sdwa %0, 2, %1 dst_sel:DWORD dst_unused:UNUSED_PAD src0_sel:DWORD src1_sel:BYTE_0" : "=v"(r) : "v"(v));
  if (K == 1) asm("v_lshlrev_b32_sdwa %0, 2, %1 dst_sel:DWORD dst_unused:UNUSED_PAD src0_sel:DWORD src1_sel:BYTE_1" : "=v"(r) : "v"(v));
  if (K == 2) asm("v_lshlrev_b32_sdwa %0, 2, %1 dst_sel:DWORD dst_unused:UNUSED_PAD src0_sel:DWORD src1_sel:BYTE_2" : "=v"(r) : "v"(v));
  if (K == 3) asm("v_lshlrev_b32_sdwa %0, 2, %1 dst_sel:DWORD dst_unused:UNUSED_PAD src0_sel:DWORD src1_sel:BYTE_3" : "=v"(r) : "v"(v));
  return r;
}
DI uint32_t byte_x4(uint32_t v, int k) { return k == 0 ? byte_x4<0>(v) : (k == 1 ? byte_x4<1>(v) : (k == 2 ? byte_x4<2>(v) : byte_x4<3>(v))); }
DI uint32_t lo_x128(uint32_t v) { uint32_t r; asm("v_mad_u32_u16 %0, %1, %2, 0" : "=v"(r) : "v"(v), "s"(128u)); return r; }
DI void lds_add_u32(uint32_t addr, float v) { __hip_atomic_fetch_add((LDSP(uint32_t))addr, __float_as_uint(v), RLX_WG); }
DI void lds_add_f32(uint32_t addr, float v) { __hip_atomic_fetch_add((LDSP(float))addr, v, RLX_WG); }

struct Lane {
  uint32_t rec;
  uint32_t qn;
  uint32_t c16;
  uint32_t ea, agg;
  f32x2 c01, c23;
  f32x2 magic;
};
struct Quad {
  u32x4 d[4];
  u32x4 r0, r1;
  uint32_t q;
};
template <int S> DI void fetch(Quad& Q, const Lane& L, __amdgpu_buffer_rsrc_t srd) {
  Q.r0 = *(LDSP(const u32x4))(L.rec + S * 32);
  Q.r1 = *(LDSP(const u32x4))(L.rec + S * 32 + 16);
  Q.q  = *(LDSP(const uint32_t))(L.qn + S * 4);
  Q.d[0] = __builtin_amdgcn_raw_buffer_load_b128(srd, Q.r0[0] + L.c16, 0, AUX_SC1);
  Q.d[1] = __builtin_amdgcn_raw_buffer_load_b128(srd, Q.r0[2] + L.c16, 0, AUX_SC1);
  Q.d[2] = __builtin_amdgcn_raw_buffer_load_b128(srd, Q.r1[0] + L.c16, 0, AUX_SC1);
  Q.d[3] = __builtin_amdgcn_raw_buffer_load_b128(srd, Q.r1[2] + L.c16, 0, AUX_SC1);
  asm volatile("" ::: "memory");
}
DI f32x2 fma_e(u32x2 rec, f32x2 c, u32x2 xb) {
  f32x2 r; asm("v_pk_fma_f32 %0, %1, %2, %3 op_sel:[1,0,0] op_sel_hi:[1,1,1]" : "=v"(r) : "v"(rec), "v"(c), "v"(xb)); return r;
}
DI f32x2 vpair(u32x2 d) {
  f32x2 r; asm("v_lshlrev_b64 %0, 16, %1" : "=v"(r) : "v"(d)); return r;
}
template <int MODE> DI void gate(const Quad& Q, const Lane& L) {
  const f32x4 ea = *(LDSP(const f32x4))(madlo_i<1>(Q.q, L.ea));
  const uint32_t ga = madhi_i<1>(Q.q, L.agg);
  const u32x2 rec[4] = {u32x2{Q.r0[0], Q.r0[1]}, u32x2{Q.r0[2], Q.r0[3]}, u32x2{Q.r1[0], Q.r1[1]}, u32x2{Q.r1[2], Q.r1[3]}};
  const f32x2 one = {1.f, 1.f};
#pragma unroll
  for (int p = 0; p < 2; p++) {
    const f32x2 c = p ? L.c23 : L.c01, eap = {ea[2 * p], ea[2 * p + 1]};
    f32x2 a[4], hv[4];
#pragma unroll
    for (int k = 0; k < 4; k++) {
      const u32x2 dd = {Q.d[k][2 * p], Q.d[k][2 * p + 1]};
      hv[k] = vpair(dd);
      const f32x2 y = fma_e(rec[k], c, dd);
      a[k] = MODE == 2 ? one + exp2x2(y + eap) : fma2(eap, exp2x2(y), one);
    }
    if (MODE == 0) {
      const f32x2 d01 = a[0] * a[1], n01 = fma2(hv[1], a[0], hv[0] * a[1]);
      const f32x2 d23 = a[2] * a[3], n23 = fma2(hv[3], a[2], hv[2] * a[3]);
      const f32x2 m = fma2(fma2(n23, d01, n01 * d23), rcpx2(d01 * d23), L.magic);
      lds_add_u32(ga + 64 * p, m[0]); lds_add_u32(ga + 64 * p + 32, m[1]);
    } else {
      const f32x2 m = (hv[0] * rcpx2(a[0]) + hv[1] * rcpx2(a[1])) + (hv[2] * rcpx2(a[2]) + hv[3] * rcpx2(a[3]));
      lds_add_f32(ga + 64 * p, m[0]); lds_add_f32(ga + 64 * p + 32, m[1]);
    }
  }
}
template <int MODE> DI void edge_loop(Lane& L, int n, __amdgpu_buffer_rsrc_t srd) {
  if (n <= 0) return;
  Quad A, B;
  fetch<0>(A, L, srd);
  while (true) {
    fetch<1>(B, L, srd);
    gate<MODE>(A, L);
    if (--n == 0) break;
    fetch<2>(A, L, srd);
    gate<MODE>(B, L);
    if (--n == 0) break;
    L.rec += 64; L.qn += 8;
  }
}

struct Recs { u32x4 rc[6]; uint32_t lc[6]; };
DI void load_recs(Recs& R, const uint32_t* sg, const char* ws, int g, int j, int done) {
  const __amdgpu_buffer_rsrc_t rsrd = __builtin_amdgcn_make_buffer_rsrc((void*)(ws + WS_REC), (short)0, NCH * CHB * 20, 0x00020000);
#pragma unroll
  for (int p = 0; p < 2; p++) {
#pragma unroll
    for (int r = 0; r < 3; r++) {
      uint32_t off = (uint32_t)((g + 96 * r) * (CHB * 20) + ((int)(sg[r] & 0xffff) + (done >> 2) + 8 * p + j) * 20);
      if ((int)(sg[r] >> 16) - done <= 32 * p + 4 * j) off = 0x40000000u;
      asm volatile("buffer_load_dwordx4 %0, %1, %2, 0 offen sc1" : "=&v"(R.rc[2 * r + p]) : "v"(off), "s"(rsrd) : "memory");
      asm volatile("buffer_load_dword %0, %1, %2, 0 offen offset:16 sc1" : "=&v"(R.lc[2 * r + p]) : "v"(off), "s"(rsrd) : "memory");
    }
  }
}
#define WAIT_RECS(R) asm volatile("s_waitcnt vmcnt(0)" : "+v"(R.rc[0]), "+v"(R.rc[1]), "+v"(R.rc[2]), "+v"(R.rc[3]), "+v"(R.rc[4]), "+v"(R.rc[5]), \
                                  "+v"(R.lc[0]), "+v"(R.lc[1]), "+v"(R.lc[2]), "+v"(R.lc[3]), "+v"(R.lc[4]), "+v"(R.lc[5]) :: "memory")

__global__ __launch_bounds__(1024) void k_mpnn(const int* __restrict__ src, const int* __restrict__ dst,
                                               const uint16_t* __restrict__ ea, const uint16_t* __restrict__ x,
                                               const uint16_t* __restrict__ uw, const uint16_t* __restrict__ vw,
                                               const uint16_t* __restrict__ gw, char* __restrict__ ws,
                                               uint16_t* __restrict__ out, int N, int E) {
  extern __shared__ __attribute__((aligned(128))) char smem[];
  const uint32_t lb = (uint32_t)(uintptr_t)(LDSP(char))smem;
  uint32_t* misc = (uint32_t*)(smem + L_MISC);
  const int t = threadIdx.x, lane = t & 63;
  const int w = blockIdx.x;
  const int half = (blockIdx.x & 7) >> 2, b = (blockIdx.x & 3) * 32 + (blockIdx.x >> 3);
  const int wv = __builtin_amdgcn_readfirstlane(t >> 6);
  const int r31 = lane & 31, h = lane >> 5;
  TS_DECL; TS(0);

  uint32_t gb = 0;
  auto group_barrier = [&](int l) { gb += 13; arrive(&misc[M_GB], l); await_lds(&misc[M_GB], gb); };
  const __amdgpu_buffer_rsrc_t xsrd = __builtin_amdgcn_make_buffer_rsrc((void*)x, (short)0, N * 128, 0x00020000);
  const uint32_t cA = (uint32_t)(((lane & 7) ^ (lane >> 4)) * 16);
  u32x2 xn, cw;
#define LOAD_XN_CW(a, b) do { \
    asm volatile("global_load_dwordx2 %0, %1, %2 sc1" : "=&v"(a) : "v"(0), "s"(ws + WS_NONCE) : "memory"); \
    asm volatile("global_load_dwordx2 %0, %1, %2" : "=&v"(b) : "v"((128 * 64 + 32 * half + 4 * (t & 7)) * 2), "s"(gw) : "memory"); } while (0)
  Lane L;
  if (wv >= 13) {
    u32x2 xn1, cw1;
    LOAD_XN_CW(xn1, cw1);
    const uint32_t img = lb + A_IMG + (wv & 3) * 4096;
    auto tile_rows = [&](uint32_t to, int tile) {
#pragma unroll
      for (int i = 0; i < 4; i++) dma16(to + i * 1024, (tile * 32 + 8 * i + (lane >> 3)) * 128 + ((i & 1) ? cA ^ 64u : cA), xsrd);
    };
    const __amdgpu_buffer_rsrc_t bsrd = __builtin_amdgcn_make_buffer_rsrc((void*)(gw + 64 * 64), (short)0, 8192, 0x00020000);
    const __amdgpu_buffer_rsrc_t vsrd = __builtin_amdgcn_make_buffer_rsrc((void*)vw, (short)0, 8192, 0x00020000);
    const int tile = w + 256 * (wv - 13);
    const bool extra = wv == 13 && w + 768 < NTILE;
    LDS_BARRIER();
    asm volatile("s_waitcnt vmcnt(0)" : "+v"(xn1), "+v"(cw1) :: "memory");
    xn = xn1; cw = cw1;
    tile_rows(img, tile);
    if (extra) tile_rows(lb + A_IMG, w + 768);
#pragma unroll
    for (int i = 0; i < 6; i++) {
      const int p = wv - 13 + 3 * i;
      if (p < 8) dma16(lb + A_W + p * 1024, p * 1024 + lane * 16, bsrd);
      else if (p < 16) dma16(lb + A_W + p * 1024, (p - 8) * 1024 + lane * 16, vsrd);
    }
    WAIT_VM(0);
    const uint32_t tag = xn[0] + 1u;
    arrive(&misc[M_CL], lane);
    await_lds(&misc[M_CL], 3u);
    const __amdgpu_buffer_rsrc_t wsrd = __builtin_amdgcn_make_buffer_rsrc((void*)(ws + WS_TAB), (short)0, 2 * NMAX * 128, 0x00020000);
    const uint32_t troff = lb + A_W + (8 * h + ((lane & 15) >> 2)) * 128 + (16 * ((lane >> 4) & 1) + 4 * (lane & 3)) * 2;
    const uint32_t xa = img + r31 * 128 + ((h ^ ((r31 >> 1) & 7)) * 16), xe = xa - img + lb + A_IMG;
    float mb = 0.f, mv = 0.f, s2 = 0.f;
    u32x4 ra[4], rb[4], rc[4], rd[4];
    auto tile_halves = [&](uint32_t x0, u32x4* r0, u32x4* r1) {
      bf16x8 xf[4]; u32x4 o[4];
#pragma unroll
      for (int s = 0; s < 4; s++) xf[s] = *(LDSP(const bf16x8))(x0 ^ (32u * s));
      table_half(o, xf, troff, 0, mb, mv, s2);
      table_image(o, img, lane);
      table_rows(r0, img, lane);
      table_half(o, xf, troff, 1, mb, mv, s2);
      table_image(o, img, lane);
      table_rows(r1, img, lane);
    };
    tile_halves(xa, ra, rb);
    if (extra) tile_halves(xe, rc, rd);
    const uint32_t m0 = wave_max_u32(__float_as_uint(mb)), m1 = wave_max_u32(__float_as_uint(mv));
    s2 = wave_sum_f32(s2);
    if (lane == 63) {
      __hip_atomic_fetch_max(&misc[M_MX], m0, RLX_WG); __hip_atomic_fetch_max(&misc[M_MX + 1], m1, RLX_WG);
      misc[M_MX + 4 + (wv & 3)] = __float_as_uint(s2);
    }
    arrive(&misc[M_CB], lane);
    await_lds(&misc[M_PUB], 1u);
    table_store(ra, tile, 0, wsrd, lane);
    table_store(rb, tile, 1, wsrd, lane);
    if (extra) { table_store(rc, w + 768, 0, wsrd, lane); table_store(rd, w + 768, 1, wsrd, lane); }
    WAIT_VM(0);
    if (arrive(&misc[M_CT], lane) == 2 && lane == 0) { __hip_atomic_store((gu64*)(ws + WS_TRG) + w, (u64)tag << 32, RLX_AGENT); TS_PUB(1); }
  } else {
    const int e0 = w * CH;
    const int n  = min(CH, E - e0);
    const int cnt = min(max(n - 4 * t, 0), 4);
    u32x4 d4, s4; u32x2 e2, xn2, cw2;
    LOAD_XN_CW(xn2, cw2);
    {
      const __amdgpu_buffer_rsrc_t dsrd = __builtin_amdgcn_make_buffer_rsrc((void*)dst, (short)0, E * 4, 0x00020000);
      const __amdgpu_buffer_rsrc_t ssrd = __builtin_amdgcn_make_buffer_rsrc((void*)src, (short)0, E * 4, 0x00020000);
      const __amdgpu_buffer_rsrc_t esrd = __builtin_amdgcn_make_buffer_rsrc((void*)ea, (short)0, (E * 2 + 3) & ~3, 0x00020000);
      const uint32_t eo = cnt > 0 ? (uint32_t)(e0 + 4 * t) : 0x20000000u;
      asm volatile("buffer_load_dwordx4 %0, %1, %2, 0 offen" : "=&v"(d4) : "v"(eo * 4), "s"(dsrd) : "memory");
      asm volatile("buffer_load_dwordx4 %0, %1, %2, 0 offen" : "=&v"(s4) : "v"(eo * 4), "s"(ssrd) : "memory");
      asm volatile("buffer_load_dwordx2 %0, %1, %2, 0 offen" : "=&v"(e2) : "v"(eo * 2), "s"(esrd) : "memory");
    }
    uint32_t* hist = (uint32_t*)(smem + A_HIST);
    uint32_t* base = (uint32_t*)(smem + A_BASE);
    if (t < 128) { hist[t] = 0; misc[t] = 0; }
    if (t >= 128 && t < 640) *(uint32_t*)(smem + (t < 384 ? L_CNT + (t - 128) * 4 : L_NQ + (t - 384) * 4)) = 0;
    if (t >= 640 && t < 672) *(float*)(smem + L_EA + 196 * 128 + (t - 640) * 4) = 0.f;
    LDS_BARRIER();
    asm volatile("s_waitcnt vmcnt(0)" : "+v"(xn2), "+v"(cw2), "+v"(d4), "+v"(s4), "+v"(e2) :: "memory");
    xn = xn2; cw = cw2;
    const u64 tag40 = ((((u64)xn[1] << 32) | xn[0]) + 1ull) & 0xffffffffffull;
    const uint32_t ev[4] = {e2[0] & 0xffffu, e2[0] >> 16, e2[1] & 0xffffu, e2[1] >> 16};
    uint32_t bk[4], rk0[4];
#pragma unroll
    for (int k = 0; k < 4; k++) {
      bk[k] = (d4[k] * 21400u) >> 22;
      rk0[k] = 0;
      if (k < cnt) rk0[k] = __hip_atomic_fetch_add(&hist[bk[k]], 1u, RLX_WG);
    }
    TS(1);
    group_barrier(lane);
    if (wv == 0) {
      const u32x2 c = *(const u32x2*)(hist + 2 * t);
      const uint32_t p0 = (c[0] + 3) >> 2, tot = p0 + ((c[1] + 3) >> 2);
      const uint32_t ex = wave_scan_add(tot) - tot;
      *(u32x2*)(base + 2 * t) = u32x2{ex, ex + p0};
      if (t == 0) { misc[M_TAG0] = xn[0]; misc[M_TAG1] = xn[1]; }
    } else if (wv < 12) {
      const __amdgpu_buffer_rsrc_t asrd = __builtin_amdgcn_make_buffer_rsrc((void*)gw, (short)0, 64 * 128, 0x00020000);
      const __amdgpu_buffer_rsrc_t usrd = __builtin_amdgcn_make_buffer_rsrc((void*)uw, (short)0, 64 * 128, 0x00020000);
      const uint32_t woff = (lane >> 2) * 128 + half * 64 + (lane & 3) * 16;
#pragma unroll
      for (int i = 0; i < 3; i++) {
        const int p = wv - 1 + 11 * i;
        if (p < 4) dma16(lb + P_W + p * 1024, p * 2048 + woff, asrd);
        else if (p < 8) dma16(lb + P_W + p * 1024, (p - 4) * 2048 + woff, usrd);
        else { const int q = p - 8; dma16(lb + P_X + q * 1024, (b * NPB + 8 * q + (lane >> 3)) * 128 + ((q & 1) ? cA ^ 64u : cA), xsrd); }
      }
    }
    const uint32_t m2 = wave_max_u32(max(max(ev[0] & 0x7fffu, ev[1] & 0x7fffu), max(ev[2] & 0x7fffu, ev[3] & 0x7fffu)) << 16);
    if (lane == 63) __hip_atomic_fetch_max(&misc[M_MX + 2], m2, RLX_WG);
    group_barrier(lane);
#pragma unroll
    for (int k = 0; k < 4; k++) {
      if (k < cnt) {
        const uint32_t p = base[bk[k]] * 4 + rk0[k];
        const uint32_t o = (p >> 2) * 20 + (p & 3);
        *(uint32_t*)(smem + A_ST + o + (p & 3) * 3) = (s4[k] & 0xffffu) | (ev[k] << 16);
        *(uint8_t*)(smem + A_ST + o + 16) = (uint8_t)(d4[k] - bk[k] * NPB);
      }
    }
    group_barrier(lane);
    TS(2);
    const __amdgpu_buffer_rsrc_t rsrd = __builtin_amdgcn_make_buffer_rsrc((void*)(ws + WS_REC + (size_t)w * CHB * 20), (short)0, CHB * 20, 0x00020000);
    const uint32_t nb = base[127] + ((hist[127] + 3) >> 2);
#pragma unroll
    for (int i = 0; i < 2; i++) {
      const int c = t + 832 * i;
      if (c * 16 < (int)nb * 20) __builtin_amdgcn_raw_buffer_store_b128(*(const u32x4*)(smem + A_ST + c * 16), rsrd, c * 16, 0, AUX_SC1);
    }
    WAIT_VM(0);
    if (arrive(&misc[M_CW], lane) == 12) {
      const u32x2 c = *(const u32x2*)(hist + 2 * lane), o = *(const u32x2*)(base + 2 * lane);
      gu64* g = (gu64*)(ws + WS_SEG) + w * NB + 2 * lane;
      __hip_atomic_store(g,     (u64)(o[0] | (c[0] << 12)) | (tag40 << 24), RLX_AGENT);
      __hip_atomic_store(g + 1, (u64)(o[1] | (c[1] << 12)) | (tag40 << 24), RLX_AGENT);
      if (lane == 0) { __hip_atomic_store((gu64*)(ws + WS_EMG) + w, (u64)misc[M_MX + 2] | ((u64)(xn[0] + 1u) << 32), RLX_AGENT); __hip_atomic_store(&misc[M_PUB], 1u, RLX_WG); TS_PUB(0); }
    }
  }
  const uint32_t tag32 = xn[0] + 1u;
  const u64 tag40 = ((((u64)xn[1] << 32) | xn[0]) + 1ull) & 0xffffffffffull;
  L.c01 = f32x2{bf2f(cw[0] & 0xffffu) * NLOG2E, __uint_as_float(cw[0] & 0xffff0000u) * NLOG2E};
  L.c23 = f32x2{bf2f(cw[1] & 0xffffu) * NLOG2E, __uint_as_float(cw[1] & 0xffff0000u) * NLOG2E};
  TS(3);

  auto thread_id = [&]() {
    int v = wv * 64 + (int)__builtin_amdgcn_mbcnt_hi(~0u, __builtin_amdgcn_mbcnt_lo(~0u, 0u));
    asm volatile("" : "+v"(v));
    return v;
  };
  const int tb = thread_id(), lb_ = tb & 63;
  const __amdgpu_buffer_rsrc_t tsrd = __builtin_amdgcn_make_buffer_rsrc((void*)(ws + WS_TAB + (size_t)half * NMAX * 128), (short)0, N * 128, 0x00020000);
  uint32_t sg[3] = {0, 0, 0};
  auto poll_segments = [&](int g) {
    gu64* gs = (gu64*)(ws + WS_SEG) + g * NB + b;
    const bool third = g + 192 < NCH;
    for (unsigned spins = 0;; spins++) {
      const u64 v0 = __hip_atomic_load(gs, RLX_AGENT), v1 = __hip_atomic_load(gs + 96 * NB, RLX_AGENT);
      const u64 v2 = __hip_atomic_load(gs + (third ? 192 * NB : 0), RLX_AGENT);
      const bool ok = (v0 >> 24) == tag40 && (v1 >> 24) == tag40 && (v2 >> 24) == tag40;
      sg[0] = ((uint32_t)v0 & 0xfffu) | ((((uint32_t)v0 >> 12) & 0xfffu) << 16);
      sg[1] = ((uint32_t)v1 & 0xfffu) | ((((uint32_t)v1 >> 12) & 0xfffu) << 16);
      sg[2] = third ? ((uint32_t)v2 & 0xfffu) | ((((uint32_t)v2 >> 12) & 0xfffu) << 16) : 0u;
      if (__all(ok)) break;
      if (spins > SPIN_CAP) { sg[0] = sg[1] = sg[2] = 0; break; }
      __builtin_amdgcn_s_sleep(12);
    }
  };
  Recs R;
  int done = 0;
  uint32_t rk[6][2]; bool part1[3] = {false, false, false};
  auto post_total = [&](int dn, int j, int l) {
    uint32_t v = 0;
    if (j == 0) v = min(max((int)(sg[0] >> 16) - dn, 0), 64) + min(max((int)(sg[1] >> 16) - dn, 0), 64) + min(max((int)(sg[2] >> 16) - dn, 0), 64);
    v = wave_scan_add(v);
    if (l == 63) __hip_atomic_fetch_add(&misc[M_TOT], v, RLX_WG);
  };
  auto rank = [&](int tt, int dn, int take) {
    const int j = tt & 7;
    const uint32_t spare = (196u + (uint32_t)(tt & 63) % 60u) * 0x01010101u;
    bool more = false;
#pragma unroll
    for (int i = 0; i < 6; i++) {
      const int len = (int)(sg[i >> 1] >> 16) - dn;
      more |= len > take;
      const int v = min(max(min(len, take) - 32 * (i & 1) - 4 * j, 0), 4);
      const uint32_t drop = (uint32_t)(0xffffffffull << (8 * v));
      R.lc[i] = (R.lc[i] & ~drop) | (spare & drop);
      if (i & 1) part1[i >> 1] = __any(v > 0);
    }
    uint32_t r[3][4];
#pragma unroll
    for (int st = 0; st < 3; st++)
#pragma unroll
      for (int k = 0; k < 4; k++) r[st][k] = __hip_atomic_fetch_add((LDSP(uint32_t))(L_CNT + byte_x4(R.lc[2 * st], k)), 1u, RLX_WG);
#pragma unroll
    for (int st = 0; st < 3; st++) {
      rk[2 * st][0] = r[st][0] | (r[st][1] << 16); rk[2 * st][1] = r[st][2] | (r[st][3] << 16);
      asm volatile("" : "+v"(rk[2 * st][0]), "+v"(rk[2 * st][1]));
      rk[2 * st + 1][0] = rk[2 * st + 1][1] = 0;
      if (part1[st]) {
        uint32_t q[4];
#pragma unroll
        for (int k = 0; k < 4; k++) q[k] = __hip_atomic_fetch_add((LDSP(uint32_t))(L_CNT + byte_x4(R.lc[2 * st + 1], k)), 1u, RLX_WG);
        rk[2 * st + 1][0] = q[0] | (q[1] << 16); rk[2 * st + 1][1] = q[2] | (q[3] << 16);
      }
    }
    if (__any(more) && (tt & 63) == 0) misc[M_MORE] = 1;
  };
  auto scan = [&](int ln_) {
    u32x4 c = *(const u32x4*)(smem + L_CNT + ln_ * 16);
    if (ln_ >= NPB / 4) c = u32x4{0, 0, 0, 0};
    const u32x4 p = (c + 3) & ~3u;
    const uint32_t s1 = p[0] + p[1], s2 = s1 + p[2], tot = s2 + p[3];
    const uint32_t inc = wave_scan_add(tot), ex = inc - tot;
    *(u32x4*)(smem + L_OFF + ln_ * 16) = ln_ < NPB / 4 ? u32x4{ex, ex + p[0], ex + s1, ex + s2} : u32x4{SORT_CAP, SORT_CAP, SORT_CAP, SORT_CAP};
    const uint32_t T4 = (uint32_t)__builtin_amdgcn_readlane((int)inc, 63);
    const uint32_t T32 = (T4 + 31) & ~31u;
    if ((T4 >> 2) + ln_ < (T32 >> 2) + 2) *(uint32_t*)(smem + L_QN + ((T4 >> 2) + ln_) * 4) = QN_DUMMY;
    if (ln_ == 0) { misc[M_NSTEP] = T32 >> 5; misc[M_T4] = T4; }
  };
  auto scatter = [&](int tt) {
    if (wv < 12) {
#pragma unroll
      for (int i = 0; i < 6; i++) {
        if ((i & 1) == 0 || part1[i >> 1]) {
#pragma unroll
          for (int k = 0; k < 4; k++) {
            const uint32_t pos = min(*(LDSP(const uint32_t))(L_OFF + byte_x4(R.lc[i], k)) + ((rk[i][k >> 1] >> (16 * (k & 1))) & 0xffffu), (uint32_t)(SORT_CAP - 1));
            const uint32_t rec = R.rc[i][k];
            *(LDSP(u32x2))(lb + L_SORT + pos * 8) = u32x2{lo_x128(rec), rec & 0xffff0000u};
          }
        }
      }
    }
    u32x2 dummy = {(uint32_t)tt, 0u};
    asm volatile("" : "+v"(dummy));
    dummy = u32x2{dummy[0] * 0u + OFF_DUMMY, 0u};
    const int u = 831 - tt;
    if (u >= 0 && u < NPB * 4) {
      const uint32_t nd = u >> 2, k = u & 3;
      const uint32_t c = *(const uint32_t*)(smem + L_CNT + nd * 4), o = *(const uint32_t*)(smem + L_OFF + nd * 4);
      const uint32_t val = nd * (128u + (AGG_ROW << 16));
      for (uint32_t q = k; 4 * q < c; q += 4) *(uint32_t*)(smem + L_QN + o + q * 4) = val;
      if (k == 3) *(uint32_t*)(smem + L_NQ + nd * 4) += (c + 3) >> 2;
      if (k < 3 && c + k < ((c + 3) & ~3u)) *(u32x2*)(smem + L_SORT + (o + c + k) * 8) = dummy;
    }
    if (wv == 12) {
      const uint32_t T4 = misc[M_T4], T32 = (T4 + 31) & ~31u, i = T4 + (tt & 63);
      if (i < T32 + 8) *(u32x2*)(smem + L_SORT + i * 8) = dummy;
    }
  };
  auto lane_consts = [&](int tt, int nstep) {
    const int n_ = (nstep + 15 - wv) >> 4, first = 8 * (wv * (nstep >> 4) + min(wv, nstep & 15));
    const int g = (tt & 63) >> 3, j = tt & 7;
    L.rec = lb + L_SORT + (first + g * n_) * 32;
    L.qn  = lb + L_QN + (first + g * n_) * 4;
    L.c16 = j * 16;
    L.ea  = lb + L_EA + j * 16;
    L.agg = lb + L_AGG + j * 4;
    asm volatile("" : "+v"(L.rec), "+v"(L.qn), "+v"(L.ea), "+v"(L.agg));
    return n_;
  };

  int take = 64;
  bool fast = false; float delta = 0.f;
  if (wv <= 12) {
    if (wv < 12) {
      __builtin_amdgcn_s_sleep(POLL_DELAY);
      poll_segments(tb >> 3);
      TS(4);
      load_recs(R, sg, ws, tb >> 3, tb & 7, 0);
      post_total(0, tb & 7, lb_);
      uint32_t v = (tb & 7) == 0 ? (sg[0] >> 16) + (sg[1] >> 16) + (sg[2] >> 16) : 0u;
      v = wave_scan_add(v);
      if (lb_ == 63) __hip_atomic_fetch_add(&misc[M_ALL], v, RLX_WG);
      arrive(&misc[M_CP], lb_);
      if (wv >= 5) {
        await_lds(&misc[M_CW], 13u);
        const int rt = wv - 5, r31_ = lb_ & 31, h_ = lb_ >> 5, row = 32 * rt + r31_;
        const uint32_t xa = lb + P_X + row * 128 + ((h_ ^ ((row >> 1) & 7)) * 16);
        const uint32_t troff = lb + P_W + (8 * h_ + ((lb_ & 15) >> 2)) * 64 + (16 * ((lb_ >> 4) & 1) + 4 * (lb_ & 3)) * 2;
        uint32_t xam = 0;
  #pragma unroll
        for (int part = 0; part < 2; part++) {
          f32x16 acc;
  #pragma unroll
          for (int i = 0; i < 16; i++) acc[i] = 0.f;
  #pragma unroll
          for (int s = 0; s < 4; s++) acc = mfma(*(LDSP(const bf16x8))(xa ^ (32u * s)), tr_frag(troff + part * 4096 + s * 1024, 256), acc);
          const uint32_t o = lb + (part ? L_XU : L_EA) + (32 * rt + 4 * h_) * 128 + r31_ * 4;
  #pragma unroll
          for (int i = 0; i < 16; i++) {
            const float v = part ? acc[i] : acc[i] * NLOG2E;
            if (rt < 6 || crow(i, 0) + 4 * h_ < 4) { *(LDSP(float))(o + crow(i, 0) * 128) = v; if (!part) xam = max(xam, absbits(v)); }
          }
        }
        xam = wave_max_u32(xam);
        if (lb_ == 63) __hip_atomic_fetch_max(&misc[M_XAM], xam, RLX_WG);
        arrive(&misc[M_CE], lb_);
      }
      WAIT_RECS(R);
      TS(5);
      rank(tb, 0, take);
    } else {
      await_lds(&misc[M_CB], 3u);
      if (lb_ == 0) {
        const u32x4 m = *(const u32x4*)(misc + M_MX), q = *(const u32x4*)(misc + M_MX + 4);
        const float sq = (__uint_as_float(q[1]) + __uint_as_float(q[2])) + __uint_as_float(q[3]);
        gu64* g = (gu64*)(ws + WS_TBG) + 2 * w;
        __hip_atomic_store(g,     (u64)(min((m[0] + 0xffffu) >> 16, 0x7fffu) | (min((m[1] + 0xffffu) >> 16, 0x7fffu) << 16)) | ((u64)tag32 << 32), RLX_AGENT);
        __hip_atomic_store(g + 1, (u64)__float_as_uint(sq) | ((u64)tag32 << 32), RLX_AGENT);
      }
    }
    group_barrier(lb_);
    if (w == 0 && tb == 0) __hip_atomic_store((gu64*)(ws + WS_NONCE), (((u64)xn[1] << 32) | xn[0]) + 1ull, RLX_AGENT);
    if (__builtin_amdgcn_readfirstlane(misc[M_TOT]) > (uint32_t)TAKE_MAX) {
      group_barrier(lb_);
      if (tb < 256) *(uint32_t*)(smem + L_CNT + tb * 4) = 0;
      group_barrier(lb_);
      take = 16;
      if (wv < 12) rank(tb, 0, take);
      group_barrier(lb_);
    }
    if (wv == 12) scan(lb_);
    else {
      for (int i = tb; i < 197 * AGG_ROW / 16; i += 768) *(u32x4*)(smem + L_AGG + i * 16) = u32x4{0, 0, 0, 0};
    }
    await_lds(&misc[M_CB], 3u);
    group_barrier(lb_);
    TS(6);
    scatter(tb);
    if (wv == 12) {
      gu64* gr = (gu64*)(ws + WS_TRG) + lb_;
      for (unsigned spins = 0; spins < SPIN_CAP; spins++) {
        bool ok = true;
#pragma unroll
        for (int i = 0; i < 4; i++) ok &= (uint32_t)(__hip_atomic_load(gr + 64 * i, RLX_AGENT) >> 32) == tag32;
        if (__all(ok)) break;
        __builtin_amdgcn_s_sleep(16);
      }
      const uint32_t cm = wave_max_u32(max(max(absbits(L.c01[0]), absbits(L.c01[1])), max(absbits(L.c23[0]), absbits(L.c23[1]))));
      gu64* gt = (gu64*)(ws + WS_TBG) + 2 * lb_;
      gu64* ge = (gu64*)(ws + WS_EMG) + lb_;
      uint32_t mb, mv_, me; float sq;
      for (unsigned spins = 0;; spins++) {
        bool ok = true; mb = mv_ = me = 0; sq = 0.f;
#pragma unroll
        for (int i = 0; i < 4; i++) {
          const u64 v0 = __hip_atomic_load(gt + 128 * i, RLX_AGENT), v1 = __hip_atomic_load(gt + 128 * i + 1, RLX_AGENT), v2 = __hip_atomic_load(ge + 64 * i, RLX_AGENT);
          ok &= (uint32_t)(v0 >> 32) == tag32 && (uint32_t)(v1 >> 32) == tag32 && (uint32_t)(v2 >> 32) == tag32;
          mb = max(mb, (uint32_t)v0 << 16); mv_ = max(mv_, (uint32_t)v0 & 0xffff0000u);
          me = max(me, (uint32_t)v2); sq += __uint_as_float((uint32_t)v1);
        }
        if (__all(ok)) break;
        if (spins > SPIN_CAP) break;
        __builtin_amdgcn_s_sleep(16);
      }
      mb = wave_max_u32(mb); mv_ = wave_max_u32(mv_); me = wave_max_u32(me); sq = wave_sum_f32(sq);
      const float mv = __uint_as_float(mv_);
      const float yb = (__uint_as_float(misc[M_XAM]) + __uint_as_float(mb) + __uint_as_float(me) * __uint_as_float(cm)) * 1.01f;
      const float all = (float)(misc[M_ALL] + 4u);
      const int p1 = (int)(__float_as_uint(4.1f * mv) >> 23) - 127 + 2, p2 = (int)(__float_as_uint(all * mv * 1.01f) >> 23) - 127 + 1 - 8;
      const int p = max(max(p1, p2), -80);
      const float dl = __uint_as_float((uint32_t)(p - 23 + 127) << 23);
      const bool fine = dl * 4096.f <= __builtin_amdgcn_sqrtf(sq * __builtin_amdgcn_rcpf(64.f * (float)N)) * 0.99f || mv == 0.f;
      if (lb_ == 63) {
        misc[M_FAST] = (yb <= 28.f) && (mv <= 1.0e12f) && fine;
        misc[M_EXP] = (uint32_t)(p + 127);
      }
    }
  }
  LDS_BARRIER();
  if (__builtin_amdgcn_readfirstlane(misc[M_FAST])) {
    for (int i = tb; i < NPB * 8; i += 1024) {
      f32x4 v = *(const f32x4*)(smem + L_EA + i * 16);
      v[0] = __builtin_amdgcn_exp2f(v[0]); v[1] = __builtin_amdgcn_exp2f(v[1]); v[2] = __builtin_amdgcn_exp2f(v[2]); v[3] = __builtin_amdgcn_exp2f(v[3]);
      *(f32x4*)(smem + L_EA + i * 16) = v;
    }
  }
  LDS_BARRIER();
  fast = __builtin_amdgcn_readfirstlane(misc[M_FAST]);
  take = __builtin_amdgcn_readfirstlane(misc[M_TOT]) > (uint32_t)TAKE_MAX ? 16 : 64;
  {
    const uint32_t pexp = __builtin_amdgcn_readfirstlane(misc[M_EXP]);
    delta = __uint_as_float((pexp - 23u) << 23);
    const float mg = __uint_as_float((pexp << 23) | 0x400000u);
    L.magic = f32x2{mg, mg};
  }
  int again = __builtin_amdgcn_readfirstlane(misc[M_MORE]);
  int nstep = __builtin_amdgcn_readfirstlane(misc[M_NSTEP]);
  TS(7);
  {
    const int n_ = lane_consts(tb, nstep);
    if (fast) edge_loop<0>(L, n_, tsrd);
    else      edge_loop<2>(L, n_, tsrd);
  }
  while (again) {
    done += take;
    const int t2 = thread_id();
    if (wv < 12) { poll_segments(t2 >> 3); load_recs(R, sg, ws, t2 >> 3, t2 & 7, done); WAIT_RECS(R); }
    LDS_BARRIER();
    if (t2 < 256) *(uint32_t*)(smem + L_CNT + t2 * 4) = 0;
    if (t2 == 0) { misc[M_MORE] = 0; misc[M_TOT] = 0; }
    LDS_BARRIER();
    if (wv < 12) post_total(done, t2 & 7, t2 & 63);
    LDS_BARRIER();
    take = __builtin_amdgcn_readfirstlane(misc[M_TOT]) <= (uint32_t)TAKE_MAX ? 64 : 16;
    if (wv < 12) rank(t2, done, take);
    LDS_BARRIER();
    if (wv == 12) scan(t2 & 63);
    LDS_BARRIER();
    scatter(t2);
    again = __builtin_amdgcn_readfirstlane(misc[M_MORE]);
    LDS_BARRIER();
    nstep = __builtin_amdgcn_readfirstlane(misc[M_NSTEP]);
    const int n_ = lane_consts(t2, nstep);
    if (fast) edge_loop<0>(L, n_, tsrd);
    else      edge_loop<2>(L, n_, tsrd);
  }
  LDS_BARRIER();

  const int te = thread_id();
  if (te < NPB * 4) {
    const int nl = te >> 2, u = te & 3, node = b * NPB + nl;
    if (node < N) {
      float y[8];
      const uint32_t bias = *(const uint32_t*)(smem + L_NQ + nl * 4) * __float_as_uint(L.magic[0]);
      const f32x4 u0 = *(const f32x4*)(smem + L_XU + nl * 128 + u * 32), u1 = *(const f32x4*)(smem + L_XU + nl * 128 + u * 32 + 16);
#pragma unroll
      for (int i = 0; i < 4; i++) {
        const u32x2 v2 = *(const u32x2*)(smem + L_AGG + nl * AGG_ROW + (8 * i + 2 * u) * 4);
#pragma unroll
        for (int j = 0; j < 2; j++) {
          const float agg = fast ? (float)(int)(v2[j] - bias) * delta : __uint_as_float(v2[j]);
          const float v = (j ? u1[i] : u0[i]) + agg;
          y[4 * j + i] = v * __builtin_amdgcn_rcpf(1.f + __builtin_amdgcn_exp2f(v * NLOG2E));
        }
      }
      const u32x4 o = {cvtpk(y[0], y[1]), cvtpk(y[2], y[3]), cvtpk(y[4], y[5]), cvtpk(y[6], y[7])};
      *(u32x4*)(out + (size_t)node * 64 + 32 * half + 8 * u) = o;
    }
  }
  TS_OUT(256 + blockIdx.x);
}

}

extern "C" void kernel_launch(void* const* d_in, const int* in_sizes, int n_in,
                              void* d_out, int out_size, void* d_ws, size_t ws_size,
                              hipStream_t stream) {
  const uint16_t* x  = (const uint16_t*)d_in[0];
  const uint16_t* ea = (const uint16_t*)d_in[1];
  const int*      src = (const int*)d_in[2];
  const int*      dst = (const int*)d_in[3];
  const uint16_t* uw = (const uint16_t*)d_in[4];
  const uint16_t* vw = (const uint16_t*)d_in[5];
  const uint16_t* gw = (const uint16_t*)d_in[6];
  const int N = in_sizes[0] / 64, E = in_sizes[2];
  char* ws = (char*)d_ws;
  (void)hipFuncSetAttribute((const void*)k_mpnn, hipFuncAttributeMaxDynamicSharedMemorySize, L_TOTAL);
  k_mpnn<<<NCH, 1024, L_TOTAL, stream>>>(src, dst, ea, x, uw, vw, gw, ws, (uint16_t*)d_out, N, E);
}
```

```cpp
#include <hip/hip_runtime.h>
#include <hip/hip_bf16.h>
#include <stdint.h>

#pragma clang diagnostic ignored "-Wint-to-pointer-cast"
#pragma clang diagnostic ignored "-Wpointer-to-int-cast"
namespace {

typedef float    f32x2  __attribute__((ext_vector_type(2)));
typedef float    f32x4  __attribute__((ext_vector_type(4)));
typedef float    f32x16 __attribute__((ext_vector_type(16)));
typedef short    bf16x8 __attribute__((ext_vector_type(8)));
typedef short    s16x4  __attribute__((ext_vector_type(4)));
typedef uint32_t u32x2  __attribute__((ext_vector_type(2)));
typedef uint32_t u32x4  __attribute__((ext_vector_type(4)));
typedef unsigned long long u64;
typedef __attribute__((address_space(1))) unsigned long long gu64;
#define LDSP(T) __attribute__((address_space(3))) T*
#define DI __device__ __forceinline__
#define RLX_AGENT __ATOMIC_RELAXED, __HIP_MEMORY_SCOPE_AGENT
#define RLX_WG    __ATOMIC_RELAXED, __HIP_MEMORY_SCOPE_WORKGROUP

constexpr int   NWG = 256;
constexpr int   NPB = 98;
constexpr int   CH  = 3128;
constexpr int   CHB = 976;
constexpr float NLOG2E = -1.4426950408889634f;
constexpr int   AUX_SC1 = 16;
constexpr unsigned SPIN_CAP = 1u << 18;
static_assert(4 * CHB >= CH + 3 * NWG && (CHB * 20) % 16 == 0, "blocks");

constexpr size_t WS_NONCE = 0;
constexpr size_t WS_SEG = 128;
constexpr size_t WS_BND = WS_SEG + (size_t)NWG * NWG * 8;
constexpr size_t WS_REC = WS_BND + (size_t)NWG * 32;
constexpr size_t WS_DBG = (WS_REC + (size_t)NWG * CHB * 20 + 1024 + 127) & ~(size_t)127;
static_assert(WS_REC % 128 == 0, "ws align");

constexpr int SORT_CAP = 4528;
constexpr int TAKE32_MAX = SORT_CAP - 3 * NPB - 64 - 8;
constexpr int L_CNT    = 0;
constexpr int L_OFF    = L_CNT + 512;
constexpr int L_NQ     = L_OFF + 512;
constexpr int L_MISC   = L_NQ + 512;
constexpr int L_QN     = L_MISC + 512;
constexpr int L_EA     = (L_QN + SORT_CAP / 2 + 15) & ~15;
constexpr int L_XU     = L_EA + 99 * 256;
constexpr int L_AGG    = (L_XU + 98 * 256 + 127) & ~127;
constexpr int L_STAGE  = L_AGG + 99 * 256;
constexpr int L_SORT   = L_STAGE + 8 * 8192;
constexpr int L_TOTAL  = L_SORT + SORT_CAP * 4;
static_assert(L_TOTAL <= 163840, "LDS");
static_assert((L_STAGE & 127) == 0 && (L_SORT & 15) == 0 && TAKE32_MAX >= 4096, "align");
constexpr int A_HIST = L_STAGE;
constexpr int A_BASE = A_HIST + 1024;
constexpr int A_ST   = A_BASE + 1024;
constexpr int A_W    = (A_ST + CHB * 20 + 127) & ~127;
constexpr int P_X    = L_AGG;
static_assert(A_W + 4 * 8192 <= L_SORT && P_X + 128 * 128 <= L_STAGE, "phase A images");
constexpr uint32_t DUMMY = 0x0000FFFFu;
constexpr uint32_t QN_DUMMY = 98u * 256u;
constexpr int M_MORE = 0, M_NTILE = 1, M_T4 = 2;
constexpr int M_CW = 3, M_CB = 4;
constexpr int M_TOT = 10, M_ALL = 11, M_XAM = 12;
constexpr int M_FAST = 13, M_EXP = 14;
constexpr int M_MX = 16;
constexpr int M_SQ = 24;

DI float    bf2f(uint32_t v16) { return __uint_as_float(v16 << 16); }
DI uint32_t cvtpk(float lo, float hi) {
  typedef __bf16 bf16x2 __attribute__((ext_vector_type(2)));
  return __builtin_bit_cast(uint32_t, __builtin_convertvector(f32x2{lo, hi}, bf16x2));
}
DI f32x16   mfma(bf16x8 a, bf16x8 b, f32x16 c) { return __builtin_amdgcn_mfma_f32_32x32x16_bf16(a, b, c, 0, 0, 0); }
DI int      crow(int r, int h) { return (r & 3) + 8 * (r >> 2) + 4 * h; }
DI uint32_t absbits(float v) { return __float_as_uint(v) & 0x7fffffffu; }

template <int CTRL, int RM> DI uint32_t dpp0(uint32_t v) { return (uint32_t)__builtin_amdgcn_update_dpp(0, (int)v, CTRL, RM, 0xf, false); }
DI uint32_t wave_scan_add(uint32_t v) {
  v += dpp0<0x111, 0xf>(v); v += dpp0<0x112, 0xf>(v); v += dpp0<0x114, 0xf>(v); v += dpp0<0x118, 0xf>(v);
  v += dpp0<0x142, 0xa>(v); v += dpp0<0x143, 0xc>(v);
  return v;
}
DI uint32_t wave_max_u32(uint32_t v) {
  v = max(v, dpp0<0x111, 0xf>(v)); v = max(v, dpp0<0x112, 0xf>(v)); v = max(v, dpp0<0x114, 0xf>(v)); v = max(v, dpp0<0x118, 0xf>(v));
  v = max(v, dpp0<0x142, 0xa>(v)); v = max(v, dpp0<0x143, 0xc>(v));
  return v;
}
DI float wave_sum_f32(float s) {
  s += __uint_as_float(dpp0<0x111, 0xf>(__float_as_uint(s))); s += __uint_as_float(dpp0<0x112, 0xf>(__float_as_uint(s)));
  s += __uint_as_float(dpp0<0x114, 0xf>(__float_as_uint(s))); s += __uint_as_float(dpp0<0x118, 0xf>(__float_as_uint(s)));
  s += __uint_as_float(dpp0<0x142, 0xa>(__float_as_uint(s))); s += __uint_as_float(dpp0<0x143, 0xc>(__float_as_uint(s)));
  return s;
}
#define SB() __builtin_amdgcn_sched_barrier(0)
#define LDS_BARRIER() asm volatile("s_waitcnt lgkmcnt(0)\n\ts_barrier" ::: "memory")
#define WAIT_VM(N) asm volatile("s_waitcnt vmcnt(" #N ")" ::: "memory")

DI uint32_t arrive(uint32_t* word, int lane) {
  asm volatile("s_waitcnt lgkmcnt(0)" ::: "memory");
  uint32_t v = 0;
  if (lane == 0) v = __hip_atomic_fetch_add(word, 1u, RLX_WG);
  return __builtin_amdgcn_readfirstlane(v);
}
DI void await_lds(uint32_t* word, uint32_t want) {
  for (unsigned spins = 0; spins < SPIN_CAP; spins++) {
    if (__builtin_amdgcn_readfirstlane(__hip_atomic_load(word, RLX_WG)) >= want) break;
    __builtin_amdgcn_s_sleep(1);
  }
  asm volatile("" ::: "memory");
}

DI void dma16(uint32_t ldsaddr, uint32_t voff, __amdgpu_buffer_rsrc_t srd) {
  asm volatile("s_mov_b32 m0, %0\n\ts_nop 0\n\tbuffer_load_dwordx4 %1, %2, 0 offen lds" :: "s"(ldsaddr), "v"(voff), "s"(srd) : "memory");
}
DI bf16x8 tr_frag(uint32_t addr, int four_rows) {
  const s16x4 lo = __builtin_amdgcn_ds_read_tr16_b64_v4i16((LDSP(s16x4))addr);
  const s16x4 hi = __builtin_amdgcn_ds_read_tr16_b64_v4i16((LDSP(s16x4))(addr + four_rows));
  return (bf16x8)__builtin_shufflevector(lo, hi, 0, 1, 2, 3, 4, 5, 6, 7);
}

#define TS(i) do { } while (0)
#define TS_DECL do { } while (0)
#define TS_OUT(slot) do { } while (0)
#define TS_PUB(i) do { } while (0)

DI f32x2 pair(const f32x16& v, int i) { f32x2 r = {v[i], v[i + 1]}; return r; }
DI f32x2 exp2x2(f32x2 v) { f32x2 r = {__builtin_amdgcn_exp2f(v[0]), __builtin_amdgcn_exp2f(v[1])}; return r; }
DI f32x2 rcpx2(f32x2 v) { f32x2 r = {__builtin_amdgcn_rcpf(v[0]), __builtin_amdgcn_rcpf(v[1])}; return r; }
DI f32x2 fma2(f32x2 a, f32x2 b, f32x2 c) { return __builtin_elementwise_fma(a, b, c); }

struct Wts { bf16x8 b[2][4], v[2][4], c[2]; };
template <int V> struct IC { static constexpr int value = V; };
struct Acc { f32x16 z, hv; };

struct Frac { f32x2 n[2], d[2]; };
DI void exp_all(f32x16& z) {
#pragma unroll
  for (int i = 0; i < 16; i++) z[i] = __builtin_amdgcn_exp2f(z[i]);
}
DI void combine(Frac& F, const f32x16& u, const f32x16& hv, int o, f32x2 ea) {
  const f32x2 one = {1.f, 1.f};
  const f32x2 h0 = pair(hv, 8 * o), h1 = pair(hv, 8 * o + 2), h2 = pair(hv, 8 * o + 4), h3 = pair(hv, 8 * o + 6);
  const f32x2 a0 = fma2(ea, pair(u, 8 * o), one),     a1 = fma2(ea, pair(u, 8 * o + 2), one);
  const f32x2 a2 = fma2(ea, pair(u, 8 * o + 4), one), a3 = fma2(ea, pair(u, 8 * o + 6), one);
  const f32x2 d01 = a0 * a1, n01 = fma2(h1, a0, h0 * a1);
  const f32x2 d23 = a2 * a3, n23 = fma2(h3, a2, h2 * a3);
  F.d[o] = d01 * d23; F.n[o] = fma2(n23, d01, n01 * d23);
}
template <bool EXPEA> DI f32x2 octet_plain(const Acc& A, int o, f32x2 ea) {
  const f32x2 one = {1.f, 1.f};
  const f32x16& z = A.z; const f32x16& hv = A.hv;
  const f32x2 h0 = pair(hv, 8 * o), h1 = pair(hv, 8 * o + 2), h2 = pair(hv, 8 * o + 4), h3 = pair(hv, 8 * o + 6);
  f32x2 r[4];
#pragma unroll
  for (int j = 0; j < 4; j++)
    r[j] = EXPEA ? rcpx2(fma2(ea, exp2x2(pair(z, 8 * o + 2 * j)), one)) : rcpx2(one + exp2x2(pair(z, 8 * o + 2 * j) + ea));
  const f32x2 r0 = r[0], r1 = r[1], r2 = r[2], r3 = r[3];
  return (h0 * r0 + h1 * r1) + (h2 * r2 + h3 * r3);
}

DI uint32_t madlo(uint32_t v, uint32_t mul, uint32_t add) {
  uint32_t r; asm("v_mad_u32_u16 %0, %1, %2, %3" : "=v"(r) : "v"(v), "s"(mul), "v"(add)); return r;
}
template <int MUL> DI uint32_t madlo_i(uint32_t v, uint32_t add) {
  uint32_t r; asm("v_mad_u32_u16 %0, %1, %3, %2" : "=v"(r) : "v"(v), "v"(add), "i"(MUL)); return r;
}
template <int MUL> DI uint32_t madhi_i(uint32_t v, uint32_t add) {
  uint32_t r; asm("v_mad_u32_u16 %0, %1, %3, %2 op_sel:[1,0,0,0]" : "=v"(r) : "v"(v), "v"(add), "i"(MUL)); return r;
}
template <int K> DI uint32_t byte_x4(uint32_t v) {
  uint32_t r;
  if (K == 0) asm("v_lshlrev_b32_sdwa %0, 2, %1 dst_sel:DWORD dst_unused:UNUSED_PAD src0_sel:DWORD src1_sel:BYTE_0" : "=v"(r) : "v"(v));
  if (K == 1) asm("v_lshlrev_b32_sdwa %0, 2, %1 dst_sel:DWORD dst_unused:UNUSED_PAD src0_sel:DWORD src1_sel:BYTE_1" : "=v"(r) : "v"(v));
  if (K == 2) asm("v_lshlrev_b32_sdwa %0, 2, %1 dst_sel:DWORD dst_unused:UNUSED_PAD src0_sel:DWORD src1_sel:BYTE_2" : "=v"(r) : "v"(v));
  if (K == 3) asm("v_lshlrev_b32_sdwa %0, 2, %1 dst_sel:DWORD dst_unused:UNUSED_PAD src0_sel:DWORD src1_sel:BYTE_3" : "=v"(r) : "v"(v));
  return r;
}
DI uint32_t byte_x4(uint32_t v, int k) { return k == 0 ? byte_x4<0>(v) : (k == 1 ? byte_x4<1>(v) : (k == 2 ? byte_x4<2>(v) : byte_x4<3>(v))); }
DI void lds_add_u32(uint32_t addr, float v) { __hip_atomic_fetch_add((LDSP(uint32_t))addr, __float_as_uint(v), RLX_WG); }
DI void lds_add_f32(uint32_t addr, float v) { __hip_atomic_fetch_add((LDSP(float))addr, v, RLX_WG); }

struct Lane {
  uint32_t rec_a;
  uint32_t cA, cB;
  uint32_t afrag[4];
  uint32_t erec;
  uint32_t esel;
  uint32_t qn_a;
  uint32_t col4;
  uint32_t stage;
};
struct Tile { bf16x8 a[4], ef; };
struct Quads { uint32_t q0, q1; };

DI void issue_tile(const Lane& L, int tile, int buf, __amdgpu_buffer_rsrc_t srd) {
  const u32x2 r0 = *(LDSP(const u32x2))(L.rec_a + tile * 128);
  const u32x2 r1 = *(LDSP(const u32x2))(L.rec_a + tile * 128 + 64);
  const uint32_t a0 = madlo(r0[0], 128u, L.cA), a1 = madlo(r0[1], 128u, L.cB);
  const uint32_t a2 = madlo(r1[0], 128u, L.cA), a3 = madlo(r1[1], 128u, L.cB);
  const uint32_t sb = L.stage + buf * 4096;
  dma16(sb, a0, srd); dma16(sb + 1024, a1, srd); dma16(sb + 2048, a2, srd); dma16(sb + 3072, a3, srd);
}
template <int BUF> DI void load_tile(Tile& T, Quads& Q, const Lane& L, int tile) {
  const uint32_t erec = *(LDSP(const uint32_t))(L.erec + tile * 128);
  Q.q0 = *(LDSP(const uint32_t))(L.qn_a + tile * 16);
  Q.q1 = *(LDSP(const uint32_t))(L.qn_a + tile * 16 + 8);
#pragma unroll
  for (int s = 0; s < 4; s++) T.a[s] = *(LDSP(const bf16x8))(L.afrag[s] + BUF * 4096);
  const u32x4 eu = {__builtin_amdgcn_perm(erec, erec, L.esel), 0u, 0u, 0u};
  T.ef = __builtin_bit_cast(bf16x8, eu);
}
DI void mma(Acc& A, const Tile& T, const Wts& W, int c) {
  f32x16 zero;
#pragma unroll
  for (int i = 0; i < 16; i++) zero[i] = 0.f;
  A.z  = mfma(T.a[0], W.b[c][0], zero);
  A.hv = mfma(T.a[0], W.v[c][0], zero);
#pragma unroll
  for (int s = 1; s < 4; s++) { A.z = mfma(T.a[s], W.b[c][s], A.z); A.hv = mfma(T.a[s], W.v[c][s], A.hv); }
  A.z  = mfma(T.ef, W.c[c], A.z);
}
struct Gate { f32x2 e0, e1; };
struct Rows { uint32_t a[4]; };
DI void rows_of(Rows& R, const Lane& L, const Quads& Q) {
  R.a[0] = madlo_i<1>(Q.q0, L.col4); R.a[1] = madhi_i<1>(Q.q0, L.col4); R.a[2] = madlo_i<1>(Q.q1, L.col4); R.a[3] = madhi_i<1>(Q.q1, L.col4);
}
DI void load_gate(Gate& G, const Rows& R, int c) {
  G.e0 = f32x2{*(LDSP(const float))(R.a[0] + 128 * c), *(LDSP(const float))(R.a[1] + 128 * c)};
  G.e1 = f32x2{*(LDSP(const float))(R.a[2] + 128 * c), *(LDSP(const float))(R.a[3] + 128 * c)};
}
constexpr int AGG_REL = L_AGG - L_EA;
#define PY 2
#define PYY PY
#define PXO 0
#define PXY 1
#define PRIO_HI() do { if (YOUNG) __builtin_amdgcn_s_setprio(PYY); else __builtin_amdgcn_s_setprio(PY); SB(); } while (0)
#define PRIO_LO() do { if (YOUNG) __builtin_amdgcn_s_setprio(PXY); else __builtin_amdgcn_s_setprio(PXO); SB(); } while (0)
#define PIN(x) asm volatile("" : "+v"(x))
#define EX(z, i) z[i] = __builtin_amdgcn_exp2f(z[i])
template <class F0, class F1>
DI void section_x(Acc& An, const Tile& T, const Wts& W, int cn, f32x16& z, Frac& F, f32x2& r0, f32x2& r1,
                  Gate& G, const Rows& Rg, int cg, const Rows& Ra, int ca, f32x2 m0, f32x2 m1, F0 extra0, F1 extra1) {
  f32x16 zero;
#pragma unroll
  for (int i = 0; i < 16; i++) zero[i] = 0.f;
  An.z = mfma(T.a[0], W.b[cn][0], zero); PIN(An.z);
  load_gate(G, Rg, cg);
  EX(z, 0); EX(z, 1); EX(z, 2);
  SB();
  An.hv = mfma(T.a[0], W.v[cn][0], zero); PIN(An.hv);
  lds_add_u32(Ra.a[0] + AGG_REL + 128 * ca, m0[0]); lds_add_u32(Ra.a[1] + AGG_REL + 128 * ca, m0[1]);
  EX(z, 3); EX(z, 4);
  SB();
  An.z = mfma(T.a[1], W.b[cn][1], An.z); PIN(An.z);
  lds_add_u32(Ra.a[2] + AGG_REL + 128 * ca, m1[0]); lds_add_u32(Ra.a[3] + AGG_REL + 128 * ca, m1[1]);
  EX(z, 5); EX(z, 6);
  SB();
  An.hv = mfma(T.a[1], W.v[cn][1], An.hv); PIN(An.hv);
  EX(z, 7); EX(z, 8);
  extra0();
  SB();
  An.z = mfma(T.a[2], W.b[cn][2], An.z); PIN(An.z);
  EX(z, 9); EX(z, 10);
  extra1();
  SB();
  An.hv = mfma(T.a[2], W.v[cn][2], An.hv); PIN(An.hv);
  EX(z, 11); EX(z, 12); EX(z, 13);
  SB();
  An.z = mfma(T.a[3], W.b[cn][3], An.z); PIN(An.z);
  EX(z, 14); EX(z, 15);
  SB();
  An.hv = mfma(T.a[3], W.v[cn][3], An.hv); PIN(An.hv);
  r0[0] = __builtin_amdgcn_rcpf(F.d[0][0]); r0[1] = __builtin_amdgcn_rcpf(F.d[0][1]); PIN(r0);
  SB();
  An.z = mfma(T.ef, W.c[cn], An.z); PIN(An.z);
  r1[0] = __builtin_amdgcn_rcpf(F.d[1][0]); r1[1] = __builtin_amdgcn_rcpf(F.d[1][1]); PIN(r1);
  PIN(G.e0); PIN(G.e1);
  SB();
}
DI void section_y(Frac& F, f32x2& m0, f32x2& m1, f32x2 r0, f32x2 r1, const f32x16& u, const f32x16& hv, const Gate& G, f32x2 magic) {
  const f32x2 one = {1.f, 1.f};
  f32x2 a[2][4], d01[2], d23[2], n01[2], n23[2];
  a[0][1] = fma2(G.e0, pair(u, 2), one);  a[1][1] = fma2(G.e1, pair(u, 10), one); SB();
  a[0][3] = fma2(G.e0, pair(u, 6), one);  a[1][3] = fma2(G.e1, pair(u, 14), one); SB();
  a[0][0] = fma2(G.e0, pair(u, 0), one);  a[1][0] = fma2(G.e1, pair(u, 8), one); SB();
  a[0][2] = fma2(G.e0, pair(u, 4), one);  a[1][2] = fma2(G.e1, pair(u, 12), one); SB();
  n01[0] = pair(hv, 0) * a[0][1]; n01[1] = pair(hv, 8) * a[1][1]; SB();
  n23[0] = pair(hv, 4) * a[0][3]; n23[1] = pair(hv, 12) * a[1][3]; SB();
  m0 = fma2(F.n[0], r0, magic); m1 = fma2(F.n[1], r1, magic); SB();
  d01[0] = a[0][0] * a[0][1]; d01[1] = a[1][0] * a[1][1]; SB();
  d23[0] = a[0][2] * a[0][3]; d23[1] = a[1][2] * a[1][3]; SB();
  n01[0] = fma2(pair(hv, 2), a[0][0], n01[0]); n01[1] = fma2(pair(hv, 10), a[1][0], n01[1]); SB();
  n23[0] = fma2(pair(hv, 6), a[0][2], n23[0]); n23[1] = fma2(pair(hv, 14), a[1][2], n23[1]); SB();
  F.d[0] = d01[0] * d23[0]; F.d[1] = d01[1] * d23[1]; SB();
  n01[0] = n01[0] * d23[0]; n01[1] = n01[1] * d23[1]; SB();
  n23[0] = n23[0] * d01[0]; n23[1] = n23[1] * d01[1]; SB();
  F.n[0] = n01[0] + n23[0]; F.n[1] = n01[1] + n23[1];
}
struct Pipe {
  Tile T; Acc A0; Rows R, RP;
  Frac F;
  f32x2 m0, m1;
  u32x2 ra, rb;
};
template <int BUF, bool YOUNG>
DI void step_fast(const Lane& L, const Wts& W, Pipe& P, int t1, int t3, f32x2 magic, __amdgpu_buffer_rsrc_t srd) {
  Acc A1; Gate G; f32x2 r0, r1; Quads QN; Rows RN; uint32_t erec, da[4];
  const uint32_t sb = L.stage + BUF * 4096;
  section_x(A1, P.T, W, 1, P.A0.z, P.F, r0, r1, G, P.R, 0, P.RP, 0, P.m0, P.m1,
            [&]() { da[0] = madlo(P.ra[0], 128u, L.cA); da[1] = madlo(P.ra[1], 128u, L.cB); },
            [&]() { da[2] = madlo(P.rb[0], 128u, L.cA); da[3] = madlo(P.rb[1], 128u, L.cB); });
  asm volatile("s_waitcnt vmcnt(0)" ::: "memory");
#pragma unroll
  for (int s = 0; s < 4; s++) P.T.a[s] = *(LDSP(const bf16x8))(L.afrag[s] + (BUF ^ 1) * 4096);
  erec  = *(LDSP(const uint32_t))(L.erec + t1 * 128);
  QN.q0 = *(LDSP(const uint32_t))(L.qn_a + t1 * 16);
  QN.q1 = *(LDSP(const uint32_t))(L.qn_a + t1 * 16 + 8);
  dma16(sb, da[0], srd);
  dma16(sb + 1024, da[1], srd);
  SB();
  PRIO_HI();
  section_y(P.F, P.m0, P.m1, r0, r1, P.A0.z, P.A0.hv, G, magic);
  SB();
  PRIO_LO();
  dma16(sb + 2048, da[2], srd);
  dma16(sb + 3072, da[3], srd);
  SB();
  section_x(P.A0, P.T, W, 0, A1.z, P.F, r0, r1, G, P.R, 1, P.RP, 1, P.m0, P.m1,
            [&]() { rows_of(RN, L, QN); },
            [&]() { const u32x4 eu = {__builtin_amdgcn_perm(erec, erec, L.esel), 0u, 0u, 0u}; P.T.ef = __builtin_bit_cast(bf16x8, eu); });
  P.ra = *(LDSP(const u32x2))(L.rec_a + t3 * 128);
  P.rb = *(LDSP(const u32x2))(L.rec_a + t3 * 128 + 64);
  SB();
  PRIO_HI();
  section_y(P.F, P.m0, P.m1, r0, r1, A1.z, A1.hv, G, magic);
  P.RP = P.R; P.R = RN;
  SB();
  PRIO_LO();
}
template <bool YOUNG> DI void edge_loop_fast(const Lane& L, const Wts& W, int wv, int ntile, f32x2 magic, __amdgpu_buffer_rsrc_t srd, unsigned long long* dbg) {
#define ST(i) do { } while (0)
  int tile = wv;
  if (tile < ntile) {
    Pipe P; Quads Q;
    if (YOUNG) __builtin_amdgcn_s_setprio(PXY); else __builtin_amdgcn_s_setprio(PXO);
    ST(0);
    asm volatile("s_waitcnt vmcnt(0)" ::: "memory");
    ST(1);
    issue_tile(L, min(tile + 8, ntile), 1, srd);
    load_tile<0>(P.T, Q, L, tile);
    rows_of(P.R, L, Q);
    mma(P.A0, P.T, W, 0);
    Q.q0 = Q.q1 = (98u * 256u) * 0x10001u;
    rows_of(P.RP, L, Q);
    P.F.n[0] = P.F.n[1] = f32x2{0.f, 0.f}; P.m0 = P.m1 = magic;
    P.F.d[0] = P.F.d[1] = f32x2{1.f, 1.f};
    { const int t2 = min(tile + 16, ntile);
      P.ra = *(LDSP(const u32x2))(L.rec_a + t2 * 128); P.rb = *(LDSP(const u32x2))(L.rec_a + t2 * 128 + 64); }
    ST(2);
    while (true) {
      step_fast<0, YOUNG>(L, W, P, min(tile + 8, ntile), min(tile + 24, ntile), magic, srd);
      tile += 8;
      if (tile >= ntile) break;
      step_fast<1, YOUNG>(L, W, P, min(tile + 8, ntile), min(tile + 24, ntile), magic, srd);
      tile += 8;
      if (tile >= ntile) break;
    }
    ST(3);
    const f32x2 z0 = fma2(P.F.n[0], rcpx2(P.F.d[0]), magic), z1 = fma2(P.F.n[1], rcpx2(P.F.d[1]), magic);
    lds_add_u32(P.RP.a[0] + AGG_REL, P.m0[0]); lds_add_u32(P.RP.a[1] + AGG_REL, P.m0[1]);
    lds_add_u32(P.RP.a[2] + AGG_REL, P.m1[0]); lds_add_u32(P.RP.a[3] + AGG_REL, P.m1[1]);
    lds_add_u32(P.RP.a[0] + AGG_REL + 128, z0[0]); lds_add_u32(P.RP.a[1] + AGG_REL + 128, z0[1]);
    lds_add_u32(P.RP.a[2] + AGG_REL + 128, z1[0]); lds_add_u32(P.RP.a[3] + AGG_REL + 128, z1[1]);
    asm volatile("s_waitcnt vmcnt(0)" ::: "memory");
    __builtin_amdgcn_s_setprio(0);
    ST(4);
  }
}
template <bool EXPEA> DI void edge_loop_plain(const Lane& L, const Wts& W, int wv, int ntile, f32x2 magic, __amdgpu_buffer_rsrc_t srd) {
  for (int tile = wv; tile < ntile; tile += 8) {
    Tile T; Quads Q;
    issue_tile(L, tile, 0, srd);
    asm volatile("s_waitcnt vmcnt(0)" ::: "memory");
    load_tile<0>(T, Q, L, tile);
#pragma unroll
    for (int c = 0; c < 2; c++) {
      Acc A; Gate G; Rows Rw;
      rows_of(Rw, L, Q);
      load_gate(G, Rw, c);
      mma(A, T, W, c);
      const f32x2 m0 = octet_plain<EXPEA>(A, 0, G.e0), m1 = octet_plain<EXPEA>(A, 1, G.e1);
      if (EXPEA) {
        const f32x2 i0 = m0 + magic, i1 = m1 + magic;
        lds_add_u32(Rw.a[0] + AGG_REL + 128 * c, i0[0]); lds_add_u32(Rw.a[1] + AGG_REL + 128 * c, i0[1]);
        lds_add_u32(Rw.a[2] + AGG_REL + 128 * c, i1[0]); lds_add_u32(Rw.a[3] + AGG_REL + 128 * c, i1[1]);
      } else {
        lds_add_f32(Rw.a[0] + AGG_REL + 128 * c, m0[0]); lds_add_f32(Rw.a[1] + AGG_REL + 128 * c, m0[1]);
        lds_add_f32(Rw.a[2] + AGG_REL + 128 * c, m1[0]); lds_add_f32(Rw.a[3] + AGG_REL + 128 * c, m1[1]);
      }
    }
  }
}

struct Recs { u32x4 rc[4]; uint32_t lc[4]; };
DI void load_recs(Recs& R, const uint32_t* sg, const char* ws, int g, int j, int done) {
  const __amdgpu_buffer_rsrc_t rsrd = __builtin_amdgcn_make_buffer_rsrc((void*)(ws + WS_REC), (short)0, NWG * CHB * 20, 0x00020000);
#pragma unroll
  for (int p = 0; p < 2; p++) {
#pragma unroll
    for (int r = 0; r < 2; r++) {
      uint32_t off = (uint32_t)((g + 128 * r) * (CHB * 20) + ((int)(sg[r] & 0xffff) + (done >> 2) + 4 * p + j) * 20);
      if ((int)(sg[r] >> 16) - done <= 16 * p + 4 * j) off = 0x40000000u;
      asm volatile("buffer_load_dwordx4 %0, %1, %2, 0 offen sc1" : "=&v"(R.rc[2 * r + p]) : "v"(off), "s"(rsrd) : "memory");
      asm volatile("buffer_load_dword %0, %1, %2, 0 offen offset:16 sc1" : "=&v"(R.lc[2 * r + p]) : "v"(off), "s"(rsrd) : "memory");
    }
  }
}
#define WAIT_RECS(R) asm volatile("s_waitcnt vmcnt(0)" : "+v"(R.rc[0]), "+v"(R.rc[1]), "+v"(R.rc[2]), "+v"(R.rc[3]), \
                                  "+v"(R.lc[0]), "+v"(R.lc[1]), "+v"(R.lc[2]), "+v"(R.lc[3]) :: "memory")

__global__ __launch_bounds__(512, 2) void k_mpnn(const int* __restrict__ src, const int* __restrict__ dst,
                                                 const uint16_t* __restrict__ ea, const uint16_t* __restrict__ x,
                                                 const uint16_t* __restrict__ uw, const uint16_t* __restrict__ vw,
                                                 const uint16_t* __restrict__ gw, char* __restrict__ ws,
                                                 uint16_t* __restrict__ out, int N, int E) {
  extern __shared__ __attribute__((aligned(128))) char smem[];
  const uint32_t lb = (uint32_t)(uintptr_t)(LDSP(char))smem;
  uint32_t* misc = (uint32_t*)(smem + L_MISC);
  const int t = threadIdx.x, lane = t & 63;
  const int w = blockIdx.x;
  const int b = (blockIdx.x & 7) * 32 + (blockIdx.x >> 3);
  const int wv = __builtin_amdgcn_readfirstlane(t >> 6);
  const int r31 = lane & 31, h = lane >> 5;
  TS_DECL; TS(0);

  const __amdgpu_buffer_rsrc_t srd = __builtin_amdgcn_make_buffer_rsrc((void*)x, (short)0, N * 128, 0x00020000);
  const int e0 = w * CH;
  const int n  = min(CH, E - e0);
  const int cnt = min(max(n - 8 * t, 0), 8);
  u32x4 d4[2], s4[2], e4; u32x2 xn;
  {
    const __amdgpu_buffer_rsrc_t dsrd = __builtin_amdgcn_make_buffer_rsrc((void*)dst, (short)0, E * 4, 0x00020000);
    const __amdgpu_buffer_rsrc_t ssrd = __builtin_amdgcn_make_buffer_rsrc((void*)src, (short)0, E * 4, 0x00020000);
    const __amdgpu_buffer_rsrc_t esrd = __builtin_amdgcn_make_buffer_rsrc((void*)ea, (short)0, (E * 2 + 3) & ~3, 0x00020000);
    const uint32_t eo = cnt > 0 ? (uint32_t)(e0 + 8 * t) : 0x20000000u;
    asm volatile("buffer_load_dwordx4 %0, %1, %2, 0 offen" : "=&v"(d4[0]) : "v"(eo * 4), "s"(dsrd) : "memory");
    asm volatile("buffer_load_dwordx4 %0, %1, %2, 0 offen offset:16" : "=&v"(d4[1]) : "v"(eo * 4), "s"(dsrd) : "memory");
    asm volatile("buffer_load_dwordx4 %0, %1, %2, 0 offen" : "=&v"(s4[0]) : "v"(eo * 4), "s"(ssrd) : "memory");
    asm volatile("buffer_load_dwordx4 %0, %1, %2, 0 offen offset:16" : "=&v"(s4[1]) : "v"(eo * 4), "s"(ssrd) : "memory");
    asm volatile("buffer_load_dwordx4 %0, %1, %2, 0 offen" : "=&v"(e4) : "v"(eo * 2), "s"(esrd) : "memory");
    asm volatile("global_load_dwordx2 %0, %1, %2 sc1" : "=&v"(xn) : "v"(0), "s"(ws + WS_NONCE) : "memory");
  }
  uint32_t* hist = (uint32_t*)(smem + A_HIST);
  uint32_t* base = (uint32_t*)(smem + A_BASE);
  if (t < 256) hist[t] = 0;
  else if (t < 384) { *(uint32_t*)(smem + L_CNT + (t - 256) * 4) = 0; *(uint32_t*)(smem + L_NQ + (t - 256) * 4) = 0; }
  else { misc[t - 384] = 0; if (t < 448) *(float*)(smem + L_EA + 98 * 256 + (t - 384) * 4) = 0.f; }
  LDS_BARRIER();
  asm volatile("s_waitcnt vmcnt(0)" : "+v"(xn), "+v"(d4[0]), "+v"(d4[1]), "+v"(s4[0]), "+v"(s4[1]), "+v"(e4) :: "memory");
  const uint32_t tag32 = xn[0] + 1u;
  const u64 tag40 = ((((u64)xn[1] << 32) | xn[0]) + 1ull) & 0xffffffffffull;
  uint32_t bk[8], rk0[8];
#pragma unroll
  for (int k = 0; k < 8; k++) {
    bk[k] = (d4[k >> 2][k & 3] * 21400u) >> 21;
    rk0[k] = 0;
    if (k < cnt) rk0[k] = __hip_atomic_fetch_add(&hist[bk[k]], 1u, RLX_WG);
  }
  TS(1);
  u32x4 wb; uint32_t c0, c1;
  asm volatile("global_load_dwordx4 %0, %1, %2" : "=&v"(wb) : "v"(t * 16), "s"(gw + 64 * 64) : "memory");
  asm volatile("global_load_ushort %0, %1, %2" : "=&v"(c0) : "v"(r31 * 2), "s"(gw + 128 * 64) : "memory");
  asm volatile("global_load_ushort %0, %1, %2 offset:64" : "=&v"(c1) : "v"(r31 * 2), "s"(gw + 128 * 64) : "memory");
  LDS_BARRIER();
  if (wv == 7) {
    const u32x4 c = *(const u32x4*)(hist + 4 * lane);
    const u32x4 p = (c + 3) >> 2;
    const uint32_t s1 = p[0] + p[1], s2 = s1 + p[2], tot = s2 + p[3];
    const uint32_t ex = wave_scan_add(tot) - tot;
    *(u32x4*)(base + 4 * lane) = u32x4{ex, ex + p[0], ex + s1, ex + s2};
  } else {
    const __amdgpu_buffer_rsrc_t asrd = __builtin_amdgcn_make_buffer_rsrc((void*)gw, (short)0, 8192, 0x00020000);
    const __amdgpu_buffer_rsrc_t usrd = __builtin_amdgcn_make_buffer_rsrc((void*)uw, (short)0, 8192, 0x00020000);
    const __amdgpu_buffer_rsrc_t vsrd = __builtin_amdgcn_make_buffer_rsrc((void*)vw, (short)0, 8192, 0x00020000);
    const uint32_t cA = (uint32_t)(((lane & 7) ^ (lane >> 4)) * 16);
#pragma unroll
    for (int i = 0; i < 6; i++) {
      const int p = wv + 7 * i;
      const uint32_t wo = (lane * 16) ^ ((lane & 16) << 2);
      if (p < 8) dma16(lb + A_W + p * 1024, p * 1024 + wo, asrd);
      else if (p < 16) dma16(lb + A_W + 16384 + (p - 8) * 1024, (p - 8) * 1024 + wo, usrd);
      else if (p < 24) dma16(lb + A_W + 24576 + (p - 16) * 1024, (p - 16) * 1024 + wo, vsrd);
      else if (p < 37) { const int q = p - 24; dma16(lb + P_X + q * 1024, (b * NPB + 8 * q + (lane >> 3)) * 128 + ((q & 1) ? cA ^ 64u : cA), srd); }
    }
  }
  {
    uint32_t m2 = 0;
#pragma unroll
    for (int k = 0; k < 4; k++) m2 = max(m2, max(e4[k] & 0x7fffu, (e4[k] >> 16) & 0x7fffu));
    m2 = wave_max_u32(m2 << 16);
    if (lane == 63) __hip_atomic_fetch_max(&misc[M_MX + 2], m2, RLX_WG);
  }
  LDS_BARRIER();
#pragma unroll
  for (int k = 0; k < 8; k++) {
    if (k < cnt) {
      const uint32_t p = base[bk[k]] * 4 + rk0[k];
      const uint32_t o = (p >> 2) * 20 + (p & 3);
      const uint32_t ev = (k & 1) ? e4[k >> 1] & 0xffff0000u : e4[k >> 1] << 16;
      *(uint32_t*)(smem + A_ST + o + (p & 3) * 3) = (s4[k >> 2][k & 3] & 0xffffu) | ev;
      *(uint8_t*)(smem + A_ST + o + 16) = (uint8_t)(d4[k >> 2][k & 3] - bk[k] * NPB);
    }
  }
  LDS_BARRIER();
  TS(2);
  {
    const __amdgpu_buffer_rsrc_t rsrd = __builtin_amdgcn_make_buffer_rsrc((void*)(ws + WS_REC + (size_t)w * CHB * 20), (short)0, CHB * 20, 0x00020000);
    const uint32_t nb = base[255] + ((hist[255] + 3) >> 2);
#pragma unroll
    for (int i = 0; i < 3; i++) {
      const int c = t + 512 * i;
      if (c * 16 < (int)nb * 20) __builtin_amdgcn_raw_buffer_store_b128(*(const u32x4*)(smem + A_ST + c * 16), rsrd, c * 16, 0, AUX_SC1);
    }
    asm volatile("s_waitcnt vmcnt(0)" : "+v"(wb), "+v"(c0), "+v"(c1) :: "memory");
#pragma unroll
    for (int k = 0; k < 4; k++) wb[k] = cvtpk(bf2f(wb[k] & 0xffffu) * NLOG2E, __uint_as_float(wb[k] & 0xffff0000u) * NLOG2E);
    *(u32x4*)(smem + A_W + 8192 + ((t * 16) ^ ((t & 16) << 2))) = wb;
    if (arrive(&misc[M_CW], lane) == 7) {
      const u32x4 c = *(const u32x4*)(hist + 4 * lane), o = *(const u32x4*)(base + 4 * lane);
      gu64* g = (gu64*)(ws + WS_SEG) + w * NWG + 4 * lane;
#pragma unroll
      for (int k = 0; k < 4; k++) __hip_atomic_store(g + k, (u64)(o[k] | (c[k] << 12)) | (tag40 << 24), RLX_AGENT);
      if (lane == 0) TS_PUB(0);
    }
  }
  await_lds(&misc[M_CW], 8u);
  Wts W;
  const uint32_t troff = lb + A_W + (8 * h + ((lane & 15) >> 2)) * 128 + (16 * ((lane >> 4) & 1) + 4 * (lane & 3)) * 2, sw = (lane & 8) << 3;
  auto weights = [&](auto cc) {
    constexpr int c = decltype(cc)::value;
#pragma unroll
    for (int s = 0; s < 4; s++) {
      W.b[c][s] = tr_frag(troff + 8192 + s * 2048 + ((c * 64) ^ sw), 512);
      W.v[c][s] = tr_frag(troff + 24576 + s * 2048 + ((c * 64) ^ sw), 512);
    }
  };
  {
    const int rt = wv >> 1, row = 32 * rt + r31;
    const uint32_t xa = lb + P_X + row * 128 + ((h ^ ((row >> 1) & 7)) * 16);
    uint32_t xam = 0, mb = 0, mv = 0; float s2 = 0.f;
    auto job = [&](auto cc) {
      constexpr int c = decltype(cc)::value;
      bf16x8 a[4], fa[4], fu[4];
#pragma unroll
      for (int s = 0; s < 4; s++) {
        a[s] = *(LDSP(const bf16x8))(xa ^ (32u * s));
        fa[s] = tr_frag(troff + s * 2048 + ((c * 64) ^ sw), 512);
        fu[s] = tr_frag(troff + 16384 + s * 2048 + ((c * 64) ^ sw), 512);
      }
      weights(cc);
      f32x16 za, zu, zb, zv;
#pragma unroll
      for (int i = 0; i < 16; i++) { za[i] = 0.f; zu[i] = 0.f; zb[i] = 0.f; zv[i] = 0.f; }
#pragma unroll
      for (int s = 0; s < 4; s++) { za = mfma(a[s], fa[s], za); zu = mfma(a[s], fu[s], zu); zb = mfma(a[s], W.b[c][s], zb); zv = mfma(a[s], W.v[c][s], zv); }
      const uint32_t o = lb + L_EA + (32 * rt + 4 * h) * 256 + (32 * c + r31) * 4;
      if (rt < 3) {
#pragma unroll
        for (int i = 0; i < 16; i++) {
          const float v = za[i] * NLOG2E;
          *(LDSP(float))(o + crow(i, 0) * 256) = v; *(LDSP(float))(o + (L_XU - L_EA) + crow(i, 0) * 256) = zu[i];
          xam = max(xam, absbits(v)); mb = max(mb, absbits(zb[i])); mv = max(mv, absbits(zv[i])); s2 = fmaf(zv[i], zv[i], s2);
        }
      } else if (h == 0) {
#pragma unroll
        for (int i = 0; i < 2; i++) {
          const float v = za[i] * NLOG2E;
          *(LDSP(float))(o + i * 256) = v; *(LDSP(float))(o + (L_XU - L_EA) + i * 256) = zu[i];
          xam = max(xam, absbits(v)); mb = max(mb, absbits(zb[i])); mv = max(mv, absbits(zv[i])); s2 = fmaf(zv[i], zv[i], s2);
        }
      }
    };
    if (wv & 1) job(IC<1>{}); else job(IC<0>{});
    xam = wave_max_u32(xam); mb = wave_max_u32(mb); mv = wave_max_u32(mv); s2 = wave_sum_f32(s2);
    const uint32_t cm = wave_max_u32(max(absbits(bf2f(c0) * NLOG2E), absbits(bf2f(c1) * NLOG2E)));
    if (lane == 63) {
      __hip_atomic_fetch_max(&misc[M_XAM], xam, RLX_WG); __hip_atomic_fetch_max(&misc[M_MX], mb, RLX_WG);
      __hip_atomic_fetch_max(&misc[M_MX + 1], mv, RLX_WG); misc[M_MX + 3] = cm; misc[M_SQ + wv] = __float_as_uint(s2);
    }
    if (arrive(&misc[M_CB], lane) == 7 && lane == 0) {
      const u32x4 m = *(const u32x4*)(misc + M_MX), q0 = *(const u32x4*)(misc + M_SQ), q1 = *(const u32x4*)(misc + M_SQ + 4);
      const float sq = ((__uint_as_float(q0[0]) + __uint_as_float(q0[1])) + (__uint_as_float(q0[2]) + __uint_as_float(q0[3])))
                     + ((__uint_as_float(q1[0]) + __uint_as_float(q1[1])) + (__uint_as_float(q1[2]) + __uint_as_float(q1[3])));
      gu64* g = (gu64*)(ws + WS_BND) + 4 * w;
      __hip_atomic_store(g,     (u64)(min((m[0] + 0xffffu) >> 16, 0x7fffu) | (min((m[1] + 0xffffu) >> 16, 0x7fffu) << 16)) | ((u64)tag32 << 32), RLX_AGENT);
      __hip_atomic_store(g + 1, (u64)__float_as_uint(sq) | ((u64)tag32 << 32), RLX_AGENT);
      __hip_atomic_store(g + 2, (u64)m[2] | ((u64)tag32 << 32), RLX_AGENT);
    }
  }
  TS(3);

  auto thread_id = [&]() {
    int v = wv * 64 + (int)__builtin_amdgcn_mbcnt_hi(~0u, __builtin_amdgcn_mbcnt_lo(~0u, 0u));
    asm volatile("" : "+v"(v));
    return v;
  };
  const int tb = thread_id(), lb_ = tb & 63;
  uint32_t sg[2] = {0, 0};
  auto poll_segments = [&](int g) {
    gu64* gs = (gu64*)(ws + WS_SEG) + g * NWG + b;
    for (unsigned spins = 0;; spins++) {
      const u64 v0 = __hip_atomic_load(gs, RLX_AGENT), v1 = __hip_atomic_load(gs + 128 * NWG, RLX_AGENT);
      const bool ok = (v0 >> 24) == tag40 && (v1 >> 24) == tag40;
      sg[0] = ((uint32_t)v0 & 0xfffu) | ((((uint32_t)v0 >> 12) & 0xfffu) << 16);
      sg[1] = ((uint32_t)v1 & 0xfffu) | ((((uint32_t)v1 >> 12) & 0xfffu) << 16);
      if (__all(ok)) break;
      if (spins > SPIN_CAP) { sg[0] = sg[1] = 0; break; }
      __builtin_amdgcn_s_sleep(8);
    }
  };
  Recs R;
  int done = 0;
  uint32_t rk[4][2]; bool part1[2] = {false, false};
  auto post_total = [&](int dn, int j, int l) {
    uint32_t v = 0;
    if (j == 0) v = min(max((int)(sg[0] >> 16) - dn, 0), 32) + min(max((int)(sg[1] >> 16) - dn, 0), 32);
    v = wave_scan_add(v);
    if (l == 63) __hip_atomic_fetch_add(&misc[M_TOT], v, RLX_WG);
  };
  auto rank = [&](int tt, int dn, int take) {
    const int j = tt & 3;
    const uint32_t spare = (98u + (uint32_t)(tt & 63) % 30u) * 0x01010101u;
    bool more = false;
#pragma unroll
    for (int i = 0; i < 4; i++) {
      const int len = (int)(sg[i >> 1] >> 16) - dn;
      more |= len > take;
      const int v = min(max(min(len, take) - 16 * (i & 1) - 4 * j, 0), 4);
      const uint32_t drop = (uint32_t)(0xffffffffull << (8 * v));
      R.lc[i] = (R.lc[i] & ~drop) | (spare & drop);
      if (i & 1) part1[i >> 1] = __any(v > 0);
    }
    uint32_t r[2][4];
#pragma unroll
    for (int st = 0; st < 2; st++)
#pragma unroll
      for (int k = 0; k < 4; k++) r[st][k] = __hip_atomic_fetch_add((LDSP(uint32_t))(L_CNT + byte_x4(R.lc[2 * st], k)), 1u, RLX_WG);
#pragma unroll
    for (int st = 0; st < 2; st++) {
      rk[2 * st][0] = r[st][0] | (r[st][1] << 16); rk[2 * st][1] = r[st][2] | (r[st][3] << 16);
      rk[2 * st + 1][0] = rk[2 * st + 1][1] = 0;
      if (part1[st]) {
        uint32_t q[4];
#pragma unroll
        for (int k = 0; k < 4; k++) q[k] = __hip_atomic_fetch_add((LDSP(uint32_t))(L_CNT + byte_x4(R.lc[2 * st + 1], k)), 1u, RLX_WG);
        rk[2 * st + 1][0] = q[0] | (q[1] << 16); rk[2 * st + 1][1] = q[2] | (q[3] << 16);
      }
    }
    if (__any(more) && (tt & 63) == 0) misc[M_MORE] = 1;
  };
  auto scan = [&](int ln_) {
    u32x2 c = *(const u32x2*)(smem + L_CNT + ln_ * 8);
    if (ln_ >= NPB / 2) c = u32x2{0, 0};
    const uint32_t p0 = (c[0] + 3) & ~3u, p1 = (c[1] + 3) & ~3u;
    const uint32_t inc = wave_scan_add(p0 + p1), ex = inc - (p0 + p1);
    *(u32x2*)(smem + L_OFF + ln_ * 8) = ln_ < NPB / 2 ? u32x2{ex, ex + p0} : u32x2{SORT_CAP, SORT_CAP};
    const uint32_t T4 = (uint32_t)__builtin_amdgcn_readlane((int)inc, 63);
    const uint32_t T32 = (T4 + 31) & ~31u;
    if (T4 + ln_ < T32 + 32) *(uint32_t*)(smem + L_SORT + (T4 + ln_) * 4) = DUMMY;
    if ((T4 >> 2) + ln_ < (T32 >> 2) + 8) *(uint16_t*)(smem + L_QN + ((T4 >> 2) + ln_) * 2) = QN_DUMMY;
    if (ln_ == 0) misc[M_NTILE] = T32 >> 5;
  };
  auto scatter = [&](int tt) {
#pragma unroll
    for (int i = 0; i < 4; i++) {
      if ((i & 1) == 0 || part1[i >> 1]) {
#pragma unroll
        for (int k = 0; k < 4; k++) {
          const uint32_t pos = min(*(LDSP(const uint32_t))(L_OFF + byte_x4(R.lc[i], k)) + ((rk[i][k >> 1] >> (16 * (k & 1))) & 0xffffu), (uint32_t)(SORT_CAP - 1));
          *(LDSP(uint32_t))(lb + L_SORT + pos * 4) = R.rc[i][k];
        }
      }
    }
    if (tt < NPB * 4) {
      const uint32_t nd = tt >> 2, k = tt & 3;
      const uint32_t c = *(const uint32_t*)(smem + L_CNT + nd * 4), o = *(const uint32_t*)(smem + L_OFF + nd * 4);
      for (uint32_t q = k; 4 * q < c; q += 4) *(uint16_t*)(smem + L_QN + (o >> 1) + q * 2) = (uint16_t)(nd << 8);
      if (k == 3) *(uint32_t*)(smem + L_NQ + nd * 4) += (c + 3) >> 2;
      if (k < 3 && c + k < ((c + 3) & ~3u)) *(uint32_t*)(smem + L_SORT + (o + c + k) * 4) = DUMMY + ((uint32_t)tt >> 12);
    }
  };
  auto lane_consts = [&](int tt) {
    Lane L;
    const int ln_ = tt & 63, g8_ = ln_ >> 3, p8_ = ln_ & 7, r31_ = ln_ & 31, h_ = ln_ >> 5;
    L.rec_a = lb + L_SORT + (2 * (g8_ >> 2) + (g8_ & 1)) * 16 + ((g8_ >> 1) & 1) * 8;
    L.cA = (uint32_t)((p8_ ^ (g8_ >> 1)) * 16); L.cB = L.cA ^ 64u;
    L.stage = lb + L_STAGE + wv * 8192;
    const uint32_t af = L.stage + r31_ * 128 + ((h_ ^ ((r31_ >> 1) & 7)) * 16);
#pragma unroll
    for (int s = 0; s < 4; s++) L.afrag[s] = af ^ (32u * s);
    const int o = r31_ >> 4, m1 = (r31_ >> 3) & 1, hh = (r31_ >> 2) & 1, m0 = (r31_ >> 1) & 1, ab = r31_ & 1;
    L.erec  = lb + L_SORT + (4 * (4 * o + 2 * hh + ab) + 2 * m0 + m1) * 4;
    L.esel  = h_ == 0 ? 0x03020302u : 0x0c0c0c0cu;
    L.qn_a  = lb + L_QN + h_ * 4;
    L.col4  = lb + L_EA + r31_ * 4;
    return L;
  };

  int take = 32;
  poll_segments(tb >> 2);
  TS(4);
  load_recs(R, sg, ws, tb >> 2, tb & 3, 0);
  post_total(0, tb & 3, lb_);
  {
    uint32_t v = (tb & 3) == 0 ? (sg[0] >> 16) + (sg[1] >> 16) : 0u;
    v = wave_scan_add(v);
    if (lb_ == 63) __hip_atomic_fetch_add(&misc[M_ALL], v, RLX_WG);
  }
  if (wv & 1) weights(IC<0>{}); else weights(IC<1>{});
#pragma unroll
  for (int c = 0; c < 2; c++) {
    const float cv = bf2f(c ? c1 : c0) * NLOG2E;
    const uint32_t chi = cvtpk(cv, 0.f);
    const uint32_t clo = cvtpk(cv - bf2f(chi), 0.f);
    const u32x4 cu = {h == 0 ? (chi | (clo << 16)) : 0u, 0u, 0u, 0u};
    W.c[c] = __builtin_bit_cast(bf16x8, cu);
  }
  WAIT_RECS(R);
  TS(5);
  rank(tb, 0, take);
  LDS_BARRIER();
  if (w == 0 && tb == 0) __hip_atomic_store((gu64*)(ws + WS_NONCE), (((u64)xn[1] << 32) | xn[0]) + 1ull, RLX_AGENT);
  if (__builtin_amdgcn_readfirstlane(misc[M_TOT]) > (uint32_t)TAKE32_MAX) {
    LDS_BARRIER();
    if (tb < 128) *(uint32_t*)(smem + L_CNT + tb * 4) = 0;
    LDS_BARRIER();
    take = 16;
    rank(tb, 0, take);
    LDS_BARRIER();
  }
  if (wv == 0) scan(lb_);
  else if (wv == 1) {
    gu64* gt = (gu64*)(ws + WS_BND) + 4 * lb_;
    uint32_t mb, mv_, me; float sq;
    for (unsigned spins = 0;; spins++) {
      bool ok = true; mb = mv_ = me = 0; sq = 0.f;
#pragma unroll
      for (int i = 0; i < 4; i++) {
        const u64 v0 = __hip_atomic_load(gt + 256 * i, RLX_AGENT), v1 = __hip_atomic_load(gt + 256 * i + 1, RLX_AGENT), v2 = __hip_atomic_load(gt + 256 * i + 2, RLX_AGENT);
        ok &= (uint32_t)(v0 >> 32) == tag32 && (uint32_t)(v1 >> 32) == tag32 && (uint32_t)(v2 >> 32) == tag32;
        mb = max(mb, (uint32_t)v0 << 16); mv_ = max(mv_, (uint32_t)v0 & 0xffff0000u);
        me = max(me, (uint32_t)v2); sq += __uint_as_float((uint32_t)v1);
      }
      if (__all(ok)) break;
      if (spins > SPIN_CAP) break;
      __builtin_amdgcn_s_sleep(8);
    }
    mb = wave_max_u32(mb); mv_ = wave_max_u32(mv_); me = wave_max_u32(me); sq = wave_sum_f32(sq);
    const float mv = __uint_as_float(mv_);
    const float yb = (__uint_as_float(misc[M_XAM]) + __uint_as_float(mb) + __uint_as_float(me) * __uint_as_float(misc[M_MX + 3])) * 1.01f;
    const float all = (float)(misc[M_ALL] + 4u);
    const int p1 = (int)(__float_as_uint(4.1f * mv) >> 23) - 127 + 2, p2 = (int)(__float_as_uint(all * mv * 1.01f) >> 23) - 127 + 1 - 8;
    const int p = max(max(p1, p2), -80);
    const float dl = __uint_as_float((uint32_t)(p - 23 + 127) << 23);
    const bool fine = dl * 4096.f <= __builtin_amdgcn_sqrtf(sq * __builtin_amdgcn_rcpf(64.f * (float)N)) * 0.99f || mv == 0.f;
    if (lb_ == 63) {
      misc[M_FAST] = (yb <= 28.f) && (mv <= 1.0e12f) && fine;
      misc[M_EXP] = (uint32_t)(p + 127);
    }
  } else {
    for (int i = tb - 128; i < 99 * 16; i += 384) *(u32x4*)(smem + L_AGG + i * 16) = u32x4{0, 0, 0, 0};
  }
  LDS_BARRIER();
  TS(6);
  const bool fast = __builtin_amdgcn_readfirstlane(misc[M_FAST]);
  float delta; f32x2 magic;
  {
    const uint32_t pexp = __builtin_amdgcn_readfirstlane(misc[M_EXP]);
    delta = __uint_as_float((pexp - 23u) << 23);
    const float mg = __uint_as_float((pexp << 23) | 0x400000u);
    magic = f32x2{mg, mg};
  }
  scatter(tb);
  int again = __builtin_amdgcn_readfirstlane(misc[M_MORE]);
  LDS_BARRIER();
  int ntile = __builtin_amdgcn_readfirstlane(misc[M_NTILE]);
  if (fast) {
    const Lane L = lane_consts(tb);
    issue_tile(L, min(wv, ntile), 0, srd);
    for (int i = tb; i < NPB * 16; i += 512) {
      f32x4 v = *(const f32x4*)(smem + L_EA + i * 16);
      v[0] = __builtin_amdgcn_exp2f(v[0]); v[1] = __builtin_amdgcn_exp2f(v[1]); v[2] = __builtin_amdgcn_exp2f(v[2]); v[3] = __builtin_amdgcn_exp2f(v[3]);
      *(f32x4*)(smem + L_EA + i * 16) = v;
    }
    LDS_BARRIER();
    TS(7);
    if (wv < 4) edge_loop_fast<false>(L, W, wv, ntile, magic, srd, (unsigned long long*)(ws + WS_DBG));
    else        edge_loop_fast<true>(L, W, wv, ntile, magic, srd, (unsigned long long*)(ws + WS_DBG));
  } else {
    const Lane L = lane_consts(thread_id());
    edge_loop_plain<false>(L, W, wv, ntile, magic, srd);
  }
  while (again) {
    done += take;
    const int t2 = thread_id();
    poll_segments(t2 >> 2);
    load_recs(R, sg, ws, t2 >> 2, t2 & 3, done);
    WAIT_RECS(R);
    LDS_BARRIER();
    if (t2 < 128) *(uint32_t*)(smem + L_CNT + t2 * 4) = 0;
    if (t2 == 0) { misc[M_MORE] = 0; misc[M_TOT] = 0; }
    LDS_BARRIER();
    post_total(done, t2 & 3, t2 & 63);
    LDS_BARRIER();
    take = __builtin_amdgcn_readfirstlane(misc[M_TOT]) <= (uint32_t)TAKE32_MAX ? 32 : 16;
    rank(t2, done, take);
    LDS_BARRIER();
    if (wv == 0) scan(t2 & 63);
    LDS_BARRIER();
    scatter(t2);
    again = __builtin_amdgcn_readfirstlane(misc[M_MORE]);
    LDS_BARRIER();
    ntile = __builtin_amdgcn_readfirstlane(misc[M_NTILE]);
    const Lane L = lane_consts(t2);
    if (fast) edge_loop_plain<true>(L, W, wv, ntile, magic, srd);
    else      edge_loop_plain<false>(L, W, wv, ntile, magic, srd);
  }
  LDS_BARRIER();

  const int te = thread_id();
  for (int i = te; i < NPB * 8; i += 512) {
    const int node = b * NPB + (i >> 3);
    if (node < N) {
      float y[8];
      const uint32_t bias = *(const uint32_t*)(smem + L_NQ + (i >> 3) * 4) * __float_as_uint(magic[0]);
#pragma unroll
      for (int k = 0; k < 2; k++) {
        const u32x4 a4 = *(const u32x4*)(smem + L_AGG + i * 32 + k * 16);
        const f32x4 u4 = *(const f32x4*)(smem + L_XU + i * 32 + k * 16);
#pragma unroll
        for (int j = 0; j < 4; j++) {
          const float v = u4[j] + (fast ? (float)(int)(a4[j] - bias) * delta : __uint_as_float(a4[j]));
          y[4 * k + j] = v * __builtin_amdgcn_rcpf(1.f + __builtin_amdgcn_exp2f(v * NLOG2E));
        }
      }
      const u32x4 o = {cvtpk(y[0], y[1]), cvtpk(y[2], y[3]), cvtpk(y[4], y[5]), cvtpk(y[6], y[7])};
      *(u32x4*)(out + (size_t)node * 64 + (i & 7) * 8) = o;
    }
  }
  TS_OUT(256 + blockIdx.x);
}

}

extern "C" void kernel_launch(void* const* d_in, const int* in_sizes, int n_in,
                              void* d_out, int out_size, void* d_ws, size_t ws_size,
                              hipStream_t stream) {
  const uint16_t* x  = (const uint16_t*)d_in[0];
  const uint16_t* ea = (const uint16_t*)d_in[1];
  const int*      src = (const int*)d_in[2];
  const int*      dst = (const int*)d_in[3];
  const uint16_t* uw = (const uint16_t*)d_in[4];
  const uint16_t* vw = (const uint16_t*)d_in[5];
  const uint16_t* gw = (const uint16_t*)d_in[6];
  const int N = in_sizes[0] / 64, E = in_sizes[2];
  char* ws = (char*)d_ws;
  (void)hipFuncSetAttribute((const void*)k_mpnn, hipFuncAttributeMaxDynamicSharedMemorySize, L_TOTAL);
  k_mpnn<<<NWG, 512, L_TOTAL, stream>>>(src, dst, ea, x, uw, vw, gw, ws, (uint16_t*)d_out, N, E);
}
```
